```python
import torch

def _softmax(x, axis=-1):
    return torch.softmax(x, dim=axis)
def _gelu(x):
    return torch.nn.functional.gelu(x, approximate="tanh")
def _silu(x):
    return torch.nn.functional.silu(x)
def _sigmoid(x):
    return torch.sigmoid(x)
def _layernorm(x, axis=-1, eps=1e-5):
    m = x.mean(dim=axis, keepdim=True)
    v = x.var(dim=axis, keepdim=True, unbiased=False)
    return (x - m) / torch.sqrt(v + eps)
def _scatter_add(tgt, idx, src, axis=0):
    return tgt.clone().index_add_(axis, idx, src)
def _segment_sum(src, seg, n):
    out = torch.zeros((n,) + src.shape[1:], dtype=src.dtype)
    return out.index_add_(0, seg, src)
def _pad(x, pw):
    flat = tuple(v for pair in reversed(pw) for v in pair)
    return torch.nn.functional.pad(x, flat)

kernel_name = 'recurrent_scan__sliding_block_cached__mixer_token_mlp__prompt'

def setup_inputs():

    _w = (lambda a: (lambda t: t.to(torch.bfloat16) if t.is_floating_point() else t)(torch.as_tensor(a)))
    def _A_setup():
        B, Sq, D = 1, 2048, 512
        Hq, Hkv, Sk, Dh = 32, 8, 2048, 128
        assert Hq % Hkv == 0, (Hq, Hkv)
        return {
            "x": _w((lambda *s: torch.randn(*s, dtype=torch.bfloat16) * 0.05)(B, Sq, D)),
            "k_cache": _w((lambda *s: torch.randn(*s, dtype=torch.bfloat16) * 1.6)(B, Hkv, Sk, Dh)),
            "v_cache": _w((lambda *s: torch.randn(*s, dtype=torch.bfloat16) * 0.8)(B, Hkv, Sk, Dh)),
            "wq": _w((lambda *s: torch.randn(*s, dtype=torch.bfloat16) * 1.6)(D, Hq * Dh) * (D ** -0.5)),
            "wo": _w((lambda *s: torch.randn(*s, dtype=torch.bfloat16) * 0.8)(Hq * Dh, D) * ((Hq * Dh) ** -0.5)),
        }
    _A_in = _A_setup()

    def _B_setup():
        B, S, D, F = 1, 2048, 512, 1792
        return {
            "x": _w((lambda *s: torch.randn(*s, dtype=torch.bfloat16) * 0.05)(B, S, D)),
            "w1": _w((lambda *s: torch.randn(*s, dtype=torch.bfloat16) * 1.6)(S, F) * (S ** -0.5)),
            "b1": _w((lambda *s: torch.randn(*s, dtype=torch.bfloat16) * 1.6)(F) * 0.02),
            "w2": _w((lambda *s: torch.randn(*s, dtype=torch.bfloat16) * 0.2)(F, S) * (F ** -0.5)),
            "b2": _w((lambda *s: torch.randn(*s, dtype=torch.bfloat16) * 0.2)(S) * 0.02),
        }
    _B_in = _B_setup()

    return {
        "A_x": _A_in["x"],
        "A_k_cache": _A_in["k_cache"],
        "A_v_cache": _A_in["v_cache"],
        "A_wq": _A_in["wq"],
        "A_wo": _A_in["wo"],
        "B_x": _B_in["x"],
        "B_w1": _B_in["w1"],
        "B_b1": _B_in["b1"],
        "B_w2": _B_in["w2"],
        "B_b2": _B_in["b2"],
    }

def reference(A_x, A_k_cache, A_v_cache, A_wq, A_wo, B_x, B_w1, B_b1, B_w2, B_b2):
    def _A_ref(x, k_cache, v_cache, wq, wo):
        B, Sq, D = x.shape
        Hq, Dh = 32, 128
        Hkv, Sk = k_cache.shape[1], k_cache.shape[2]
        W = min(4096, max(256, Sk // 4))
        rep = Hq // Hkv
        q = (lambda x, *d: x.permute(*d))((x @ wq).reshape(B, Sq, Hq, Dh), 0, 2, 1, 3)
        k = (lambda x, n, axis: torch.repeat_interleave(x, n, dim=axis))(k_cache[:, :, Sk - W:, :], rep, axis=1)
        v = (lambda x, n, axis: torch.repeat_interleave(x, n, dim=axis))(v_cache[:, :, Sk - W:, :], rep, axis=1)
        scores = torch.einsum("bhqd,bhkd->bhqk", q, k) * (Dh ** -0.5)
        p = _softmax(scores, axis=-1)
        o = torch.einsum("bhqk,bhkd->bhqd", p, v)
        return (lambda x, *d: x.permute(*d))(o, 0, 2, 1, 3).reshape(B, Sq, Hq * Dh) @ wo

    def _B_ref(x, w1, b1, w2, b2):
        B, S, D = x.shape
        h = _layernorm(x, axis=-1)
        u = torch.einsum("bsd,sf->bdf", h, w1) + b1
        u = _gelu(u)
        y = torch.einsum("bdf,fs->bds", u, w2) + b2
        return (lambda x, *d: x.permute(*d))(y, 0, 2, 1)

    x = A_x
    xn = _layernorm(x, axis=-1)
    s = _A_ref(xn, A_k_cache, A_v_cache, A_wq, A_wo)
    g = _silu(_B_ref(xn, B_w1, B_b1, B_w2, B_b2))
    return x + s * g
```

```cpp
#include <hip/hip_runtime.h>
#include <hip/hip_bf16.h>
typedef __hip_bfloat16 bf16;
__device__ __forceinline__ float b2f(bf16 v){ return __bfloat162float(v);}
__device__ __forceinline__ bf16 f2b(float v){ return __float2bfloat16(v);}

__global__ void ln_kernel(const bf16* x, bf16* xn, bf16* hT) {
  int row = blockIdx.x; int lane = threadIdx.x;
  float v[8]; float s=0;
  for (int i=0;i<8;i++){ v[i]=b2f(x[row*512+lane*8+i]); s+=v[i]; }
  for (int o=32;o;o>>=1) s+=__shfl_xor(s,o);
  float m=s/512.f; float q=0;
  for (int i=0;i<8;i++){ float d=v[i]-m; q+=d*d; }
  for (int o=32;o;o>>=1) q+=__shfl_xor(q,o);
  float r=1.f/sqrtf(q/512.f+1e-5f);
  s=0;
  for (int i=0;i<8;i++){ bf16 t=f2b((v[i]-m)*r); xn[row*512+lane*8+i]=t; v[i]=b2f(t); s+=v[i]; }
  for (int o=32;o;o>>=1) s+=__shfl_xor(s,o);
  m=s/512.f; q=0;
  for (int i=0;i<8;i++){ float d=v[i]-m; q+=d*d; }
  for (int o=32;o;o>>=1) q+=__shfl_xor(q,o);
  r=1.f/sqrtf(q/512.f+1e-5f);
  for (int i=0;i<8;i++){ hT[(lane*8+i)*2048+row]=f2b((v[i]-m)*r); }
}
__global__ void gemm_naive(const bf16* A, const bf16* B, const bf16* bias, bf16* C, int M,int N,int K,int mode){
  int n = blockIdx.x*64+threadIdx.x; int m=blockIdx.y*4+threadIdx.y;
  float acc=0;
  for(int k=0;k<K;k++) acc+=b2f(A[(size_t)m*K+k])*b2f(B[(size_t)k*N+n]);
  if(mode>=1) acc+=b2f(bias[n]);
  if(mode==1){ float u=acc; acc=0.5f*u*(1.f+tanhf(0.7978845608f*(u+0.044715f*u*u*u))); }
  C[(size_t)m*N+n]=f2b(acc);
}
__global__ void attn_naive(const bf16* q, const bf16* kc, const bf16* vc, bf16* o){
  int row=blockIdx.x, h=blockIdx.y, lane=threadIdx.x; int g=h/4;
  __shared__ float qs[128]; __shared__ float p[512];
  qs[lane]=b2f(q[(size_t)row*4096+h*128+lane]); qs[lane+64]=b2f(q[(size_t)row*4096+h*128+lane+64]);
  __syncthreads();
  float sc[8]; float mx=-1e30f;
  for(int i=0;i<8;i++){ int key=1536+lane+64*i; const bf16* kr=kc+((size_t)g*2048+key)*128; float a=0; for(int d=0;d<128;d++) a+=qs[d]*b2f(kr[d]); a*=0.08838834764f; sc[i]=a; mx=fmaxf(mx,a);}
  for (int o2=32;o2;o2>>=1) mx=fmaxf(mx,__shfl_xor(mx,o2));
  float s=0; for(int i=0;i<8;i++){ sc[i]=__expf(sc[i]-mx); s+=sc[i]; }
  for (int o2=32;o2;o2>>=1) s+=__shfl_xor(s,o2);
  for(int i=0;i<8;i++) p[lane+64*i]=sc[i]/s;
  __syncthreads();
  float a0=0,a1=0;
  for(int k=0;k<512;k++){ const bf16* vr=vc+((size_t)g*2048+1536+k)*128; a0+=p[k]*b2f(vr[lane]); a1+=p[k]*b2f(vr[lane+64]); }
  o[(size_t)row*4096+h*128+lane]=f2b(a0); o[(size_t)row*4096+h*128+lane+64]=f2b(a1);
}
__global__ void final_kernel(const bf16* x, const bf16* s, const bf16* y, bf16* out){
  int i=blockIdx.x*256+threadIdx.x; int row=i/512, d=i%512;
  float yy=b2f(y[(size_t)d*2048+row]); float g=yy/(1.f+__expf(-yy));
  out[i]=f2b(b2f(x[i])+b2f(s[i])*g);
}
extern "C" void kernel_launch(void* const* d_in, const int* in_sizes, int n_in, void* d_out, int out_size, void* d_ws, size_t ws_size, hipStream_t stream) {
  const bf16* x=(const bf16*)d_in[0]; const bf16* kc=(const bf16*)d_in[1]; const bf16* vc=(const bf16*)d_in[2];
  const bf16* wq=(const bf16*)d_in[3]; const bf16* wo=(const bf16*)d_in[4];
  const bf16* w1=(const bf16*)d_in[6]; const bf16* b1=(const bf16*)d_in[7]; const bf16* w2=(const bf16*)d_in[8]; const bf16* b2=(const bf16*)d_in[9];
  char* ws=(char*)d_ws;
  bf16* xn=(bf16*)ws; bf16* hT=(bf16*)(ws+(4<<20)); bf16* q=(bf16*)(ws+(8<<20)); bf16* o=(bf16*)(ws+(32<<20));
  bf16* s=(bf16*)(ws+(56<<20)); bf16* u=(bf16*)(ws+(60<<20)); bf16* y=(bf16*)(ws+(64<<20));
  ln_kernel<<<2048,64,0,stream>>>(x,xn,hT);
  gemm_naive<<<dim3(4096/64,2048/4),dim3(64,4),0,stream>>>(xn,wq,nullptr,q,2048,4096,512,0);
  attn_naive<<<dim3(2048,32),64,0,stream>>>(q,kc,vc,o);
  gemm_naive<<<dim3(512/64,2048/4),dim3(64,4),0,stream>>>(o,wo,nullptr,s,2048,512,4096,0);
  gemm_naive<<<dim3(1792/64,512/4),dim3(64,4),0,stream>>>(hT,w1,b1,u,512,1792,2048,1);
  gemm_naive<<<dim3(2048/64,512/4),dim3(64,4),0,stream>>>(u,w2,b2,y,512,2048,1792,2);
  final_kernel<<<2048*512/256,256,0,stream>>>(x,s,y,(bf16*)d_out);
}
```

```cpp
#include <hip/hip_runtime.h>
#include <stdint.h>
#include <type_traits>
#pragma clang diagnostic ignored "-Wint-to-pointer-cast"

typedef __bf16 bf16x8 __attribute__((ext_vector_type(8)));
typedef __bf16 bf16x4 __attribute__((ext_vector_type(4)));
typedef __bf16 bf16x2 __attribute__((ext_vector_type(2)));
typedef float f32x16 __attribute__((ext_vector_type(16)));
typedef float f32x4 __attribute__((ext_vector_type(4)));
typedef float f32x2 __attribute__((ext_vector_type(2)));
typedef unsigned u32x4 __attribute__((ext_vector_type(4)));
typedef unsigned u32x2 __attribute__((ext_vector_type(2)));
typedef unsigned long long u64;
#define LDS3 __attribute__((address_space(3)))
#define GAS __attribute__((address_space(1)))
#define DI __device__ __forceinline__
#define SB() __builtin_amdgcn_sched_barrier(0)
#define PRIO(n) __builtin_amdgcn_s_setprio(n)

constexpr int SLOT = 32768;
constexpr int XO = 98304;
constexpr int LDS_BYTES = 163840;
constexpr size_t WS_EPOCH = 0;
constexpr size_t WS_HFLAG = 4096;
constexpr size_t WS_UFLAG = 8192;
constexpr size_t WS_SFLAG = 12288;
constexpr size_t WS_DAG   = 65536;
constexpr size_t WS_H     = 1u << 20;
constexpr size_t WS_U     = 4u << 20;
constexpr size_t WS_PART  = 8u << 20;

constexpr float THR = 16.0f;
constexpr float QSCALE = 0.08838834764831845f * 1.4426950408889634f;

DI void dma16(const void* sbase, unsigned voff, unsigned lds) {
  asm volatile("s_mov_b32 m0, %2\n\ts_nop 0\n\tglobal_load_lds_dwordx4 %0, %1" :: "v"(voff), "s"(sbase), "s"(lds) : "memory");
}
DI void dma16_sc1(const void* sbase, unsigned voff, unsigned lds) {
  asm volatile("s_mov_b32 m0, %2\n\ts_nop 0\n\tglobal_load_lds_dwordx4 %0, %1 sc1" :: "v"(voff), "s"(sbase), "s"(lds) : "memory");
}
DI void dma16x4_sc1(const void* sbase, unsigned voff, unsigned lds) {
  asm volatile("s_mov_b32 m0, %2\n\ts_nop 0\n\tglobal_load_lds_dwordx4 %0, %1 sc1\n\tglobal_load_lds_dwordx4 %0, %1 offset:1024 sc1\n\t"
               "global_load_lds_dwordx4 %0, %1 offset:2048 sc1\n\tglobal_load_lds_dwordx4 %0, %1 offset:3072 sc1" :: "v"(voff), "s"(sbase), "s"(lds) : "memory");
}
DI bf16x8 lds_b128(unsigned a) { return *(LDS3 bf16x8*)a; }
DI bf16x8 lds_tr2(unsigned a0, unsigned a1) {
  bf16x4 lo = __builtin_amdgcn_ds_read_tr16_b64_v4bf16((LDS3 bf16x4*)a0);
  bf16x4 hi = __builtin_amdgcn_ds_read_tr16_b64_v4bf16((LDS3 bf16x4*)a1);
  return __builtin_shufflevector(lo, hi, 0, 1, 2, 3, 4, 5, 6, 7);
}
DI f32x16 mfma(bf16x8 a, bf16x8 b, f32x16 c) { return __builtin_amdgcn_mfma_f32_32x32x16_bf16(a, b, c, 0, 0, 0); }
DI unsigned pk2(float lo, float hi) { f32x2 v = {lo, hi}; bf16x2 b = __builtin_convertvector(v, bf16x2); return __builtin_bit_cast(unsigned, b); }
template <int S> DI bf16x8 pack8(const f32x16& x, float sc) {
  u32x4 p;
  p[0] = pk2(x[8 * S + 0] * sc, x[8 * S + 1] * sc); p[1] = pk2(x[8 * S + 2] * sc, x[8 * S + 3] * sc);
  p[2] = pk2(x[8 * S + 4] * sc, x[8 * S + 5] * sc); p[3] = pk2(x[8 * S + 6] * sc, x[8 * S + 7] * sc);
  return __builtin_bit_cast(bf16x8, p);
}
template <int S> DI bf16x8 pack8n(const f32x16& x) {
  u32x4 p;
  p[0] = pk2(x[8 * S + 0], x[8 * S + 1]); p[1] = pk2(x[8 * S + 2], x[8 * S + 3]);
  p[2] = pk2(x[8 * S + 4], x[8 * S + 5]); p[3] = pk2(x[8 * S + 6], x[8 * S + 7]);
  return __builtin_bit_cast(bf16x8, p);
}
DI float bflo(unsigned u) { return __uint_as_float(u << 16); }
DI float bfhi(unsigned u) { return __uint_as_float(u & 0xffff0000u); }
template <int C> DI float dadd(float v) { return v + __builtin_bit_cast(float, __builtin_amdgcn_update_dpp(0, __builtin_bit_cast(int, v), C, 0xf, 0xf, true)); }
DI float half_sum(float v) {
  auto b = __builtin_amdgcn_permlane32_swap(__float_as_uint(v), __float_as_uint(v), false, false);
  return __uint_as_float(b[0]) + __uint_as_float(b[1]);
}
DI float half_max(float v) {
  auto b = __builtin_amdgcn_permlane32_swap(__float_as_uint(v), __float_as_uint(v), false, false);
  return fmaxf(fmaxf(-3.0e38f, __uint_as_float(b[0])), __uint_as_float(b[1]));
}
DI float wave_sum(float v) {
  v = dadd<0xB1>(v); v = dadd<0x4E>(v); v = dadd<0x141>(v); v = dadd<0x140>(v);
  auto a = __builtin_amdgcn_permlane16_swap(__float_as_uint(v), __float_as_uint(v), false, false);
  v = __uint_as_float(a[0]) + __uint_as_float(a[1]);
  return half_sum(v);
}
DI void store16_sc1(void* p, u32x4 v) {
  asm volatile("global_store_dwordx4 %0, %1, off sc1\n\ts_nop 1" :: "v"(p), "v"(v) : "memory");
}
#define VMWAIT(N) asm volatile("s_waitcnt vmcnt(" #N ")" ::: "memory")
#define LBAR() do { asm volatile("" ::: "memory"); __builtin_amdgcn_s_waitcnt(0xC07F); __builtin_amdgcn_s_barrier(); asm volatile("" ::: "memory"); } while (0)
#define STEPBAR() do { if (DW) VMWAIT(8); LBAR(); } while (0)

DI void poll_flags(const u64* f, int nflags, u64 tag, int lane) {
  for (unsigned spins = 0; spins < (1u << 20); ++spins) {
    bool ok = true;
    for (int i = lane; i < nflags; i += 64)
      ok &= (__hip_atomic_load((GAS const u64*)(f + i), __ATOMIC_RELAXED, __HIP_MEMORY_SCOPE_AGENT) == tag);
    if (__all(ok)) break;
    __builtin_amdgcn_s_sleep(2);
  }
  asm volatile("" ::: "memory");
}
DI void flag_store(char* p, u64 tag) { __hip_atomic_store((GAS u64*)p, tag, __ATOMIC_RELAXED, __HIP_MEMORY_SCOPE_AGENT); }

#define TS()
#define STAMP(i)

struct Args {
  const __bf16* x; const __bf16* kc; const __bf16* vc; const __bf16* wq; const __bf16* wo; const __bf16* w1;
  const __bf16* b1; const __bf16* w2; const __bf16* b2; __bf16* out; char* ws;
};
struct Desc { const char* p[4]; unsigned lds[4]; unsigned v[4]; };

template <int OFF> DI void dma16o(const void* sbase, unsigned voff, unsigned lds) {
  asm volatile("s_add_u32 m0, %2, %3\n\ts_nop 0\n\tglobal_load_lds_dwordx4 %0, %1 sc1" :: "v"(voff), "s"(sbase), "s"(lds), "i"(OFF) : "memory", "scc");
}
DI void warm_line(const void* sbase, unsigned voff, unsigned lds) {
  asm volatile("s_mov_b32 m0, %2\n\ts_nop 0\n\tglobal_load_lds_dword %0, %1" :: "v"(voff), "s"(sbase), "s"(lds) : "memory");
}

template <bool DW>
DI void body(const Args& A, const int w, const int l) {
  const __bf16* __restrict__ x = A.x; char* __restrict__ ws = A.ws;
  STAMP(0);
  TS();
  const int wd = w & 3;
  const int b = blockIdx.x, xcd = b & 7, idx = b >> 3;
  const int g = 2 * (xcd & 3) + (idx & 1), rb = 16 * (xcd >> 2) + (idx >> 1);
  const int r = l & 31, h = l >> 5, gq = (l >> 4) & 1, q = (l >> 2) & 3, p = l & 3;
  const unsigned l4 = l >> 4;

  const int xchunk = (w + g + 4) & 7;
  const int xrow8 = 8 * xchunk + (l >> 3);
  u32x4 xr[8];
  u32x2 ep2;
  {
    const char* xb = (const char*)x + (size_t)(64 * rb + xrow8) * 1024 + (l & 7) * 16;
    asm volatile("global_load_dwordx4 %0, %9, off\n\tglobal_load_dwordx4 %1, %9, off offset:128\n\tglobal_load_dwordx4 %2, %9, off offset:256\n\t"
                 "global_load_dwordx4 %3, %9, off offset:384\n\tglobal_load_dwordx4 %4, %9, off offset:512\n\tglobal_load_dwordx4 %5, %9, off offset:640\n\t"
                 "global_load_dwordx4 %6, %9, off offset:768\n\tglobal_load_dwordx4 %7, %9, off offset:896\n\tglobal_load_dwordx2 %8, %10, off sc1"
                 : "=&v"(xr[0]), "=&v"(xr[1]), "=&v"(xr[2]), "=&v"(xr[3]), "=&v"(xr[4]), "=&v"(xr[5]), "=&v"(xr[6]), "=&v"(xr[7]), "=&v"(ep2)
                 : "v"(xb), "v"(ws + WS_EPOCH) : "memory");
  }

  const int rq = idx >> 1, rt = (idx >> 2) & 7, ro = 2 * (idx >> 2);
  const int pidx = ((idx >> 2) << 1) | (idx & 1);
  const bool do_g2 = (rb & 1) == 0, do_g1 = !do_g2 && pidx < 14;
  const int ft1 = 7 * (xcd >> 1) + (pidx >> 1), dp1 = 2 * (xcd & 1) + (pidx & 1);
  const int pp2 = 4 * (xcd >> 1) + (pidx >> 2), dt2 = 4 * (xcd & 1) + (pidx & 3);
  const int r1 = (16 * (pidx & 1) + 4 * (pidx >> 1) + (pidx >> 2)) & 31;
  const int r2 = (7 * (pidx & 3) + 7 * (pidx >> 2) / 4 + 2 * (pidx >> 2)) % 28;
  const unsigned wsw = (unsigned)((l ^ (wd << 2)) << 4);
  const unsigned gsw = (unsigned)(((l & 7) ^ (((l >> 4) & 1) << 2)) << 4);
  const unsigned grow = 8 * wd + (l >> 3);
  Desc DA;
  auto d_lds = [&](Desc& D, unsigned slot) { D.lds[0] = slot + wd * 1024; D.lds[1] = D.lds[0] + 8192; D.lds[2] = D.lds[0] + 16384; D.lds[3] = D.lds[0] + 24576; };
  auto d_ldsg = [&](Desc& D, int j) { const unsigned b0 = j < 3 ? j * 32768u : 24576u, st = j < 3 ? 8192u : 32768u;
                                      D.lds[0] = D.lds[1] = b0 + wd * 1024; D.lds[2] = D.lds[0] + st; D.lds[3] = D.lds[0] + 2 * st; };
  auto d_wq = [&](Desc& D, int s, unsigned slot) {
    D.p[0] = (const char*)A.wq + (size_t)(32 * ((s + rq) & 15)) * 8192 + g * 1024;
    D.p[1] = D.p[0] + 8 * 8192; D.p[2] = D.p[0] + 16 * 8192; D.p[3] = D.p[0] + 24 * 8192;
    D.v[0] = D.v[2] = wd * 8192u + wsw; D.v[1] = D.v[3] = D.v[0] + 4 * 8192; d_lds(D, slot);
  };
  auto d_wo = [&](Desc& D, int s, unsigned slot) {
    D.p[0] = (const char*)A.wo + (size_t)(512 * g + 32 * ((s + ro) & 15)) * 1024;
    D.p[1] = D.p[0] + 8192; D.p[2] = D.p[0] + 16384; D.p[3] = D.p[0] + 24576;
    D.v[0] = D.v[2] = wd * 1024u + wsw; D.v[1] = D.v[3] = D.v[0] + 4096; d_lds(D, slot);
  };
  auto d_kv = [&](Desc& D, int u, unsigned slot) {
    const size_t o = ((size_t)g * 2048 + 1536) * 256;
    D.p[0] = (const char*)A.kc + o + (size_t)((u + 1 + rt) & 7) * 16384; D.p[1] = D.p[0] + 8192;
    D.p[2] = (const char*)A.vc + o + (size_t)((u + rt) & 7) * 16384 + 8192;
    D.p[3] = (const char*)A.vc + o + (size_t)((u + 1 + rt) & 7) * 16384;
    D.v[0] = (4 * wd + l4) * 256 + (((l & 15) ^ (4 * wd + l4)) << 4); D.v[1] = D.v[0] + 4096;
    D.v[2] = (4 * wd + l4) * 256 + (((l & 15) ^ (l4 << 2)) << 4); D.v[3] = D.v[2] + 4096; d_lds(D, slot);
  };
  auto d_g1 = [&](Desc& D, int t, int j) {
    const int tt = (t + r1) & 31;
    D.p[0] = D.p[1] = (const char*)A.w1 + (size_t)(64 * tt) * 3584 + ft1 * 128;
    D.p[2] = ws + WS_H + (size_t)(64 * tt) * 1024 + dp1 * 256; D.p[3] = D.p[2] + 128;
    D.v[0] = grow * 3584u + gsw; D.v[1] = D.v[0] + 32 * 3584; D.v[2] = grow * 1024u + gsw; D.v[3] = D.v[2] + 32 * 1024; d_ldsg(D, j);
  };
  auto d_g2 = [&](Desc& D, int t, int j) {
    int tt = t + r2; if (tt >= 28) tt -= 28;
    D.p[0] = D.p[1] = ws + WS_U + (size_t)dt2 * (224 * 1024) + (size_t)tt * 8192;
    D.p[2] = (const char*)A.w2 + (size_t)(64 * tt) * 4096 + pp2 * 256; D.p[3] = D.p[2] + 128;
    D.v[0] = wd * 1024u + l * 16u; D.v[1] = D.v[0] + 4096; D.v[2] = grow * 4096u + gsw; D.v[3] = D.v[2] + 32 * 4096; d_ldsg(D, j);
  };
#define DWX DW
#define DMX(D, i) do { if (DWX) dma16o<((i) & 1) * 4096>(D.p[(i) >> 1], D.v[((i) >> 2) * 2 + ((i) & 1)], D.lds[(i) >> 1]); } while (0)
#define DMA(i) DMX(DA, i)
#define DMX_ALL(D) do { DMX(D, 0); DMX(D, 1); DMX(D, 2); DMX(D, 3); DMX(D, 4); DMX(D, 5); DMX(D, 6); DMX(D, 7); } while (0)
#define DMX_G(D) do { DMX(D, 0); DMX(D, 1); DMX(D, 4); DMX(D, 5); DMX(D, 6); DMX(D, 7); } while (0)
  auto nextslot = [](unsigned s) { return s >= 2 * SLOT ? 0u : s + SLOT; };
  auto next5 = [](unsigned s) { return (s == 2 * SLOT || s == 4 * SLOT) ? 0u : s + SLOT; };

  if (DW) { d_wq(DA, 0, 0); DMX_ALL(DA); d_wq(DA, 1, SLOT); }
#define XOPS "+v"(xr[0]), "+v"(xr[1]), "+v"(xr[2]), "+v"(xr[3]), "+v"(xr[4]), "+v"(xr[5]), "+v"(xr[6]), "+v"(xr[7]), "+v"(ep2)
  if (DWX) asm volatile("s_waitcnt vmcnt(8)" : XOPS :: "memory"); else asm volatile("s_waitcnt vmcnt(0)" : XOPS :: "memory");
  const u64 tag = (((u64)__builtin_amdgcn_readfirstlane(ep2[1]) << 32) | (u64)__builtin_amdgcn_readfirstlane(ep2[0])) + 0x9E3779B97F4A7C15ull;
  const unsigned tagl = (unsigned)tag, tagh = (unsigned)(tag >> 32);

  {
    float sm = 0.f, sq = 0.f, sm2 = 0.f, sq2 = 0.f;
    #pragma unroll
    for (int i = 0; i < 8; ++i)
      #pragma unroll
      for (int j = 0; j < 4; j += 2) {
        const unsigned t0 = xr[i][j], t1 = xr[i][j + 1];
        const bf16x2 a0 = __builtin_bit_cast(bf16x2, t0), a1 = __builtin_bit_cast(bf16x2, t1), one = __builtin_bit_cast(bf16x2, 0x3f803f80u);
        sm = __builtin_amdgcn_fdot2_f32_bf16(a0, one, sm, false); sq = __builtin_amdgcn_fdot2_f32_bf16(a0, a0, sq, false);
        sm2 = __builtin_amdgcn_fdot2_f32_bf16(a1, one, sm2, false); sq2 = __builtin_amdgcn_fdot2_f32_bf16(a1, a1, sq2, false);
      }
    sm += sm2; sq += sq2;
    sm = dadd<0xB1>(sm); sq = dadd<0xB1>(sq); sm = dadd<0x4E>(sm); sq = dadd<0x4E>(sq); sm = dadd<0x141>(sm); sq = dadd<0x141>(sq);
    const float m = sm * (1.f / 512.f);
    const float var = fmaxf(sq * (1.f / 512.f) - m * m, 0.f);
    const float rs = __builtin_amdgcn_rsqf(var + 1e-5f);
    const float mb = -m * rs;
    const unsigned xw = XO + (l & 7) * 1024 + ((xrow8 ^ (l & 7)) << 4);
    const float rs2 = rs * __builtin_amdgcn_rsqf(var * rs * rs + 1e-5f), mb2 = -m * rs2;
    char* hp = ws + WS_H + (size_t)(64 * rb + xrow8) * 1024 + (l & 7) * 16;
#define LNROW(i) do { u32x4 o; _Pragma("unroll") for (int j = 0; j < 4; ++j) o[j] = pk2(bflo(xr[i][j]) * rs + mb, bfhi(xr[i][j]) * rs + mb); \
      *(LDS3 u32x4*)(xw + (i) * 8192) = o; DMA(i); } while (0)
    LNROW(0); LNROW(1); LNROW(2); LNROW(3); LNROW(4); LNROW(5); LNROW(6); LNROW(7);
    if (!DW && w == 4) {
      #pragma unroll
      for (int i = 0; i < 8; ++i) {
        u32x4 o;
        #pragma unroll
        for (int j = 0; j < 4; ++j) o[j] = pk2(bflo(xr[i][j]) * rs2 + mb2, bfhi(xr[i][j]) * rs2 + mb2);
        store16_sc1(hp + i * 128, o);
      }
    }
  }
  STAMP(1);
  unsigned cur = 0;

#define PMM(P, ks_, i_, j_) do { if (MM) acc[i_][j_] = mfma(fa[P][ks_][j_], fb[P][ks_][i_], acc[i_][j_]); SB(); } while (0)
#define PSTEP(P, N, s_, MM_, DC, DN) do { constexpr bool MM = MM_; STEPBAR(); PRIO(1); \
      PMM(P, 0, 1, 0); fb[N][0][1] = PLB(1, s_, 0); SB(); DMX(DC, 0); SB(); \
      PMM(P, 0, 0, 0); fa[N][0][0] = PLA(0, 0); SB(); DMX(DC, 1); SB(); \
      PMM(P, 0, 1, 1); fb[N][0][0] = PLB(0, s_, 0); fa[N][0][1] = PLA(1, 0); SB(); DMX(DC, 2); SB(); \
      PMM(P, 0, 0, 1); fb[N][1][1] = PLB(1, s_, 1); SB(); DMX(DC, 3); SB(); PRIO(0); \
      PMM(P, 1, 1, 0); fa[N][1][0] = PLA(0, 1); fb[N][1][0] = PLB(0, s_, 1); SB(); DMX(DC, 4); SB(); \
      PMM(P, 1, 1, 1); fa[N][1][1] = PLA(1, 1); SB(); DMX(DC, 5); SB(); \
      PMM(P, 1, 0, 0); DMX(DC, 6); DMX(DC, 7); SB(); mid(s_, DN); cur = nextslot(cur); SB(); \
      PMM(P, 1, 0, 1); } while (0)
#define PLOOP() do { PSTEP(1, 0, 0, false, DA, DA); \
    for (int s = 1; s < 15; s += 2) { PSTEP(0, 1, s, true, DA, DA); PSTEP(1, 0, s + 1, true, DA, DA); } \
    PSTEP(0, 1, 15, true, DA, DA); } while (0)

#define GMM(P, k_) do { if (MM) acc = mfma(fa[P][k_], fb[P][k_], acc); SB(); } while (0)
#define GSTEP(P, N, J, MM_, DD, DC, MID) do { constexpr bool MM = MM_; asm volatile("" : "+v"(acc)); \
    const unsigned ga_ = (J) < 3 ? gbase[(J) % 3] : 24576u, gb_ = (J) < 3 ? gbase[(J) % 3] + gsub8 : 24576u + gsub32; \
    GMM(P, 0); GLD(N, 0); SB(); if (DD) { DMX(DC, 0); DMX(DC, 1); } SB(); GLD(N, 1); SB(); if ((DD) == 2) { DMX(DC, 2); DMX(DC, 3); } SB(); \
    GMM(P, 1); GLD(N, 2); SB(); if (DD) { DMX(DC, 4); DMX(DC, 5); } SB(); GLD(N, 3); SB(); \
    GMM(P, 2); if (DD) { DMX(DC, 6); DMX(DC, 7); } SB(); MID; SB(); \
    GMM(P, 3); } while (0)
#define GBAR(N) do { if (DW) VMWAIT(N); LBAR(); } while (0)

  const int hh = w >> 1, rh = w & 1;
  bf16x8 qf[8];
  {
    f32x16 acc[2][2];
    #pragma unroll
    for (int i = 0; i < 2; ++i)
      #pragma unroll
      for (int j = 0; j < 2; ++j) acc[i][j] = f32x16{};
    unsigned aq[2], xrow[2];
    #pragma unroll
    for (int j = 0; j < 2; ++j) {
      aq[j] = (8 * h + q) * 1024 + (16 * hh + 4 * ((2 * rh + j) ^ q) + 2 * gq + (p & 1)) * 16 + (p >> 1) * 8;
      xrow[j] = (unsigned)(((32 * (rh ^ j) + r) ^ h) << 4) + h * 1024 + XO;
    }
    bf16x8 fa[2][2][2], fb[2][2][2];
    u32x4 pf0, pf1;
#define PLB(i_, s_, ks_) lds_b128((xrow[i_] ^ (unsigned)((4 * (((s_) + rq) & 1) + 2 * (ks_)) << 4)) + (4 * (((s_) + rq) & 15) + 2 * (ks_)) * 1024)
#define PLA(j_, ks_) lds_tr2(cur + aq[j_] + (16 * (ks_)) * 1024, cur + aq[j_] + (16 * (ks_) + 4) * 1024)
    auto mid = [&](int s, Desc& DN) {
      if (!DW) {
        if (w == 4) {
          if (s == 3) { VMWAIT(0); if (l == 0) flag_store(ws + WS_HFLAG + (rb * 8 + g) * 8, tag); }
          if (do_g1) {
            if (s == 9) {
              const char* fp = ws + WS_HFLAG + l * 32;
              asm volatile("global_load_dwordx4 %0, %2, off sc1\n\tglobal_load_dwordx4 %1, %2, off offset:16 sc1" : "=&v"(pf0), "=&v"(pf1) : "v"(fp) : "memory");
            }
            if (s == 13) {
              asm volatile("s_waitcnt vmcnt(0)" : "+v"(pf0), "+v"(pf1) :: "memory");
              const bool ok = pf0[0] == tagl && pf0[1] == tagh && pf0[2] == tagl && pf0[3] == tagh && pf1[0] == tagl && pf1[1] == tagh && pf1[2] == tagl && pf1[3] == tagh;
              if (!__all(ok)) poll_flags((const u64*)(ws + WS_HFLAG), 256, tag, l);
              if (b == 16 && l == 0) flag_store(ws + WS_EPOCH, tag);
            }
          }
        }
      } else {
        if (s + 3 < 16) d_wq(DN, s + 3, cur);
        else if (do_g1) d_g1(DN, s - 13, cur >> 15);
        else d_kv(DN, s - 14, cur);
      }
    };
    if (DW) d_wq(DA, 2, 2 * SLOT);
    PLOOP();
#undef PLB
#undef PLA
    { constexpr bool MM = true;
      PMM(1, 0, 1, 0); PMM(1, 0, 1, 1); PMM(1, 1, 1, 0); PMM(1, 1, 1, 1);
      LBAR();
      PMM(1, 0, 0, 0); PMM(1, 0, 0, 1);
      const unsigned xa = XO + w * 4096 + l * 16;
      *(LDS3 bf16x8*)(xa) = pack8<0>(acc[1][0], QSCALE); *(LDS3 bf16x8*)(xa + 1024) = pack8<1>(acc[1][0], QSCALE); SB();
      PMM(1, 1, 0, 0);
      *(LDS3 bf16x8*)(xa + 2048) = pack8<0>(acc[1][1], QSCALE); *(LDS3 bf16x8*)(xa + 3072) = pack8<1>(acc[1][1], QSCALE); SB();
      PMM(1, 1, 0, 1);
      STEPBAR();
      const unsigned xb2 = XO + (w ^ 1) * 4096 + l * 16;
      qf[4] = lds_b128(xb2); qf[5] = lds_b128(xb2 + 1024); qf[6] = lds_b128(xb2 + 2048); qf[7] = lds_b128(xb2 + 3072);
      qf[0] = pack8<0>(acc[0][0], QSCALE); qf[1] = pack8<1>(acc[0][0], QSCALE); qf[2] = pack8<0>(acc[0][1], QSCALE); qf[3] = pack8<1>(acc[0][1], QSCALE);
    }
  }
  STAMP(2);
  unsigned fill = 0;

  if (do_g1) {
    const int fh = w & 1, dq = w >> 1;
    f32x16 acc = f32x16{};
    const unsigned tb = (8 * h + q) * 128 + (2 * gq + (p >> 1)) * 16 + (p & 1) * 8;
    const unsigned aa = tb + 64 * (fh ^ (q >> 1));
    const unsigned ba = tb + 64 * ((dq & 1) ^ (q >> 1));
    const unsigned gbase[3] = {32768u, 65536u, 0u};
    const unsigned gsub8 = 8192u * (1 + (dq >> 1)), gsub32 = 32768u * (1 + (dq >> 1));
    bf16x8 fa[2][4], fb[2][4];
#define GLD(N, kk) do { fa[N][kk] = lds_tr2(ga_ + aa + (16 * (kk)) * 128, ga_ + aa + (16 * (kk) + 4) * 128); \
                        fb[N][kk] = lds_tr2(gb_ + ba + (16 * (kk)) * 128, gb_ + ba + (16 * (kk) + 4) * 128); } while (0)
#define G1D(D, t_, j_) do { if (DW) d_g1(D, t_, j_); } while (0)
    if (DW) { DMX_G(DA); d_g1(DA, 3, 3); }
    GSTEP(1, 0, 0, false, 1, DA, G1D(DA, 4, 1));
    GBAR(12); GSTEP(0, 1, 1, true, 1, DA, G1D(DA, 5, 2));
    GBAR(12); GSTEP(1, 0, 2, true, 1, DA, G1D(DA, 6, 0));
    GBAR(12); GSTEP(0, 1, 3, true, 1, DA, G1D(DA, 7, 3));
    for (int t = 4; t < 28; t += 4) {
      GBAR(12); GSTEP(1, 0, 0, true, 1, DA, G1D(DA, t + 4, 1));
      GBAR(12); GSTEP(0, 1, 1, true, 1, DA, G1D(DA, t + 5, 2));
      GBAR(12); GSTEP(1, 0, 2, true, 1, DA, G1D(DA, t + 6, 0));
      GBAR(12); GSTEP(0, 1, 3, true, 1, DA, G1D(DA, t + 7, 3));
    }
    GBAR(12); GSTEP(1, 0, 0, true, 1, DA, if (DW) d_kv(DA, -1, XO));
    GBAR(12); GSTEP(0, 1, 1, true, 2, DA, if (DW) d_kv(DA, 0, XO + SLOT));
    GBAR(14); GSTEP(1, 0, 2, true, 2, DA, (void)0);
    const __bf16* bp = A.b1 + 64 * ft1 + 32 * fh + 4 * h;
    u32x2 bb[4];
    GBAR(16);
    asm volatile("global_load_dwordx2 %0, %4, off\n\tglobal_load_dwordx2 %1, %4, off offset:16\n\tglobal_load_dwordx2 %2, %4, off offset:32\n\tglobal_load_dwordx2 %3, %4, off offset:48"
                 : "=&v"(bb[0]), "=&v"(bb[1]), "=&v"(bb[2]), "=&v"(bb[3]) : "v"(bp) : "memory");
    GSTEP(0, 1, 3, true, 0, DA, if (DW) d_kv(DA, 1, 0));
    { constexpr bool MM = true; GMM(1, 0); GMM(1, 1); GMM(1, 2); GMM(1, 3); }
#undef GLD
    asm volatile("s_waitcnt vmcnt(0)" : "+v"(bb[0]), "+v"(bb[1]), "+v"(bb[2]), "+v"(bb[3]) :: "memory");
    #pragma unroll
    for (int m = 0; m < 4; ++m) {
      acc[4 * m] += bflo(bb[m][0]); acc[4 * m + 1] += bfhi(bb[m][0]); acc[4 * m + 2] += bflo(bb[m][1]); acc[4 * m + 3] += bfhi(bb[m][1]);
    }
    #pragma unroll
    for (int e = 0; e < 16; ++e) {
      const float u = acc[e];
      const float z = u * (1.f + 0.044715f * u * u) * (-2.f * 0.7978845608028654f * 1.4426950408889634f);
      acc[e] = u * __builtin_amdgcn_rcpf(1.f + __builtin_amdgcn_exp2f(z));
    }
    char* up = ws + WS_U + (size_t)(2 * dp1 + (dq >> 1)) * (224 * 1024) + (size_t)(ft1 * 8 + fh * 4 + h) * 1024 + (32 * (dq & 1) + r) * 16;
    store16_sc1(up, __builtin_bit_cast(u32x4, pack8n<0>(acc)));
    store16_sc1(up + 2048, __builtin_bit_cast(u32x4, pack8n<1>(acc)));
    cur = XO;
    LBAR();
  }
  STAMP(3);

  {
    f32x16 o[4];
    f32x16 negm, sA, sB;
    float lsum = 0.f;
    unsigned ka[8];
    #pragma unroll
    for (int kk = 0; kk < 8; ++kk) ka[kk] = (r * 256 + ((h ^ (r & 15)) << 4)) ^ (unsigned)((kk ^ (4 * rh)) << 5);
    unsigned va[4];
    #pragma unroll
    for (int d = 0; d < 4; ++d) va[d] = 16384 + (4 * h + q) * 256 + (4 * (d ^ q) + 2 * gq + (p >> 1)) * 16 + (p & 1) * 8;
    bf16x8 kf[8], vf[8];
    u32x4 pw0 = {}, pw1 = {};
    float mx;
    unsigned kc_[8], vc_[4];
#define SETADDR() do { _Pragma("unroll") for (int i_ = 0; i_ < 8; ++i_) { kc_[i_] = cur + ka[i_]; } _Pragma("unroll") for (int i_ = 0; i_ < 4; ++i_) { vc_[i_] = cur + va[i_]; } } while (0)
#define LDK(i, KB) kf[i] = lds_b128(kc_[i] + (KB) * 8192)
#define LDV(i, VP) vf[i] = lds_tr2(vc_[(i) & 3] + ((VP) * 32 + 16 * ((i) >> 2)) * 256, vc_[(i) & 3] + ((VP) * 32 + 16 * ((i) >> 2) + 8) * 256)
#define EXP(Y, e) do { Y[e] = __builtin_amdgcn_exp2f(Y[e]); } while (0)
#define ADD(Y, e) do { lsum += Y[e]; } while (0)
#define CVT(P, Y, c) do { P[(c) & 3] = pk2(Y[2 * (c)], Y[2 * (c) + 1]); } while (0)
#define MX(X, e) do { mx = fmaxf(fmaxf(mx, X[2 * (e)]), X[2 * (e) + 1]); } while (0)
#define QK(X, i) do { X = mfma(kf[i], qf[i], (i) == 0 ? negm : X); } while (0)
#define PV(i) do { o[(i) & 3] = mfma(vf[i], __builtin_bit_cast(bf16x8, (i) < 4 ? pw0 : pw1), o[(i) & 3]); } while (0)
#define DMAC(C, i) do { if (C) DMA(i); } while (0)
#define STAGE_A(X, Y, KB, VP, DC) do { \
      QK(X, 0); LDK(6, KB); EXP(Y, 0); EXP(Y, 1); DMAC(DC, 3); SB(); \
      QK(X, 1); LDK(7, KB); EXP(Y, 2); CVT(pw0, Y, 0); DMAC(DC, 4); SB(); \
      QK(X, 2); LDV(0, VP); EXP(Y, 3); EXP(Y, 4); DMAC(DC, 5); SB(); \
      QK(X, 3); LDV(1, VP); EXP(Y, 5); ADD(Y, 0); CVT(pw0, Y, 1); DMAC(DC, 6); SB(); \
      QK(X, 4); LDV(2, VP); EXP(Y, 6); EXP(Y, 7); CVT(pw0, Y, 2); DMAC(DC, 7); SB(); \
      QK(X, 5); LDV(3, VP); EXP(Y, 8); ADD(Y, 1); ADD(Y, 2); CVT(pw0, Y, 3); SB(); \
      QK(X, 6); LDV(4, VP); EXP(Y, 9); EXP(Y, 10); ADD(Y, 3); SB(); \
      QK(X, 7); LDV(5, VP); EXP(Y, 11); ADD(Y, 4); CVT(pw1, Y, 4); SB(); \
    } while (0)
#define RESCALE(X) do { \
      if (__any(mx > THR)) { \
        const float delta = fmaxf(mx, 0.f); \
        const float alpha = __builtin_amdgcn_exp2f(-delta); \
        _Pragma("unroll") for (int e = 0; e < 16; ++e) { X[e] -= delta; negm[e] -= delta; } \
        _Pragma("unroll") for (int d = 0; d < 4; ++d) _Pragma("unroll") for (int e = 0; e < 16; ++e) o[d][e] *= alpha; \
        lsum *= alpha; \
      } } while (0)
#define STAGE_B(X, Y, VP, KBN) do { \
      PV(0); LDV(6, VP); EXP(Y, 12); EXP(Y, 13); CVT(pw1, Y, 5); SB(); \
      PV(1); LDV(7, VP); EXP(Y, 14); EXP(Y, 15); ADD(Y, 5); SB(); \
      PV(2); LDK(0, KBN); CVT(pw1, Y, 6); CVT(pw1, Y, 7); ADD(Y, 6); mx = -3.0e38f; MX(X, 0); SB(); \
      PV(3); LDK(1, KBN); MX(X, 1); MX(X, 2); ADD(Y, 7); ADD(Y, 8); SB(); \
      PV(4); LDK(2, KBN); MX(X, 3); MX(X, 4); ADD(Y, 9); ADD(Y, 10); SB(); \
      PV(5); LDK(3, KBN); MX(X, 5); MX(X, 6); ADD(Y, 11); ADD(Y, 12); SB(); \
      PV(6); LDK(4, KBN); MX(X, 7); ADD(Y, 13); ADD(Y, 14); SB(); \
      PV(7); LDK(5, KBN); mx = half_max(mx); ADD(Y, 15); SB(); \
      RESCALE(X); SB(); \
    } while (0)
#define STAGE_B_BAR(X, Y, VP, WAITS, SETD, POST) do { \
      PV(0); LDV(6, VP); EXP(Y, 12); EXP(Y, 13); CVT(pw1, Y, 5); SB(); \
      PV(1); LDV(7, VP); EXP(Y, 14); EXP(Y, 15); ADD(Y, 5); SB(); \
      PV(2); CVT(pw1, Y, 6); CVT(pw1, Y, 7); ADD(Y, 6); mx = -3.0e38f; MX(X, 0); SB(); \
      cur = next5(cur); \
      PV(3); SETADDR(); SETD; SB(); \
      PV(4); SB(); \
      WAITS; LBAR(); PRIO(3); \
      PV(5); SB(); LDK(0, 0); LDK(1, 0); LDK(2, 0); LDK(3, 0); MX(X, 1); MX(X, 2); MX(X, 3); MX(X, 4); ADD(Y, 7); ADD(Y, 8); ADD(Y, 9); POST; DMA(0); SB(); \
      PV(6); LDK(4, 0); MX(X, 5); MX(X, 6); MX(X, 7); ADD(Y, 10); ADD(Y, 11); ADD(Y, 12); DMA(1); SB(); \
      PV(7); LDK(5, 0); mx = half_max(mx); ADD(Y, 13); ADD(Y, 14); ADD(Y, 15); DMA(2); SB(); \
      RESCALE(X); SB(); \
    } while (0)

    SETADDR();
    #pragma unroll
    for (int i = 0; i < 8; ++i) LDK(i, 0);
    negm = f32x16{};
    #pragma unroll
    for (int d = 0; d < 4; ++d) o[d] = f32x16{};
    SB();
    QK(sA, 0); LDK(0, 1); DMA(0); SB(); QK(sA, 1); LDK(1, 1); DMA(1); SB(); QK(sA, 2); LDK(2, 1); DMA(2); SB(); QK(sA, 3); LDK(3, 1); SB();
    QK(sA, 4); LDK(4, 1); SB(); QK(sA, 5); LDK(5, 1); SB(); QK(sA, 6); SB(); QK(sA, 7); SB();
    mx = -3.0e38f;
    #pragma unroll
    for (int e = 0; e < 8; ++e) MX(sA, e);
    mx = half_max(mx);
    #pragma unroll
    for (int e = 0; e < 16; ++e) { sA[e] -= mx; negm[e] = -mx; }
    SB();
    STAGE_A(sB, sA, 1, 1, true);
    fill = SLOT;
    STAGE_B_BAR(sB, sA, 1, VMWAIT(0), if (DW) d_kv(DA, 2, fill),
                if (!DW && do_g1 && w == 4 && l == 0) flag_store(ws + WS_UFLAG + (dp1 * 28 + ft1) * 8, tag));
    for (int u = 0; u < 7; ++u) {
      STAGE_A(sA, sB, 0, 0, true);
      PRIO(2);
      STAGE_B(sA, sB, 0, 1);
      PRIO(1);
      STAGE_A(sB, sA, 1, 1, false);
      PRIO(0);
      fill = nextslot(fill);
      STAGE_B_BAR(sB, sA, 1, if (DW) VMWAIT(8), if (DW) { if (u + 3 < 8) d_kv(DA, u + 3, fill); else d_wo(DA, u - 5, fill); }, (void)0);
    }
    PRIO(0);
    #pragma unroll
    for (int i = 0; i < 8; ++i) LDV(i, 0);
    DMA(3); DMA(4);
    #pragma unroll
    for (int e = 0; e < 16; ++e) { EXP(sB, e); ADD(sB, e); }
    #pragma unroll
    for (int c = 0; c < 4; ++c) { CVT(pw0, sB, c); CVT(pw1, sB, 4 + c); }
    SB();
    fill = nextslot(fill); cur = nextslot(cur);
    const float inv = __builtin_amdgcn_rcpf(half_sum(lsum));
    const unsigned ob = XO + (hh * 16 + h) * 1024 + (32 * rh + r) * 16;
#define OWR(d) do { *(LDS3 bf16x8*)(ob + (4 * (d)) * 1024) = pack8<0>(o[d], inv); *(LDS3 bf16x8*)(ob + (4 * (d) + 2) * 1024) = pack8<1>(o[d], inv); } while (0)
    PV(0); PV(4); DMA(5); SB(); PV(1); PV(5); DMA(6); SB(); PV(2); OWR(0); SB(); PV(6); DMA(7); SB(); PV(3); OWR(1); if (DW) d_wo(DA, 2, fill); SB(); PV(7); SB(); OWR(2); OWR(3);
  }
  STAMP(4);

  {
    f32x16 acc[2][2];
    #pragma unroll
    for (int i = 0; i < 2; ++i)
      #pragma unroll
      for (int j = 0; j < 2; ++j) acc[i][j] = f32x16{};
    unsigned oa[2];
    #pragma unroll
    for (int j = 0; j < 2; ++j) oa[j] = (4 * h + q) * 1024 + (16 * (w >> 1) + 4 * ((2 * (w & 1) + j) ^ q) + 2 * gq + (p >> 1)) * 16 + (p & 1) * 8;
    const unsigned ob = XO + h * 1024 + r * 16;
    bf16x8 fa[2][2][2], fb[2][2][2];
    u32x2 pfl;
#define PLB(i_, s_, ks_) lds_b128(ob + (4 * (((s_) + ro) & 15) + 2 * (ks_)) * 1024 + 512 * (i_))
#define PLA(j_, ks_) lds_tr2(cur + oa[j_] + (16 * (ks_)) * 1024, cur + oa[j_] + (16 * (ks_) + 8) * 1024)
    auto mid = [&](int s, Desc& DN) {
      if (DW) { if (s + 3 < 16 || !do_g2) d_wo(DN, s + 3, cur); else d_g2(DN, s - 13, cur >> 15); }
      else if (w == 4 && do_g2) {
        if (s == 9) {
          const char* fp = ws + WS_UFLAG + ((dt2 >> 1) * 28 + (l < 28 ? l : 0)) * 8;
          asm volatile("global_load_dwordx2 %0, %1, off sc1" : "=&v"(pfl) : "v"(fp) : "memory");
        }
        if (s == 13) {
          asm volatile("s_waitcnt vmcnt(0)" : "+v"(pfl) :: "memory");
          if (!__all(pfl[0] == tagl && pfl[1] == tagh)) poll_flags((const u64*)(ws + WS_UFLAG) + (dt2 >> 1) * 28, 28, tag, l);
        }
      }
    };
    PLOOP();
#undef PLB
#undef PLA
    char* pp = ws + WS_PART + ((size_t)(rb * 8 + w) * 8 + g) * 8192 + l * 16;
    { constexpr bool MM = true;
      PMM(1, 0, 0, 0); PMM(1, 1, 0, 0); PMM(1, 0, 0, 1); PMM(1, 1, 0, 1); PMM(1, 0, 1, 0);
      store16_sc1(pp, __builtin_bit_cast(u32x4, pack8n<0>(acc[0][0]))); store16_sc1(pp + 4096, __builtin_bit_cast(u32x4, pack8n<1>(acc[0][0]))); SB();
      PMM(1, 1, 1, 0);
      store16_sc1(pp + 1024, __builtin_bit_cast(u32x4, pack8n<0>(acc[0][1]))); store16_sc1(pp + 5120, __builtin_bit_cast(u32x4, pack8n<1>(acc[0][1]))); SB();
      PMM(1, 0, 1, 1); PMM(1, 1, 1, 1);
      store16_sc1(pp + 2048, __builtin_bit_cast(u32x4, pack8n<0>(acc[1][0]))); store16_sc1(pp + 6144, __builtin_bit_cast(u32x4, pack8n<1>(acc[1][0])));
      store16_sc1(pp + 3072, __builtin_bit_cast(u32x4, pack8n<0>(acc[1][1]))); store16_sc1(pp + 7168, __builtin_bit_cast(u32x4, pack8n<1>(acc[1][1])));
    }
    STAMP(5);
    VMWAIT(0); LBAR();
    if (!DW && w == 4 && l == 0) flag_store(ws + WS_SFLAG + (rb * 8 + g) * 8, tag);
  }
  STAMP(6);

  if (do_g2) {
    const int wi = (w >> 1) & 1, wj = w & 1, sb2 = DW ? 1 : 0;
    f32x16 acc = f32x16{};
    const unsigned aa = h * 1024 + (32 * wj + r) * 16;
    const unsigned ba = (4 * h + q) * 128 + (4 * (wi ^ (q >> 1)) + 2 * gq + (p >> 1)) * 16 + (p & 1) * 8;
    const unsigned gbase[3] = {65536u, 0u, 32768u};
    constexpr unsigned gsub8 = 8192u * (1 + (DW ? 1 : 0)), gsub32 = 32768u * (1 + (DW ? 1 : 0));
    bf16x8 fa[2][4], fb[2][4];
    const int srow = 64 * (2 * pp2 + sb2) + 32 * wi + r;
    const size_t xo = (size_t)srow * 512 + 64 * dt2 + 32 * wj + 8 * h;
    const char* pp = ws + WS_PART + (size_t)((2 * pp2 + sb2) * 8 + dt2) * 65536 + (wi * 2 + wj) * 1024 + l * 16;
    const u64* sfl = (const u64*)(ws + WS_SFLAG) + (2 * pp2 + sb2) * 8;
    u32x4 pv[16];
    u32x2 pfl;
#define GLD(N, kk) do { fa[N][kk] = lds_b128(ga_ + aa + 2 * (kk) * 1024); \
                        fb[N][kk] = lds_tr2(gb_ + ba + (16 * (kk)) * 128, gb_ + ba + (16 * (kk) + 8) * 128); } while (0)
#define PLD(c) asm volatile("global_load_dwordx4 %0, %1, off sc1" : "=v"(pv[c]) : "v"(pp + ((c) >> 1) * 8192 + ((c) & 1) * 4096) : "memory")
#define G2D(D, t_, j_, AUX) do { if (DW) d_g2(D, t_, j_); else { AUX; } } while (0)
    if (DW) { DMX_G(DA); d_g2(DA, 3, 3); }
    GSTEP(1, 0, 0, false, 1, DA, G2D(DA, 4, 2, (void)0));
    GBAR(12); GSTEP(0, 1, 1, true, 1, DA, G2D(DA, 5, 0, (void)0));
    GBAR(12); GSTEP(1, 0, 2, true, 1, DA, G2D(DA, 6, 1, (void)0));
    GBAR(12); GSTEP(0, 1, 3, true, 1, DA, G2D(DA, 7, 3, (void)0));
    for (int t = 4; t < 12; t += 4) {
      GBAR(12); GSTEP(1, 0, 0, true, 1, DA, G2D(DA, t + 4, 2, (void)0));
      GBAR(12); GSTEP(0, 1, 1, true, 1, DA, G2D(DA, t + 5, 0, (void)0));
      GBAR(12); GSTEP(1, 0, 2, true, 1, DA, G2D(DA, t + 6, 1, (void)0));
      GBAR(12); GSTEP(0, 1, 3, true, 1, DA, G2D(DA, t + 7, 3, (void)0));
    }
    GBAR(12); GSTEP(1, 0, 0, true, 1, DA, G2D(DA, 16, 2, asm volatile("global_load_dwordx2 %0, %1, off sc1" : "=&v"(pfl) : "v"(sfl + (l & 7)) : "memory")));
    GBAR(12); GSTEP(0, 1, 1, true, 1, DA, G2D(DA, 17, 0, (void)0));
    GBAR(12); GSTEP(1, 0, 2, true, 1, DA, G2D(DA, 18, 1, (void)0));
    GBAR(12); GSTEP(0, 1, 3, true, 1, DA, G2D(DA, 19, 3, { asm volatile("s_waitcnt vmcnt(0)" : "+v"(pfl) :: "memory");
                                                          if (!__all(pfl[0] == tagl && pfl[1] == tagh)) poll_flags(sfl, 8, tag, l); PLD(0); PLD(1); }));
    GBAR(12); GSTEP(1, 0, 0, true, 1, DA, G2D(DA, 20, 2, { PLD(2); PLD(3); }));
    GBAR(12); GSTEP(0, 1, 1, true, 1, DA, G2D(DA, 21, 0, { PLD(4); PLD(5); }));
    GBAR(12); GSTEP(1, 0, 2, true, 1, DA, G2D(DA, 22, 1, { PLD(6); PLD(7); }));
    GBAR(12); GSTEP(0, 1, 3, true, 1, DA, G2D(DA, 23, 3, { PLD(8); PLD(9); }));
    GBAR(12); GSTEP(1, 0, 0, true, 1, DA, G2D(DA, 24, 2, { PLD(10); PLD(11); }));
    GBAR(12); GSTEP(0, 1, 1, true, 1, DA, G2D(DA, 25, 0, { PLD(12); PLD(13); }));
    GBAR(12); GSTEP(1, 0, 2, true, 1, DA, G2D(DA, 26, 1, { PLD(14); PLD(15); }));
    GBAR(12); GSTEP(0, 1, 3, true, 1, DA, G2D(DA, 27, 3, (void)0));
    GBAR(12); GSTEP(1, 0, 0, true, 1, DA, (void)0);
    u32x4 xv0, xv1; unsigned b2r;
    asm volatile("global_load_dwordx4 %0, %3, off\n\tglobal_load_dwordx4 %1, %3, off offset:32\n\tglobal_load_ushort %2, %4, off"
                 : "=&v"(xv0), "=&v"(xv1), "=&v"(b2r) : "v"(x + xo), "v"(A.b2 + srow) : "memory");
    GBAR(15); GSTEP(0, 1, 1, true, 0, DA, (void)0);
    GBAR(9); GSTEP(1, 0, 2, true, 0, DA, (void)0);
    GBAR(3); GSTEP(0, 1, 3, true, 0, DA, (void)0);
    { constexpr bool MM = true; GMM(1, 0); GMM(1, 1); GMM(1, 2); GMM(1, 3); }
#undef GLD
    STAMP(7);
    if (DW) {
      poll_flags(sfl, 8, tag, l);
      PLD(0); PLD(1); PLD(2); PLD(3); PLD(4); PLD(5); PLD(6); PLD(7); PLD(8); PLD(9); PLD(10); PLD(11); PLD(12); PLD(13); PLD(14); PLD(15);
    }
    asm volatile("s_waitcnt vmcnt(16)" : "+v"(xv0), "+v"(xv1), "+v"(b2r) :: "memory");
    if (!DW) asm volatile("s_waitcnt vmcnt(0)" : "+v"(xv0), "+v"(xv1), "+v"(b2r) :: "memory");
    const float bias2 = bflo(b2r);
    #pragma unroll
    for (int e = 0; e < 16; ++e) {
      const float yy = acc[e] + bias2;
      acc[e] = yy * __builtin_amdgcn_rcpf(1.f + __builtin_amdgcn_exp2f(-1.4426950408889634f * yy));
    }
    asm volatile("s_waitcnt vmcnt(0)" : "+v"(pv[0]), "+v"(pv[1]), "+v"(pv[2]), "+v"(pv[3]), "+v"(pv[4]), "+v"(pv[5]), "+v"(pv[6]), "+v"(pv[7]),
                 "+v"(pv[8]), "+v"(pv[9]), "+v"(pv[10]), "+v"(pv[11]), "+v"(pv[12]), "+v"(pv[13]), "+v"(pv[14]), "+v"(pv[15]) :: "memory");
    #pragma unroll
    for (int c = 0; c < 2; ++c) {
      float t[8];
      #pragma unroll
      for (int e = 0; e < 8; ++e) t[e] = 0.f;
      #pragma unroll
      for (int gg = 0; gg < 8; ++gg)
        #pragma unroll
        for (int e = 0; e < 4; ++e) { t[2 * e] += bflo(pv[2 * gg + c][e]); t[2 * e + 1] += bfhi(pv[2 * gg + c][e]); }
      #pragma unroll
      for (int e = 0; e < 8; ++e) t[e] *= acc[8 * c + e];
      #pragma unroll
      for (int e = 0; e < 4; ++e) {
        auto rr = __builtin_amdgcn_permlane32_swap(__float_as_uint(t[e]), __float_as_uint(t[4 + e]), false, false);
        t[e] = __uint_as_float(rr[0]); t[4 + e] = __uint_as_float(rr[1]);
      }
      const u32x4 xv = c ? xv1 : xv0;
      u32x4 ov;
      #pragma unroll
      for (int e = 0; e < 4; ++e) ov[e] = pk2(t[2 * e] + bflo(xv[e]), t[2 * e + 1] + bfhi(xv[e]));
      *(u32x4*)(A.out + xo + 16 * c) = ov;
    }
  } else { STAMP(7); }
  STAMP(8);
  TS();
}

__global__ __launch_bounds__(512, 2) void fused_kernel(Args A) {
  extern __shared__ __attribute__((aligned(16))) char lds_raw[];
  const int l = threadIdx.x & 63;
  const int w = __builtin_amdgcn_readfirstlane(threadIdx.x >> 6);
  if (w < 4) body<true>(A, w, l); else body<false>(A, w, l);
}

extern "C" void kernel_launch(void* const* d_in, const int* in_sizes, int n_in, void* d_out, int out_size, void* d_ws, size_t ws_size, hipStream_t stream) {
  (void)hipFuncSetAttribute((const void*)fused_kernel, hipFuncAttributeMaxDynamicSharedMemorySize, LDS_BYTES);
  Args A;
  A.x = (const __bf16*)d_in[0]; A.kc = (const __bf16*)d_in[1]; A.vc = (const __bf16*)d_in[2]; A.wq = (const __bf16*)d_in[3]; A.wo = (const __bf16*)d_in[4];
  A.w1 = (const __bf16*)d_in[6]; A.b1 = (const __bf16*)d_in[7]; A.w2 = (const __bf16*)d_in[8]; A.b2 = (const __bf16*)d_in[9];
  A.out = (__bf16*)d_out; A.ws = (char*)d_ws;
  fused_kernel<<<256, 512, LDS_BYTES, stream>>>(A);
}
```

```cpp
#define NOWARM
#include <hip/hip_runtime.h>
#include <stdint.h>
#include <type_traits>
#pragma clang diagnostic ignored "-Wint-to-pointer-cast"

typedef __bf16 bf16x8 __attribute__((ext_vector_type(8)));
typedef __bf16 bf16x4 __attribute__((ext_vector_type(4)));
typedef __bf16 bf16x2 __attribute__((ext_vector_type(2)));
typedef float f32x16 __attribute__((ext_vector_type(16)));
typedef float f32x4 __attribute__((ext_vector_type(4)));
typedef float f32x2 __attribute__((ext_vector_type(2)));
typedef unsigned u32x4 __attribute__((ext_vector_type(4)));
typedef unsigned u32x2 __attribute__((ext_vector_type(2)));
typedef unsigned long long u64;
#define LDS3 __attribute__((address_space(3)))
#define GAS __attribute__((address_space(1)))
#define DI __device__ __forceinline__
#define SB() __builtin_amdgcn_sched_barrier(0)
#define PRIO(n) __builtin_amdgcn_s_setprio(n)

constexpr int SLOT = 32768;
constexpr int XO = 98304;
constexpr int LDS_BYTES = 163840;
constexpr size_t WS_EPOCH = 0;
constexpr size_t WS_HFLAG = 4096;
constexpr size_t WS_UFLAG = 8192;
constexpr size_t WS_SFLAG = 12288;
constexpr size_t WS_DAG   = 65536;
constexpr size_t WS_H     = 1u << 20;
constexpr size_t WS_U     = 4u << 20;
constexpr size_t WS_PART  = 8u << 20;

constexpr int CODE_KB = 64;
constexpr float THR = 16.0f;
constexpr float QSCALE = 0.08838834764831845f * 1.4426950408889634f;

DI void dma16(const void* sbase, unsigned voff, unsigned lds) {
  asm volatile("s_mov_b32 m0, %2\n\ts_nop 0\n\tglobal_load_lds_dwordx4 %0, %1" :: "v"(voff), "s"(sbase), "s"(lds) : "memory");
}
DI void dma16_sc1(const void* sbase, unsigned voff, unsigned lds) {
  asm volatile("s_mov_b32 m0, %2\n\ts_nop 0\n\tglobal_load_lds_dwordx4 %0, %1 sc1" :: "v"(voff), "s"(sbase), "s"(lds) : "memory");
}
DI void dma16x4_sc1(const void* sbase, unsigned voff, unsigned lds) {
  asm volatile("s_mov_b32 m0, %2\n\ts_nop 0\n\tglobal_load_lds_dwordx4 %0, %1 sc1\n\tglobal_load_lds_dwordx4 %0, %1 offset:1024 sc1\n\t"
               "global_load_lds_dwordx4 %0, %1 offset:2048 sc1\n\tglobal_load_lds_dwordx4 %0, %1 offset:3072 sc1" :: "v"(voff), "s"(sbase), "s"(lds) : "memory");
}
DI bf16x8 lds_b128(unsigned a) { return *(LDS3 bf16x8*)a; }
DI bf16x8 lds_tr2(unsigned a0, unsigned a1) {
  bf16x4 lo = __builtin_amdgcn_ds_read_tr16_b64_v4bf16((LDS3 bf16x4*)a0);
  bf16x4 hi = __builtin_amdgcn_ds_read_tr16_b64_v4bf16((LDS3 bf16x4*)a1);
  return __builtin_shufflevector(lo, hi, 0, 1, 2, 3, 4, 5, 6, 7);
}
DI f32x16 mfma(bf16x8 a, bf16x8 b, f32x16 c) { return __builtin_amdgcn_mfma_f32_32x32x16_bf16(a, b, c, 0, 0, 0); }
DI unsigned pk2(float lo, float hi) { f32x2 v = {lo, hi}; bf16x2 b = __builtin_convertvector(v, bf16x2); return __builtin_bit_cast(unsigned, b); }
template <int S> DI bf16x8 pack8(const f32x16& x, float sc) {
  u32x4 p;
  p[0] = pk2(x[8 * S + 0] * sc, x[8 * S + 1] * sc); p[1] = pk2(x[8 * S + 2] * sc, x[8 * S + 3] * sc);
  p[2] = pk2(x[8 * S + 4] * sc, x[8 * S + 5] * sc); p[3] = pk2(x[8 * S + 6] * sc, x[8 * S + 7] * sc);
  return __builtin_bit_cast(bf16x8, p);
}
template <int S> DI bf16x8 pack8n(const f32x16& x) {
  u32x4 p;
  p[0] = pk2(x[8 * S + 0], x[8 * S + 1]); p[1] = pk2(x[8 * S + 2], x[8 * S + 3]);
  p[2] = pk2(x[8 * S + 4], x[8 * S + 5]); p[3] = pk2(x[8 * S + 6], x[8 * S + 7]);
  return __builtin_bit_cast(bf16x8, p);
}
DI float bflo(unsigned u) { return __uint_as_float(u << 16); }
DI float bfhi(unsigned u) { return __uint_as_float(u & 0xffff0000u); }
template <int C> DI float dadd(float v) { return v + __builtin_bit_cast(float, __builtin_amdgcn_update_dpp(0, __builtin_bit_cast(int, v), C, 0xf, 0xf, true)); }
DI float half_sum(float v) {
  auto b = __builtin_amdgcn_permlane32_swap(__float_as_uint(v), __float_as_uint(v), false, false);
  return __uint_as_float(b[0]) + __uint_as_float(b[1]);
}
DI float half_max(float v) {
  auto b = __builtin_amdgcn_permlane32_swap(__float_as_uint(v), __float_as_uint(v), false, false);
  return fmaxf(fmaxf(-3.0e38f, __uint_as_float(b[0])), __uint_as_float(b[1]));
}
DI float wave_sum(float v) {
  v = dadd<0xB1>(v); v = dadd<0x4E>(v); v = dadd<0x141>(v); v = dadd<0x140>(v);
  auto a = __builtin_amdgcn_permlane16_swap(__float_as_uint(v), __float_as_uint(v), false, false);
  v = __uint_as_float(a[0]) + __uint_as_float(a[1]);
  return half_sum(v);
}
DI void store16_sc1(void* p, u32x4 v) {
  asm volatile("global_store_dwordx4 %0, %1, off sc1\n\ts_nop 1" :: "v"(p), "v"(v) : "memory");
}
#define VMWAIT(N) asm volatile("s_waitcnt vmcnt(" #N ")" ::: "memory")
#define LBAR() do { asm volatile("" ::: "memory"); __builtin_amdgcn_s_waitcnt(0xC07F); __builtin_amdgcn_s_barrier(); asm volatile("" ::: "memory"); } while (0)
#define STEPBAR() do { if (DW) VMWAIT(8); LBAR(); } while (0)

DI void poll_flags(const u64* f, int nflags, u64 tag, int lane) {
  for (unsigned spins = 0; spins < (1u << 20); ++spins) {
    bool ok = true;
    for (int i = lane; i < nflags; i += 64)
      ok &= (__hip_atomic_load((GAS const u64*)(f + i), __ATOMIC_RELAXED, __HIP_MEMORY_SCOPE_AGENT) == tag);
    if (__all(ok)) break;
    __builtin_amdgcn_s_sleep(2);
  }
  asm volatile("" ::: "memory");
}
DI void flag_store(char* p, u64 tag) { __hip_atomic_store((GAS u64*)p, tag, __ATOMIC_RELAXED, __HIP_MEMORY_SCOPE_AGENT); }

#define TS()
#define STAMP(i)

struct Args {
  const __bf16* x; const __bf16* kc; const __bf16* vc; const __bf16* wq; const __bf16* wo; const __bf16* w1;
  const __bf16* b1; const __bf16* w2; const __bf16* b2; __bf16* out; char* ws;
};
struct Desc { const char* p[4]; unsigned lds[4]; unsigned v[4]; };

template <int OFF> DI void dma16o(const void* sbase, unsigned voff, unsigned lds) {
  asm volatile("s_add_u32 m0, %2, %3\n\ts_nop 0\n\tglobal_load_lds_dwordx4 %0, %1 sc1" :: "v"(voff), "s"(sbase), "s"(__builtin_amdgcn_readfirstlane(lds)), "i"(OFF) : "memory", "scc");
}
DI void warm_line(const void* sbase, unsigned voff) {
}

template <bool DW>
DI void body(const Args& A, const int w, const int l) {
  const __bf16* __restrict__ x = A.x; char* __restrict__ ws = A.ws;
  STAMP(0);
  TS();
  const int wd = w & 3;
  const int b = blockIdx.x, xcd = b & 7, idx = b >> 3;
  const int g = 2 * (xcd & 3) + (idx & 1), rb = 16 * (xcd >> 2) + (idx >> 1);
  const int r = l & 31, h = l >> 5, gq = (l >> 4) & 1, q = (l >> 2) & 3, p = l & 3;
  const unsigned l4 = l >> 4;

  const int xchunk = (w + g + 4) & 7;
  const int xrow8 = 8 * xchunk + (l >> 3);
  u32x4 xr[8];
  u32x2 ep2;
  {
    const char* xb = (const char*)x + (size_t)(64 * rb + xrow8) * 1024 + (l & 7) * 16;
    asm volatile("global_load_dwordx4 %0, %9, off\n\tglobal_load_dwordx4 %1, %9, off offset:128\n\tglobal_load_dwordx4 %2, %9, off offset:256\n\t"
                 "global_load_dwordx4 %3, %9, off offset:384\n\tglobal_load_dwordx4 %4, %9, off offset:512\n\tglobal_load_dwordx4 %5, %9, off offset:640\n\t"
                 "global_load_dwordx4 %6, %9, off offset:768\n\tglobal_load_dwordx4 %7, %9, off offset:896\n\tglobal_load_dwordx2 %8, %10, off sc1"
                 : "=&v"(xr[0]), "=&v"(xr[1]), "=&v"(xr[2]), "=&v"(xr[3]), "=&v"(xr[4]), "=&v"(xr[5]), "=&v"(xr[6]), "=&v"(xr[7]), "=&v"(ep2)
                 : "v"(xb), "v"(ws + WS_EPOCH) : "memory");
  }

  if (!DW && w == 7) {
    u64 pc; asm volatile("s_getpc_b64 %0" : "=s"(pc));
    const char* cb = (const char*)(pc & ~(u64)127);
    #pragma unroll
    for (int i = 0; i < CODE_KB / 8; ++i) asm volatile("global_load_dword v255, %0, %1" :: "v"(l * 128u + i * 8192u), "s"(cb) : "v255", "memory");
  }
  const int rq = idx >> 1, rt = (idx >> 2) & 7, ro = 2 * (idx >> 2);
  const int pidx = ((idx >> 2) << 1) | (idx & 1);
  const bool do_g2 = (rb & 1) == 0, do_g1 = !do_g2 && pidx < 14;
  const int ft1 = 7 * (xcd >> 1) + (pidx >> 1), dp1 = 2 * (xcd & 1) + (pidx & 1);
  const int pp2 = 4 * (xcd >> 1) + (pidx >> 2), dt2 = 4 * (xcd & 1) + (pidx & 3);
  const int r1 = (16 * (pidx & 1) + 4 * (pidx >> 1) + (pidx >> 2)) & 31;
  const int r2 = (7 * (pidx & 3) + 7 * (pidx >> 2) / 4 + 2 * (pidx >> 2)) % 28;
  const unsigned wsw = (unsigned)((l ^ (wd << 2)) << 4);
  const unsigned gsw = (unsigned)(((l & 7) ^ (((l >> 4) & 1) << 2)) << 4);
  const unsigned grow = 8 * wd + (l >> 3);
  Desc DA;
  auto d_lds = [&](Desc& D, unsigned slot) { D.lds[0] = slot + wd * 1024; D.lds[1] = D.lds[0] + 8192; D.lds[2] = D.lds[0] + 16384; D.lds[3] = D.lds[0] + 24576; };
  auto d_ldsg = [&](Desc& D, int j) { const unsigned b0 = j < 3 ? j * 32768u : 24576u, st = j < 3 ? 8192u : 32768u;
                                      D.lds[0] = D.lds[1] = b0 + wd * 1024; D.lds[2] = D.lds[0] + st; D.lds[3] = D.lds[0] + 2 * st; };
  auto d_wq = [&](Desc& D, int s, unsigned slot) {
    D.p[0] = (const char*)A.wq + (size_t)(32 * ((s + rq) & 15)) * 8192 + g * 1024;
    D.p[1] = D.p[0] + 8 * 8192; D.p[2] = D.p[0] + 16 * 8192; D.p[3] = D.p[0] + 24 * 8192;
    D.v[0] = D.v[2] = wd * 8192u + wsw; D.v[1] = D.v[3] = D.v[0] + 4 * 8192; d_lds(D, slot);
  };
  auto d_wo = [&](Desc& D, int s, unsigned slot) {
    D.p[0] = (const char*)A.wo + (size_t)(512 * g + 32 * ((s + ro) & 15)) * 1024;
    D.p[1] = D.p[0] + 8192; D.p[2] = D.p[0] + 16384; D.p[3] = D.p[0] + 24576;
    D.v[0] = D.v[2] = wd * 1024u + wsw; D.v[1] = D.v[3] = D.v[0] + 4096; d_lds(D, slot);
  };
  auto d_kv = [&](Desc& D, int u, unsigned slot) {
    const size_t o = ((size_t)g * 2048 + 1536) * 256;
    D.p[0] = (const char*)A.kc + o + (size_t)((u + 1 + rt) & 7) * 16384; D.p[1] = D.p[0] + 8192;
    D.p[2] = (const char*)A.vc + o + (size_t)((u + rt) & 7) * 16384 + 8192;
    D.p[3] = (const char*)A.vc + o + (size_t)((u + 1 + rt) & 7) * 16384;
    D.v[0] = (4 * wd + l4) * 256 + (((l & 15) ^ (4 * wd + l4)) << 4); D.v[1] = D.v[0] + 4096;
    D.v[2] = (4 * wd + l4) * 256 + (((l & 15) ^ (l4 << 2)) << 4); D.v[3] = D.v[2] + 4096; d_lds(D, slot);
  };
  auto d_g1 = [&](Desc& D, int t, int j) {
    const int tt = (t + r1) & 31;
    D.p[0] = D.p[1] = (const char*)A.w1 + (size_t)(64 * tt) * 3584 + ft1 * 128;
    D.p[2] = ws + WS_H + (size_t)(64 * tt) * 1024 + dp1 * 256; D.p[3] = D.p[2] + 128;
    D.v[0] = grow * 3584u + gsw; D.v[1] = D.v[0] + 32 * 3584; D.v[2] = grow * 1024u + gsw; D.v[3] = D.v[2] + 32 * 1024; d_ldsg(D, j);
  };
  auto d_g2 = [&](Desc& D, int t, int j) {
    int tt = t + r2; if (tt >= 28) tt -= 28;
    D.p[0] = D.p[1] = ws + WS_U + (size_t)dt2 * (224 * 1024) + (size_t)tt * 8192;
    D.p[2] = (const char*)A.w2 + (size_t)(64 * tt) * 4096 + pp2 * 256; D.p[3] = D.p[2] + 128;
    D.v[0] = wd * 1024u + l * 16u; D.v[1] = D.v[0] + 4096; D.v[2] = grow * 4096u + gsw; D.v[3] = D.v[2] + 32 * 4096; d_ldsg(D, j);
  };
#define DWX DW
#define DMX(D, i) do { if (DWX) dma16o<((i) & 1) * 4096>(D.p[(i) >> 1], D.v[((i) >> 2) * 2 + ((i) & 1)], D.lds[(i) >> 1]); } while (0)
#define DMA(i) DMX(DA, i)
#define DMX_ALL(D) do { DMX(D, 0); DMX(D, 1); DMX(D, 2); DMX(D, 3); DMX(D, 4); DMX(D, 5); DMX(D, 6); DMX(D, 7); } while (0)
#define DMX_G(D) do { DMX(D, 0); DMX(D, 1); DMX(D, 4); DMX(D, 5); DMX(D, 6); DMX(D, 7); } while (0)
  auto nextslot = [](unsigned s) { return s >= 2 * SLOT ? 0u : s + SLOT; };
  auto next5 = [](unsigned s) { return (s == 2 * SLOT || s == 4 * SLOT) ? 0u : s + SLOT; };

  if (DW) { d_wq(DA, 0, 0); DMX_ALL(DA); d_wq(DA, 1, SLOT); }
#define XOPS "+v"(xr[0]), "+v"(xr[1]), "+v"(xr[2]), "+v"(xr[3]), "+v"(xr[4]), "+v"(xr[5]), "+v"(xr[6]), "+v"(xr[7]), "+v"(ep2)
  static_assert(CODE_KB == 64, "wave 7 leaves exactly 8 prefetch loads in flight");
  if (!DW && w != 7) asm volatile("s_waitcnt vmcnt(0)" ::: "memory");
  if (DWX || !DW) asm volatile("s_waitcnt vmcnt(8) ; REL" : XOPS :: "memory"); else asm volatile("s_waitcnt vmcnt(0)" : XOPS :: "memory");
  const u64 tag = (((u64)__builtin_amdgcn_readfirstlane(ep2[1]) << 32) | (u64)__builtin_amdgcn_readfirstlane(ep2[0])) + 0x9E3779B97F4A7C15ull;
  const unsigned tagl = (unsigned)tag, tagh = (unsigned)(tag >> 32);

  {
    float sm = 0.f, sq = 0.f, sm2 = 0.f, sq2 = 0.f;
    #pragma unroll
    for (int i = 0; i < 8; ++i)
      #pragma unroll
      for (int j = 0; j < 4; j += 2) {
        const unsigned t0 = xr[i][j], t1 = xr[i][j + 1];
        const bf16x2 a0 = __builtin_bit_cast(bf16x2, t0), a1 = __builtin_bit_cast(bf16x2, t1), one = __builtin_bit_cast(bf16x2, 0x3f803f80u);
        sm = __builtin_amdgcn_fdot2_f32_bf16(a0, one, sm, false); sq = __builtin_amdgcn_fdot2_f32_bf16(a0, a0, sq, false);
        sm2 = __builtin_amdgcn_fdot2_f32_bf16(a1, one, sm2, false); sq2 = __builtin_amdgcn_fdot2_f32_bf16(a1, a1, sq2, false);
      }
    sm += sm2; sq += sq2;
    sm = dadd<0xB1>(sm); sq = dadd<0xB1>(sq); sm = dadd<0x4E>(sm); sq = dadd<0x4E>(sq); sm = dadd<0x141>(sm); sq = dadd<0x141>(sq);
    const float m = sm * (1.f / 512.f);
    const float var = fmaxf(sq * (1.f / 512.f) - m * m, 0.f);
    const float rs = __builtin_amdgcn_rsqf(var + 1e-5f);
    const float mb = -m * rs;
    const unsigned xw = XO + (l & 7) * 1024 + ((xrow8 ^ (l & 7)) << 4);
    const float rs2 = rs * __builtin_amdgcn_rsqf(var * rs * rs + 1e-5f), mb2 = -m * rs2;
    char* hp = ws + WS_H + (size_t)(64 * rb + xrow8) * 1024 + (l & 7) * 16;
#define LNROW(i) do { u32x4 o; _Pragma("unroll") for (int j = 0; j < 4; ++j) o[j] = pk2(bflo(xr[i][j]) * rs + mb, bfhi(xr[i][j]) * rs + mb); \
      *(LDS3 u32x4*)(xw + (i) * 8192) = o; DMA(i); } while (0)
    LNROW(0); LNROW(1); LNROW(2); LNROW(3); LNROW(4); LNROW(5); LNROW(6); LNROW(7);
    if (!DW && w == 4) {
      #pragma unroll
      for (int i = 0; i < 8; ++i) {
        u32x4 o;
        #pragma unroll
        for (int j = 0; j < 4; ++j) o[j] = pk2(bflo(xr[i][j]) * rs2 + mb2, bfhi(xr[i][j]) * rs2 + mb2);
        store16_sc1(hp + i * 128, o);
      }
    }
  }
  STAMP(1);
  unsigned cur = 0;
  unsigned fre = 2 * SLOT + wd * 1024;

#define PMM(P, ks_, i_, j_) do { if (MM) acc[i_][j_] = mfma(fa[P][ks_][j_], fb[P][ks_][i_], acc[i_][j_]); SB(); } while (0)
#define PDM(FAST, i) do { if (FAST) { if (DWX) dma16o<(i) * 4096>(pb_, pv8[i], fre); } else DMX(DA, i); SB(); } while (0)
#define PSTEP(P, N, s_, MM_, FAST, MID) do { constexpr bool MM = MM_; STEPBAR(); PRIO(1); \
      PMM(P, 0, 1, 0); fb[N][0][1] = PLB(1, s_, 0); const char* pb_ = PBASE((s_) + 2); SB(); PDM(FAST, 0); \
      PMM(P, 0, 0, 0); fa[N][0][0] = PLA(0, 0); SB(); PDM(FAST, 1); \
      PMM(P, 0, 1, 1); fb[N][0][0] = PLB(0, s_, 0); fa[N][0][1] = PLA(1, 0); SB(); PDM(FAST, 2); \
      PMM(P, 0, 0, 1); fb[N][1][1] = PLB(1, s_, 1); SB(); PDM(FAST, 3); PRIO(0); \
      PMM(P, 1, 1, 0); fa[N][1][0] = PLA(0, 1); fb[N][1][0] = PLB(0, s_, 1); SB(); PDM(FAST, 4); \
      PMM(P, 1, 1, 1); fa[N][1][1] = PLA(1, 1); SB(); PDM(FAST, 5); \
      PMM(P, 1, 0, 0); PDM(FAST, 6); PDM(FAST, 7); MID; fre = cur + wd * 1024; cur = nextslot(cur); SB(); \
      PMM(P, 1, 0, 1); } while (0)
#define PLOOP() do { PSTEP(1, 0, 0, false, true, (void)0); \
    for (int s = 1; s < 11; s += 2) { PSTEP(0, 1, s, true, true, auxmid(s)); PSTEP(1, 0, s + 1, true, true, (void)0); } \
    PSTEP(0, 1, 11, true, true, POLL_ISSUE); PSTEP(1, 0, 12, true, true, (void)0); \
    PSTEP(0, 1, 13, true, true, { POLL_CHECK; nextd(0); }); PSTEP(1, 0, 14, true, false, nextd(1)); PSTEP(0, 1, 15, true, false, nextd(2)); } while (0)

#define GMM(P, k_, j_) do { if (MM) acc[j_] = mfma(fa[P][k_][j_], fb[P][k_], acc[j_]); SB(); } while (0)
#define GSTEP(P, N, J, MM_, DD, MID) do { constexpr bool MM = MM_; \
    if (DW) { if ((DD) == 1) DMX_G(DA); if ((DD) == 2) DMX_ALL(DA); MID; } else { \
    asm volatile("" : "+v"(acc[0]), "+v"(acc[1])); \
    const unsigned ga_ = (J) < 3 ? gbase[(J) % 3] : 24576u, gb_ = (J) < 3 ? gbase[(J) % 3] + gsub8 : 24576u + gsub32; \
    GMM(P, 0, 0); GLB(N, 0); GLA(N, 0, 0); SB(); GMM(P, 0, 1); GLA(N, 0, 1); GLB(N, 1); SB(); \
    GMM(P, 1, 0); GLA(N, 1, 0); GLA(N, 1, 1); SB(); GMM(P, 1, 1); GLB(N, 2); GLA(N, 2, 0); SB(); \
    GMM(P, 2, 0); GLA(N, 2, 1); GLB(N, 3); SB(); GMM(P, 2, 1); GLA(N, 3, 0); GLA(N, 3, 1); SB(); \
    GMM(P, 3, 0); MID; SB(); GMM(P, 3, 1); } } while (0)
#define GLAST() do { if (!DW) { constexpr bool MM = true; GMM(1, 0, 0); GMM(1, 0, 1); GMM(1, 1, 0); GMM(1, 1, 1); GMM(1, 2, 0); GMM(1, 2, 1); GMM(1, 3, 0); GMM(1, 3, 1); } } while (0)
#define GBAR(N) do { if (DW) VMWAIT(N); LBAR(); } while (0)

  const int hh = w >> 1, rh = w & 1;
  bf16x8 qf[8];
  {
    f32x16 acc[2][2];
    #pragma unroll
    for (int i = 0; i < 2; ++i)
      #pragma unroll
      for (int j = 0; j < 2; ++j) acc[i][j] = f32x16{};
    unsigned aq[2], xrow[2];
    #pragma unroll
    for (int j = 0; j < 2; ++j) {
      aq[j] = (8 * h + q) * 1024 + (16 * hh + 4 * ((2 * rh + j) ^ q) + 2 * gq + (p & 1)) * 16 + (p >> 1) * 8;
      xrow[j] = (unsigned)(((32 * (rh ^ j) + r) ^ h) << 4) + h * 1024 + XO;
    }
    bf16x8 fa[2][2][2], fb[2][2][2];
    u32x4 pf0, pf1;
#define PLB(i_, s_, ks_) lds_b128((xrow[i_] ^ (unsigned)((4 * (((s_) + rq) & 1) + 2 * (ks_)) << 4)) + (4 * (((s_) + rq) & 15) + 2 * (ks_)) * 1024)
#define PLA(j_, ks_) lds_tr2(cur + aq[j_] + (16 * (ks_)) * 1024, cur + aq[j_] + (16 * (ks_) + 4) * 1024)
#define PBASE(s_) ((const char*)A.wq + (size_t)(((s_) + rq) & 15) * (32 * 8192) + g * 1024)
    unsigned pv8[8];
    #pragma unroll
    for (int i = 0; i < 8; ++i) pv8[i] = (4 * i + wd) * 8192u + wsw;
    auto auxmid = [&](int s) {
      if (!DW) {
        if (w == 4) { if (s == 3) { VMWAIT(0); if (l == 0) flag_store(ws + WS_HFLAG + (rb * 8 + g) * 8, tag); } }
        else if (w == 7) { if (s >= 7) warm_line((const char*)(s == 7 ? A.kc : A.vc) + ((size_t)g * 2048 + 1536) * 256 + (idx >> 1) * 8192, l * 128u); }
        else { const int c = (s >> 1) * 2 + (w - 5);
               if (c < 7) warm_line((const char*)A.w1 + (size_t)(64 * idx) * 3584 + (xcd >> 1) * 896 + c * 128, l * 3584u); }
      }
    };
#define POLL_ISSUE do { if (!DW) asm volatile("global_load_dwordx4 %0, %2, off sc1\n\tglobal_load_dwordx4 %1, %2, off offset:16 sc1" \
                          : "=&v"(pf0), "=&v"(pf1) : "v"(ws + WS_HFLAG + l * 32) : "memory"); } while (0)
#define POLL_CHECK do { if (!DW) { \
      asm volatile("s_waitcnt vmcnt(0)" : "+v"(pf0), "+v"(pf1) :: "memory"); \
      const bool ok = pf0[0] == tagl && pf0[1] == tagh && pf0[2] == tagl && pf0[3] == tagh && pf1[0] == tagl && pf1[1] == tagh && pf1[2] == tagl && pf1[3] == tagh; \
      if (do_g1 && w == 4) { if (!__all(ok)) poll_flags((const u64*)(ws + WS_HFLAG), 256, tag, l); \
        if (b == 16 && l == 0) flag_store(ws + WS_EPOCH, tag); } } } while (0)
    auto nextd = [&](int n) { if (DW) { if (do_g1) d_g1(DA, n, (1 + n) % 3); else d_kv(DA, n - 1, ((1 + n) % 3) * SLOT); } };
    PLOOP();
#undef PLB
#undef PLA
#undef PBASE
#undef POLL_ISSUE
#undef POLL_CHECK
    { constexpr bool MM = true;
      PMM(1, 0, 1, 0); PMM(1, 0, 1, 1); PMM(1, 1, 1, 0); PMM(1, 1, 1, 1);
      LBAR();
      PMM(1, 0, 0, 0); PMM(1, 0, 0, 1);
      const unsigned xa = XO + w * 4096 + l * 16;
      *(LDS3 bf16x8*)(xa) = pack8<0>(acc[1][0], QSCALE); *(LDS3 bf16x8*)(xa + 1024) = pack8<1>(acc[1][0], QSCALE); SB();
      PMM(1, 1, 0, 0);
      *(LDS3 bf16x8*)(xa + 2048) = pack8<0>(acc[1][1], QSCALE); *(LDS3 bf16x8*)(xa + 3072) = pack8<1>(acc[1][1], QSCALE); SB();
      PMM(1, 1, 0, 1);
      STEPBAR();
      const unsigned xb2 = XO + (w ^ 1) * 4096 + l * 16;
      qf[4] = lds_b128(xb2); qf[5] = lds_b128(xb2 + 1024); qf[6] = lds_b128(xb2 + 2048); qf[7] = lds_b128(xb2 + 3072);
      qf[0] = pack8<0>(acc[0][0], QSCALE); qf[1] = pack8<1>(acc[0][0], QSCALE); qf[2] = pack8<0>(acc[0][1], QSCALE); qf[3] = pack8<1>(acc[0][1], QSCALE);
    }
  }
  STAMP(2);
  unsigned fill = 0;

  if (do_g1) {
    f32x16 acc[2] = {f32x16{}, f32x16{}};
    const unsigned tb = (8 * h + q) * 128 + (2 * gq + (p >> 1)) * 16 + (p & 1) * 8;
    const unsigned aa[2] = {tb + 64 * (q >> 1), tb + 64 * (1 ^ (q >> 1))};
    const unsigned ba = tb + 64 * ((wd & 1) ^ (q >> 1));
    const unsigned gbase[3] = {32768u, 65536u, 0u};
    const unsigned gsub8 = 8192u * (1 + (wd >> 1)), gsub32 = 32768u * (1 + (wd >> 1));
    bf16x8 fa[2][4][2], fb[2][4];
#define GLA(N, kk, j_) fa[N][kk][j_] = lds_tr2(ga_ + aa[j_] + (16 * (kk)) * 128, ga_ + aa[j_] + (16 * (kk) + 4) * 128)
#define GLB(N, kk) fb[N][kk] = lds_tr2(gb_ + ba + (16 * (kk)) * 128, gb_ + ba + (16 * (kk) + 4) * 128)
#define G1D(t_, j_) do { if (DW) d_g1(DA, t_, j_); } while (0)
    if (DW) { DMX_G(DA); d_g1(DA, 3, 3); }
    GSTEP(1, 0, 0, false, 1, G1D(4, 1));
    GBAR(12); GSTEP(0, 1, 1, true, 1, G1D(5, 2));
    GBAR(12); GSTEP(1, 0, 2, true, 1, G1D(6, 0));
    GBAR(12); GSTEP(0, 1, 3, true, 1, G1D(7, 3));
    for (int t = 4; t < 28; t += 4) {
      GBAR(12); GSTEP(1, 0, 0, true, 1, G1D(t + 4, 1));
      GBAR(12); GSTEP(0, 1, 1, true, 1, G1D(t + 5, 2));
      GBAR(12); GSTEP(1, 0, 2, true, 1, G1D(t + 6, 0));
      GBAR(12); GSTEP(0, 1, 3, true, 1, G1D(t + 7, 3));
    }
    GBAR(12); GSTEP(1, 0, 0, true, 1, if (DW) d_kv(DA, -1, XO));
    GBAR(12); GSTEP(0, 1, 1, true, 2, if (DW) d_kv(DA, 0, XO + SLOT));
    GBAR(14); GSTEP(1, 0, 2, true, 2, if (DW) d_kv(DA, 1, 0));
    constexpr int f2 = DW ? 1 : 0;
    u32x2 bb[4];
    asm volatile("global_load_dwordx2 %0, %4, off\n\tglobal_load_dwordx2 %1, %4, off offset:16\n\tglobal_load_dwordx2 %2, %4, off offset:32\n\tglobal_load_dwordx2 %3, %4, off offset:48"
                 : "=&v"(bb[0]), "=&v"(bb[1]), "=&v"(bb[2]), "=&v"(bb[3]) : "v"(A.b1 + 64 * ft1 + 32 * f2 + 4 * h) : "memory");
    GBAR(20); GSTEP(0, 1, 3, true, 0, (void)0);
    if (!DW) {
      constexpr bool MM = true;
      GMM(1, 0, 1); GMM(1, 1, 1); GMM(1, 2, 1); GMM(1, 3, 1); GMM(1, 0, 0); GMM(1, 1, 0);
      #pragma unroll
      for (int m = 0; m < 4; ++m) {
        const f32x4 v4 = {acc[1][4 * m], acc[1][4 * m + 1], acc[1][4 * m + 2], acc[1][4 * m + 3]};
        *(LDS3 f32x4*)(SLOT + wd * 4096 + m * 1024 + l * 16) = v4;
      }
      SB(); GMM(1, 2, 0); GMM(1, 3, 0);
    }
    asm volatile("s_waitcnt vmcnt(0)" : "+v"(bb[0]), "+v"(bb[1]), "+v"(bb[2]), "+v"(bb[3]) :: "memory");
    LBAR();
#undef GLA
#undef GLB
    if (DW) {
      #pragma unroll
      for (int m = 0; m < 4; ++m) {
        const f32x4 v4 = *(LDS3 f32x4*)(SLOT + wd * 4096 + m * 1024 + l * 16);
        acc[0][4 * m] = v4[0]; acc[0][4 * m + 1] = v4[1]; acc[0][4 * m + 2] = v4[2]; acc[0][4 * m + 3] = v4[3];
      }
    }
    #pragma unroll
    for (int m = 0; m < 4; ++m) {
      acc[0][4 * m] += bflo(bb[m][0]); acc[0][4 * m + 1] += bfhi(bb[m][0]); acc[0][4 * m + 2] += bflo(bb[m][1]); acc[0][4 * m + 3] += bfhi(bb[m][1]);
    }
    #pragma unroll
    for (int e = 0; e < 16; ++e) {
      const float u = acc[0][e];
      const float z = u * (1.f + 0.044715f * u * u) * (-2.f * 0.7978845608028654f * 1.4426950408889634f);
      acc[0][e] = u * __builtin_amdgcn_rcpf(1.f + __builtin_amdgcn_exp2f(z));
    }
    char* up = ws + WS_U + (size_t)(2 * dp1 + (wd >> 1)) * (224 * 1024) + (size_t)(ft1 * 8 + f2 * 4 + h) * 1024 + (32 * (wd & 1) + r) * 16;
    store16_sc1(up, __builtin_bit_cast(u32x4, pack8n<0>(acc[0])));
    store16_sc1(up + 2048, __builtin_bit_cast(u32x4, pack8n<1>(acc[0])));
    cur = XO;
  }
  STAMP(3);

  {
    f32x16 o[4];
    f32x16 negm, sA, sB;
    float lsum = 0.f;
    unsigned ka[8];
    #pragma unroll
    for (int kk = 0; kk < 8; ++kk) ka[kk] = (r * 256 + ((h ^ (r & 15)) << 4)) ^ (unsigned)((kk ^ (4 * rh)) << 5);
    unsigned va[4];
    #pragma unroll
    for (int d = 0; d < 4; ++d) va[d] = 16384 + (4 * h + q) * 256 + (4 * (d ^ q) + 2 * gq + (p >> 1)) * 16 + (p & 1) * 8;
    bf16x8 kf[8], vf[8];
    u32x4 pw0 = {}, pw1 = {};
    float mx;
    unsigned kc_[8], vc_[4];
#define SETADDR() do { _Pragma("unroll") for (int i_ = 0; i_ < 8; ++i_) { kc_[i_] = cur + ka[i_]; } _Pragma("unroll") for (int i_ = 0; i_ < 4; ++i_) { vc_[i_] = cur + va[i_]; } } while (0)
#define LDK(i, KB) kf[i] = lds_b128(kc_[i] + (KB) * 8192)
#define LDV(i, VP) vf[i] = lds_tr2(vc_[(i) & 3] + ((VP) * 32 + 16 * ((i) >> 2)) * 256, vc_[(i) & 3] + ((VP) * 32 + 16 * ((i) >> 2) + 8) * 256)
#define EXP(Y, e) do { Y[e] = __builtin_amdgcn_exp2f(Y[e]); } while (0)
#define ADD(Y, e) do { lsum += Y[e]; } while (0)
#define CVT(P, Y, c) do { P[(c) & 3] = pk2(Y[2 * (c)], Y[2 * (c) + 1]); } while (0)
#define MX(X, e) do { mx = fmaxf(fmaxf(mx, X[2 * (e)]), X[2 * (e) + 1]); } while (0)
#define QK(X, i) do { X = mfma(kf[i], qf[i], (i) == 0 ? negm : X); } while (0)
#define PV(i) do { o[(i) & 3] = mfma(vf[i], __builtin_bit_cast(bf16x8, (i) < 4 ? pw0 : pw1), o[(i) & 3]); } while (0)
#define DMAC(C, i) do { if (C) DMA(i); } while (0)
#define STAGE_A(X, Y, KB, VP, DC) do { \
      QK(X, 0); LDK(6, KB); EXP(Y, 0); EXP(Y, 1); DMAC(DC, 3); SB(); \
      QK(X, 1); LDK(7, KB); EXP(Y, 2); CVT(pw0, Y, 0); DMAC(DC, 4); SB(); \
      QK(X, 2); LDV(0, VP); EXP(Y, 3); EXP(Y, 4); DMAC(DC, 5); SB(); \
      QK(X, 3); LDV(1, VP); EXP(Y, 5); ADD(Y, 0); CVT(pw0, Y, 1); DMAC(DC, 6); SB(); \
      QK(X, 4); LDV(2, VP); EXP(Y, 6); EXP(Y, 7); CVT(pw0, Y, 2); DMAC(DC, 7); SB(); \
      QK(X, 5); LDV(3, VP); EXP(Y, 8); ADD(Y, 1); ADD(Y, 2); CVT(pw0, Y, 3); SB(); \
      QK(X, 6); LDV(4, VP); EXP(Y, 9); EXP(Y, 10); ADD(Y, 3); SB(); \
      QK(X, 7); LDV(5, VP); EXP(Y, 11); ADD(Y, 4); CVT(pw1, Y, 4); SB(); \
    } while (0)
#define RESCALE(X) do { \
      if (__any(mx > THR)) { \
        const float delta = fmaxf(mx, 0.f); \
        const float alpha = __builtin_amdgcn_exp2f(-delta); \
        _Pragma("unroll") for (int e = 0; e < 16; ++e) { X[e] -= delta; negm[e] -= delta; } \
        _Pragma("unroll") for (int d = 0; d < 4; ++d) _Pragma("unroll") for (int e = 0; e < 16; ++e) o[d][e] *= alpha; \
        lsum *= alpha; \
      } } while (0)
#define STAGE_B(X, Y, VP, KBN) do { \
      PV(0); LDV(6, VP); EXP(Y, 12); EXP(Y, 13); CVT(pw1, Y, 5); SB(); \
      PV(1); LDV(7, VP); EXP(Y, 14); EXP(Y, 15); ADD(Y, 5); SB(); \
      PV(2); LDK(0, KBN); CVT(pw1, Y, 6); CVT(pw1, Y, 7); ADD(Y, 6); mx = -3.0e38f; MX(X, 0); SB(); \
      PV(3); LDK(1, KBN); MX(X, 1); MX(X, 2); ADD(Y, 7); ADD(Y, 8); SB(); \
      PV(4); LDK(2, KBN); MX(X, 3); MX(X, 4); ADD(Y, 9); ADD(Y, 10); SB(); \
      PV(5); LDK(3, KBN); MX(X, 5); MX(X, 6); ADD(Y, 11); ADD(Y, 12); SB(); \
      PV(6); LDK(4, KBN); MX(X, 7); ADD(Y, 13); ADD(Y, 14); SB(); \
      PV(7); LDK(5, KBN); mx = half_max(mx); ADD(Y, 15); SB(); \
      RESCALE(X); SB(); \
    } while (0)
#define STAGE_B_BAR(X, Y, VP, WAITS, SETD, POST) do { \
      PV(0); LDV(6, VP); EXP(Y, 12); EXP(Y, 13); CVT(pw1, Y, 5); SB(); \
      PV(1); LDV(7, VP); EXP(Y, 14); EXP(Y, 15); ADD(Y, 5); SB(); \
      PV(2); CVT(pw1, Y, 6); CVT(pw1, Y, 7); ADD(Y, 6); mx = -3.0e38f; MX(X, 0); SB(); \
      cur = next5(cur); \
      PV(3); SETADDR(); SETD; SB(); \
      PV(4); SB(); \
      WAITS; LBAR(); PRIO(3); \
      PV(5); SB(); LDK(0, 0); LDK(1, 0); LDK(2, 0); LDK(3, 0); MX(X, 1); MX(X, 2); MX(X, 3); MX(X, 4); ADD(Y, 7); ADD(Y, 8); ADD(Y, 9); POST; DMA(0); SB(); \
      PV(6); LDK(4, 0); MX(X, 5); MX(X, 6); MX(X, 7); ADD(Y, 10); ADD(Y, 11); ADD(Y, 12); DMA(1); SB(); \
      PV(7); LDK(5, 0); mx = half_max(mx); ADD(Y, 13); ADD(Y, 14); ADD(Y, 15); DMA(2); SB(); \
      RESCALE(X); SB(); \
    } while (0)

    SETADDR();
    #pragma unroll
    for (int i = 0; i < 8; ++i) LDK(i, 0);
    negm = f32x16{};
    #pragma unroll
    for (int d = 0; d < 4; ++d) o[d] = f32x16{};
    SB();
    QK(sA, 0); LDK(0, 1); DMA(0); SB(); QK(sA, 1); LDK(1, 1); DMA(1); SB(); QK(sA, 2); LDK(2, 1); DMA(2); SB(); QK(sA, 3); LDK(3, 1); SB();
    QK(sA, 4); LDK(4, 1); SB(); QK(sA, 5); LDK(5, 1); SB(); QK(sA, 6); SB(); QK(sA, 7); SB();
    mx = -3.0e38f;
    #pragma unroll
    for (int e = 0; e < 8; ++e) MX(sA, e);
    mx = half_max(mx);
    #pragma unroll
    for (int e = 0; e < 16; ++e) { sA[e] -= mx; negm[e] = -mx; }
    SB();
    STAGE_A(sB, sA, 1, 1, true);
    fill = SLOT;
    STAGE_B_BAR(sB, sA, 1, VMWAIT(0), if (DW) d_kv(DA, 2, fill),
                if (!DW && do_g1 && w == 4 && l == 0) flag_store(ws + WS_UFLAG + (dp1 * 28 + ft1) * 8, tag));
    auto auxatt = [&](int u) {
      if (!DW && do_g2) {
        const int c = u * 4 + wd;
        if (c < 8) warm_line((const char*)A.wo + (size_t)g * 524288 + (size_t)(idx >> 2) * 65536 + c * 8192, l * 128u);
        else if (c < 22) warm_line((const char*)A.w2 + (size_t)(112 * pidx + 8 * (c - 8)) * 4096 + (xcd >> 1) * 1024, (l >> 3) * 4096u + (l & 7) * 128u);
      }
    };
    for (int u = 0; u < 7; ++u) {
      STAGE_A(sA, sB, 0, 0, true);
      PRIO(2);
      STAGE_B(sA, sB, 0, 1);
      PRIO(1);
      STAGE_A(sB, sA, 1, 1, false);
      PRIO(0);
      fill = nextslot(fill);
      STAGE_B_BAR(sB, sA, 1, if (DW) VMWAIT(8), if (DW) { if (u + 3 < 8) d_kv(DA, u + 3, fill); else d_wo(DA, u - 5, fill); }, auxatt(u));
    }
    PRIO(0);
    #pragma unroll
    for (int i = 0; i < 8; ++i) LDV(i, 0);
    DMA(3); DMA(4);
    #pragma unroll
    for (int e = 0; e < 16; ++e) { EXP(sB, e); ADD(sB, e); }
    #pragma unroll
    for (int c = 0; c < 4; ++c) { CVT(pw0, sB, c); CVT(pw1, sB, 4 + c); }
    SB();
    fre = cur + wd * 1024; cur = nextslot(cur);
    const float inv = __builtin_amdgcn_rcpf(half_sum(lsum));
    const unsigned ob = XO + (hh * 16 + h) * 1024 + (32 * rh + r) * 16;
#define OWR(d) do { *(LDS3 bf16x8*)(ob + (4 * (d)) * 1024) = pack8<0>(o[d], inv); *(LDS3 bf16x8*)(ob + (4 * (d) + 2) * 1024) = pack8<1>(o[d], inv); } while (0)
    PV(0); PV(4); DMA(5); SB(); PV(1); PV(5); DMA(6); SB(); PV(2); OWR(0); SB(); PV(6); DMA(7); SB(); PV(3); OWR(1); SB(); PV(7); SB(); OWR(2); OWR(3);
  }
  STAMP(4);

  {
    f32x16 acc[2][2];
    #pragma unroll
    for (int i = 0; i < 2; ++i)
      #pragma unroll
      for (int j = 0; j < 2; ++j) acc[i][j] = f32x16{};
    unsigned oa[2];
    #pragma unroll
    for (int j = 0; j < 2; ++j) oa[j] = (4 * h + q) * 1024 + (16 * (w >> 1) + 4 * ((2 * (w & 1) + j) ^ q) + 2 * gq + (p >> 1)) * 16 + (p & 1) * 8;
    const unsigned ob = XO + h * 1024 + r * 16;
    bf16x8 fa[2][2][2], fb[2][2][2];
    u32x2 pfl;
#define PLB(i_, s_, ks_) lds_b128(ob + (4 * (((s_) + ro) & 15) + 2 * (ks_)) * 1024 + 512 * (i_))
#define PLA(j_, ks_) lds_tr2(cur + oa[j_] + (16 * (ks_)) * 1024, cur + oa[j_] + (16 * (ks_) + 8) * 1024)
#define PBASE(s_) ((const char*)A.wo + (size_t)(16 * g + (((s_) + ro) & 15)) * 32768)
    unsigned pv8[8];
    #pragma unroll
    for (int i = 0; i < 8; ++i) pv8[i] = (4 * i + wd) * 1024u + wsw;
    auto auxmid = [&](int) {};
#define POLL_ISSUE do { if (!DW) asm volatile("global_load_dwordx2 %0, %1, off sc1" : "=&v"(pfl) : "v"(ws + WS_UFLAG + ((dt2 >> 1) * 28 + (l < 28 ? l : 0)) * 8) : "memory"); } while (0)
#define POLL_CHECK do { if (!DW) { asm volatile("s_waitcnt vmcnt(0)" : "+v"(pfl) :: "memory"); \
      if (do_g2 && w == 4 && !__all(pfl[0] == tagl && pfl[1] == tagh)) poll_flags((const u64*)(ws + WS_UFLAG) + (dt2 >> 1) * 28, 28, tag, l); } } while (0)
    auto nextd = [&](int n) { if (DW) { if (do_g2) d_g2(DA, n, (2 + n) % 3); else d_wo(DA, n, ((2 + n) % 3) * SLOT); } };
    PLOOP();
#undef PLB
#undef PLA
#undef PBASE
#undef POLL_ISSUE
#undef POLL_CHECK
    char* pp = ws + WS_PART + ((size_t)(rb * 8 + w) * 8 + g) * 8192 + l * 16;
    { constexpr bool MM = true;
      PMM(1, 0, 0, 0); PMM(1, 1, 0, 0); PMM(1, 0, 0, 1); PMM(1, 1, 0, 1); PMM(1, 0, 1, 0);
      store16_sc1(pp, __builtin_bit_cast(u32x4, pack8n<0>(acc[0][0]))); store16_sc1(pp + 4096, __builtin_bit_cast(u32x4, pack8n<1>(acc[0][0]))); SB();
      PMM(1, 1, 1, 0);
      store16_sc1(pp + 1024, __builtin_bit_cast(u32x4, pack8n<0>(acc[0][1]))); store16_sc1(pp + 5120, __builtin_bit_cast(u32x4, pack8n<1>(acc[0][1]))); SB();
      PMM(1, 0, 1, 1); PMM(1, 1, 1, 1);
      store16_sc1(pp + 2048, __builtin_bit_cast(u32x4, pack8n<0>(acc[1][0]))); store16_sc1(pp + 6144, __builtin_bit_cast(u32x4, pack8n<1>(acc[1][0])));
      store16_sc1(pp + 3072, __builtin_bit_cast(u32x4, pack8n<0>(acc[1][1]))); store16_sc1(pp + 7168, __builtin_bit_cast(u32x4, pack8n<1>(acc[1][1])));
    }
    STAMP(5);
    VMWAIT(0); LBAR();
    if (!DW && w == 4 && l == 0) flag_store(ws + WS_SFLAG + (rb * 8 + g) * 8, tag);
  }
  STAMP(6);

  if (do_g2) {
    const int wi = (w >> 1) & 1, wj = w & 1, sb2 = DW ? 1 : 0;
    f32x16 acc[2] = {f32x16{}, f32x16{}};
    const unsigned aa = h * 1024 + r * 16;
    const unsigned ba = (4 * h + q) * 128 + (4 * ((wd & 1) ^ (q >> 1)) + 2 * gq + (p >> 1)) * 16 + (p & 1) * 8;
    const unsigned gbase[3] = {65536u, 0u, 32768u};
    const unsigned gsub8 = 8192u * (1 + (wd >> 1)), gsub32 = 32768u * (1 + (wd >> 1));
    bf16x8 fa[2][4][2], fb[2][4];
    const int srow = 64 * (2 * pp2 + sb2) + 32 * wi + r;
    const size_t xo = (size_t)srow * 512 + 64 * dt2 + 32 * wj + 8 * h;
    const char* pp = ws + WS_PART + (size_t)((2 * pp2 + sb2) * 8 + dt2) * 65536 + (wi * 2 + wj) * 1024 + l * 16;
    const u64* sfl = (const u64*)(ws + WS_SFLAG) + (2 * pp2 + sb2) * 8;
    u32x4 pv[16];
    u32x2 pfl;
#define GLA(N, kk, j_) fa[N][kk][j_] = lds_b128(ga_ + aa + 2 * (kk) * 1024 + 512 * (j_))
#define GLB(N, kk) fb[N][kk] = lds_tr2(gb_ + ba + (16 * (kk)) * 128, gb_ + ba + (16 * (kk) + 8) * 128)
#define PLD(c) asm volatile("global_load_dwordx4 %0, %1, off sc1" : "=v"(pv[c]) : "v"(pp + ((c) >> 1) * 8192 + ((c) & 1) * 4096) : "memory")
#define G2D(t_, j_, AUX) do { if (DW) d_g2(DA, t_, j_); else { AUX; } } while (0)
    if (DW) { DMX_G(DA); d_g2(DA, 3, 3); }
    GSTEP(1, 0, 0, false, 1, G2D(4, 2, (void)0));
    GBAR(12); GSTEP(0, 1, 1, true, 1, G2D(5, 0, (void)0));
    GBAR(12); GSTEP(1, 0, 2, true, 1, G2D(6, 1, (void)0));
    GBAR(12); GSTEP(0, 1, 3, true, 1, G2D(7, 3, (void)0));
    for (int t = 4; t < 12; t += 4) {
      GBAR(12); GSTEP(1, 0, 0, true, 1, G2D(t + 4, 2, (void)0));
      GBAR(12); GSTEP(0, 1, 1, true, 1, G2D(t + 5, 0, (void)0));
      GBAR(12); GSTEP(1, 0, 2, true, 1, G2D(t + 6, 1, (void)0));
      GBAR(12); GSTEP(0, 1, 3, true, 1, G2D(t + 7, 3, (void)0));
    }
    GBAR(12); GSTEP(1, 0, 0, true, 1, G2D(16, 2, asm volatile("global_load_dwordx2 %0, %1, off sc1" : "=&v"(pfl) : "v"(sfl + (l & 7)) : "memory")));
    GBAR(12); GSTEP(0, 1, 1, true, 1, G2D(17, 0, (void)0));
    GBAR(12); GSTEP(1, 0, 2, true, 1, G2D(18, 1, (void)0));
    GBAR(12); GSTEP(0, 1, 3, true, 1, G2D(19, 3, { asm volatile("s_waitcnt vmcnt(0)" : "+v"(pfl) :: "memory");
                                                  if (!__all(pfl[0] == tagl && pfl[1] == tagh)) poll_flags(sfl, 8, tag, l); PLD(0); PLD(1); }));
    GBAR(12); GSTEP(1, 0, 0, true, 1, G2D(20, 2, { PLD(2); PLD(3); }));
    GBAR(12); GSTEP(0, 1, 1, true, 1, G2D(21, 0, { PLD(4); PLD(5); }));
    GBAR(12); GSTEP(1, 0, 2, true, 1, G2D(22, 1, { PLD(6); PLD(7); }));
    GBAR(12); GSTEP(0, 1, 3, true, 1, G2D(23, 3, { PLD(8); PLD(9); }));
    GBAR(12); GSTEP(1, 0, 0, true, 1, G2D(24, 2, { PLD(10); PLD(11); }));
    GBAR(12); GSTEP(0, 1, 1, true, 1, G2D(25, 0, { PLD(12); PLD(13); }));
    GBAR(12); GSTEP(1, 0, 2, true, 1, G2D(26, 1, { PLD(14); PLD(15); }));
    GBAR(12); GSTEP(0, 1, 3, true, 1, G2D(27, 3, (void)0));
    GBAR(12); GSTEP(1, 0, 0, true, 1, (void)0);
    u32x4 xv0, xv1; unsigned b2r;
    asm volatile("global_load_dwordx4 %0, %3, off\n\tglobal_load_dwordx4 %1, %3, off offset:32\n\tglobal_load_ushort %2, %4, off"
                 : "=&v"(xv0), "=&v"(xv1), "=&v"(b2r) : "v"(x + xo), "v"(A.b2 + srow) : "memory");
    GBAR(15); GSTEP(0, 1, 1, true, 0, (void)0);
    GBAR(9); GSTEP(1, 0, 2, true, 0, (void)0);
    GBAR(3); GSTEP(0, 1, 3, true, 0, (void)0);
    GLAST();
#undef GLA
#undef GLB
    if (!DW) {
      #pragma unroll
      for (int dh = 0; dh < 2; ++dh)
        #pragma unroll
        for (int m = 0; m < 4; ++m) {
          const f32x4 v4 = {acc[dh][4 * m], acc[dh][4 * m + 1], acc[dh][4 * m + 2], acc[dh][4 * m + 3]};
          *(LDS3 f32x4*)(XO + (wd * 2 + dh) * 4096 + m * 1024 + l * 16) = v4;
        }
    }
    LBAR();
    f32x16 yv;
    #pragma unroll
    for (int m = 0; m < 4; ++m) {
      const f32x4 v4 = *(LDS3 f32x4*)(XO + ((sb2 * 2 + wi) * 2 + wj) * 4096 + m * 1024 + l * 16);
      yv[4 * m] = v4[0]; yv[4 * m + 1] = v4[1]; yv[4 * m + 2] = v4[2]; yv[4 * m + 3] = v4[3];
    }
    STAMP(7);
    if (DW) {
      poll_flags(sfl, 8, tag, l);
      PLD(0); PLD(1); PLD(2); PLD(3); PLD(4); PLD(5); PLD(6); PLD(7); PLD(8); PLD(9); PLD(10); PLD(11); PLD(12); PLD(13); PLD(14); PLD(15);
    }
    asm volatile("s_waitcnt vmcnt(16)" : "+v"(xv0), "+v"(xv1), "+v"(b2r) :: "memory");
    if (!DW) asm volatile("s_waitcnt vmcnt(0)" : "+v"(xv0), "+v"(xv1), "+v"(b2r) :: "memory");
    const float bias2 = bflo(b2r);
    #pragma unroll
    for (int e = 0; e < 16; ++e) {
      const float yy = yv[e] + bias2;
      yv[e] = yy * __builtin_amdgcn_rcpf(1.f + __builtin_amdgcn_exp2f(-1.4426950408889634f * yy));
    }
    asm volatile("s_waitcnt vmcnt(0)" : "+v"(pv[0]), "+v"(pv[1]), "+v"(pv[2]), "+v"(pv[3]), "+v"(pv[4]), "+v"(pv[5]), "+v"(pv[6]), "+v"(pv[7]),
                 "+v"(pv[8]), "+v"(pv[9]), "+v"(pv[10]), "+v"(pv[11]), "+v"(pv[12]), "+v"(pv[13]), "+v"(pv[14]), "+v"(pv[15]) :: "memory");
    #pragma unroll
    for (int c = 0; c < 2; ++c) {
      float t[8];
      #pragma unroll
      for (int e = 0; e < 8; ++e) t[e] = 0.f;
      #pragma unroll
      for (int gg = 0; gg < 8; ++gg)
        #pragma unroll
        for (int e = 0; e < 4; ++e) { t[2 * e] += bflo(pv[2 * gg + c][e]); t[2 * e + 1] += bfhi(pv[2 * gg + c][e]); }
      #pragma unroll
      for (int e = 0; e < 8; ++e) t[e] *= yv[8 * c + e];
      #pragma unroll
      for (int e = 0; e < 4; ++e) {
        auto rr = __builtin_amdgcn_permlane32_swap(__float_as_uint(t[e]), __float_as_uint(t[4 + e]), false, false);
        t[e] = __uint_as_float(rr[0]); t[4 + e] = __uint_as_float(rr[1]);
      }
      const u32x4 xv = c ? xv1 : xv0;
      u32x4 ov;
      #pragma unroll
      for (int e = 0; e < 4; ++e) ov[e] = pk2(t[2 * e] + bflo(xv[e]), t[2 * e + 1] + bfhi(xv[e]));
      *(u32x4*)(A.out + xo + 16 * c) = ov;
    }
  } else { STAMP(7); }
  STAMP(8);
  TS();
}

__global__ __launch_bounds__(512, 2) __attribute__((amdgpu_num_vgpr(248))) void fused_kernel(Args A) {
  extern __shared__ __attribute__((aligned(16))) char lds_raw[];
  const int l = threadIdx.x & 63;
  const int w = __builtin_amdgcn_readfirstlane(threadIdx.x >> 6);
  if (w < 4) body<true>(A, w, l); else body<false>(A, w, l);
}

extern "C" void kernel_launch(void* const* d_in, const int* in_sizes, int n_in, void* d_out, int out_size, void* d_ws, size_t ws_size, hipStream_t stream) {
  (void)hipFuncSetAttribute((const void*)fused_kernel, hipFuncAttributeMaxDynamicSharedMemorySize, LDS_BYTES);
  Args A;
  A.x = (const __bf16*)d_in[0]; A.kc = (const __bf16*)d_in[1]; A.vc = (const __bf16*)d_in[2]; A.wq = (const __bf16*)d_in[3]; A.wo = (const __bf16*)d_in[4];
  A.w1 = (const __bf16*)d_in[6]; A.b1 = (const __bf16*)d_in[7]; A.w2 = (const __bf16*)d_in[8]; A.b2 = (const __bf16*)d_in[9];
  A.out = (__bf16*)d_out; A.ws = (char*)d_ws;
  fused_kernel<<<256, 512, LDS_BYTES, stream>>>(A);
}
```

```cpp
#include <hip/hip_runtime.h>
#include <stdint.h>
#include <type_traits>
#pragma clang diagnostic ignored "-Wint-to-pointer-cast"

typedef __bf16 bf16x8 __attribute__((ext_vector_type(8)));
typedef __bf16 bf16x4 __attribute__((ext_vector_type(4)));
typedef __bf16 bf16x2 __attribute__((ext_vector_type(2)));
typedef float f32x16 __attribute__((ext_vector_type(16)));
typedef float f32x4 __attribute__((ext_vector_type(4)));
typedef float f32x2 __attribute__((ext_vector_type(2)));
typedef unsigned u32x4 __attribute__((ext_vector_type(4)));
typedef unsigned u32x2 __attribute__((ext_vector_type(2)));
typedef unsigned long long u64;
#define LDS3 __attribute__((address_space(3)))
#define GAS __attribute__((address_space(1)))
#define DI __device__ __forceinline__
#define SB() __builtin_amdgcn_sched_barrier(0)
#define PLAINPOL ""
#define PRIO(n) __builtin_amdgcn_s_setprio(n)

constexpr int SLOT = 32768;
constexpr int XO = 98304;
constexpr int LDS_BYTES = 163840;
constexpr size_t WS_EPOCH = 0;
constexpr size_t WS_HFLAG = 4096;
constexpr size_t WS_UFLAG = 8192;
constexpr size_t WS_SFLAG = 16384;
constexpr size_t WS_DAG   = 65536;
constexpr size_t WS_H     = 1u << 20;
constexpr size_t WS_U     = 4u << 20;
constexpr size_t WS_PART  = 8u << 20;

constexpr int CODE_KB = 64;
constexpr float THR = 16.0f;
constexpr float QSCALE = 0.08838834764831845f * 1.4426950408889634f;

DI void dma16(const void* sbase, unsigned voff, unsigned lds) {
  asm volatile("s_mov_b32 m0, %2\n\ts_nop 0\n\tglobal_load_lds_dwordx4 %0, %1" :: "v"(voff), "s"(sbase), "s"(lds) : "memory");
}
DI void dma16_sc1(const void* sbase, unsigned voff, unsigned lds) {
  asm volatile("s_mov_b32 m0, %2\n\ts_nop 0\n\tglobal_load_lds_dwordx4 %0, %1 sc1" :: "v"(voff), "s"(sbase), "s"(lds) : "memory");
}
DI void dma16x4_sc1(const void* sbase, unsigned voff, unsigned lds) {
  asm volatile("s_mov_b32 m0, %2\n\ts_nop 0\n\tglobal_load_lds_dwordx4 %0, %1 sc1\n\tglobal_load_lds_dwordx4 %0, %1 offset:1024 sc1\n\t"
               "global_load_lds_dwordx4 %0, %1 offset:2048 sc1\n\tglobal_load_lds_dwordx4 %0, %1 offset:3072 sc1" :: "v"(voff), "s"(sbase), "s"(lds) : "memory");
}
DI bf16x8 lds_b128(unsigned a) { return *(LDS3 bf16x8*)a; }
DI bf16x8 lds_tr2(unsigned a0, unsigned a1) {
  bf16x4 lo = __builtin_amdgcn_ds_read_tr16_b64_v4bf16((LDS3 bf16x4*)a0);
  bf16x4 hi = __builtin_amdgcn_ds_read_tr16_b64_v4bf16((LDS3 bf16x4*)a1);
  return __builtin_shufflevector(lo, hi, 0, 1, 2, 3, 4, 5, 6, 7);
}
DI f32x16 mfma(bf16x8 a, bf16x8 b, f32x16 c) { return __builtin_amdgcn_mfma_f32_32x32x16_bf16(a, b, c, 0, 0, 0); }
DI unsigned pk2(float lo, float hi) { f32x2 v = {lo, hi}; bf16x2 b = __builtin_convertvector(v, bf16x2); return __builtin_bit_cast(unsigned, b); }
template <int S> DI bf16x8 pack8(const f32x16& x, float sc) {
  u32x4 p;
  p[0] = pk2(x[8 * S + 0] * sc, x[8 * S + 1] * sc); p[1] = pk2(x[8 * S + 2] * sc, x[8 * S + 3] * sc);
  p[2] = pk2(x[8 * S + 4] * sc, x[8 * S + 5] * sc); p[3] = pk2(x[8 * S + 6] * sc, x[8 * S + 7] * sc);
  return __builtin_bit_cast(bf16x8, p);
}
template <int S> DI bf16x8 pack8n(const f32x16& x) {
  u32x4 p;
  p[0] = pk2(x[8 * S + 0], x[8 * S + 1]); p[1] = pk2(x[8 * S + 2], x[8 * S + 3]);
  p[2] = pk2(x[8 * S + 4], x[8 * S + 5]); p[3] = pk2(x[8 * S + 6], x[8 * S + 7]);
  return __builtin_bit_cast(bf16x8, p);
}
DI float bflo(unsigned u) { return __uint_as_float(u << 16); }
DI float bfhi(unsigned u) { return __uint_as_float(u & 0xffff0000u); }
template <int C> DI float dadd(float v) { return v + __builtin_bit_cast(float, __builtin_amdgcn_update_dpp(0, __builtin_bit_cast(int, v), C, 0xf, 0xf, true)); }
DI float dot_one(unsigned u, float c) { return __builtin_amdgcn_fdot2_f32_bf16(__builtin_bit_cast(bf16x2, u), __builtin_bit_cast(bf16x2, 0x3f803f80u), c, false); }
DI float dot_sq(unsigned u, float c) { return __builtin_amdgcn_fdot2_f32_bf16(__builtin_bit_cast(bf16x2, u), __builtin_bit_cast(bf16x2, u), c, false); }
DI void frag_sum(bf16x8 f, float& c) { const u32x4 u = __builtin_bit_cast(u32x4, f); const unsigned a = u[0], b = u[1], d = u[2], e = u[3]; c = dot_one(e, dot_one(d, dot_one(b, dot_one(a, c)))); }
DI void frag_sq(bf16x8 f, float& c) { const u32x4 u = __builtin_bit_cast(u32x4, f); const unsigned a = u[0], b = u[1], d = u[2], e = u[3]; c = dot_sq(e, dot_sq(d, dot_sq(b, dot_sq(a, c)))); }
DI float half_sum(float v) {
  auto b = __builtin_amdgcn_permlane32_swap(__float_as_uint(v), __float_as_uint(v), false, false);
  return __uint_as_float(b[0]) + __uint_as_float(b[1]);
}
DI float half_max(float v) {
  auto b = __builtin_amdgcn_permlane32_swap(__float_as_uint(v), __float_as_uint(v), false, false);
  return fmaxf(fmaxf(-3.0e38f, __uint_as_float(b[0])), __uint_as_float(b[1]));
}
DI float wave_sum(float v) {
  v = dadd<0xB1>(v); v = dadd<0x4E>(v); v = dadd<0x141>(v); v = dadd<0x140>(v);
  auto a = __builtin_amdgcn_permlane16_swap(__float_as_uint(v), __float_as_uint(v), false, false);
  v = __uint_as_float(a[0]) + __uint_as_float(a[1]);
  return half_sum(v);
}
DI void store16_sc1(void* p, u32x4 v) {
  asm volatile("global_store_dwordx4 %0, %1, off sc1\n\ts_nop 1" :: "v"(p), "v"(v) : "memory");
}
#define VMWAIT(N) asm volatile("s_waitcnt vmcnt(" #N ")" ::: "memory")
#define LBAR() do { asm volatile("" ::: "memory"); __builtin_amdgcn_s_waitcnt(0xC07F); __builtin_amdgcn_s_barrier(); asm volatile("" ::: "memory"); } while (0)
#define STEPBAR() do { if (DW) VMWAIT(8); LBAR(); } while (0)

DI void poll_flags(const u64* f, int nflags, u64 tag, int lane) {
  for (unsigned spins = 0; spins < (1u << 20); ++spins) {
    bool ok = true;
    for (int i = lane; i < nflags; i += 64)
      ok &= (__hip_atomic_load((GAS const u64*)(f + i), __ATOMIC_RELAXED, __HIP_MEMORY_SCOPE_AGENT) == tag);
    if (__all(ok)) break;
    __builtin_amdgcn_s_sleep(2);
  }
  asm volatile("" ::: "memory");
}
DI void flag_store(char* p, u64 tag) { __hip_atomic_store((GAS u64*)p, tag, __ATOMIC_RELAXED, __HIP_MEMORY_SCOPE_AGENT); }

#define TS()
#define STAMP(i)

struct Args {
  const __bf16* x; const __bf16* kc; const __bf16* vc; const __bf16* wq; const __bf16* wo; const __bf16* w1;
  const __bf16* b1; const __bf16* w2; const __bf16* b2; __bf16* out; char* ws;
};
struct Desc { const char* p[4]; unsigned lds[4]; unsigned v[4]; };

template <int OFF> DI void dma16p(const void* sbase, unsigned voff, unsigned lds) {
  asm volatile("s_add_u32 m0, %2, %3\n\ts_nop 0\n\tglobal_load_lds_dwordx4 %0, %1" PLAINPOL :: "v"(voff), "s"(sbase), "s"(__builtin_amdgcn_readfirstlane(lds)), "i"(OFF) : "memory", "scc");
}
template <int OFF> DI void dma16o(const void* sbase, unsigned voff, unsigned lds) {
  asm volatile("s_add_u32 m0, %2, %3\n\ts_nop 0\n\tglobal_load_lds_dwordx4 %0, %1 sc1" :: "v"(voff), "s"(sbase), "s"(__builtin_amdgcn_readfirstlane(lds)), "i"(OFF) : "memory", "scc");
}
DI void warm_line(const void* sbase, unsigned voff) {
  asm volatile("global_load_dword v255, %0, %1" :: "v"(voff), "s"(sbase) : "v255", "memory");
}

template <bool DW>
DI void body(const Args& A, const int w, const int l) {
  const __bf16* __restrict__ x = A.x; char* __restrict__ ws = A.ws;
  STAMP(0);
  TS();
  const int wd = w & 3;
  const int b = blockIdx.x, xcd = b & 7, idx = b >> 3;
  const int g = 2 * (xcd & 3) + (idx & 1), rb = 16 * (xcd >> 2) + (idx >> 1);
  const int r = l & 31, h = l >> 5, gq = (l >> 4) & 1, q = (l >> 2) & 3, p = l & 3;
  const unsigned l4 = l >> 4;

  const int xrow8 = 8 * w + (l >> 3);
  const int hrow = 64 * rb + 8 * g + 2 * wd + h;
  u32x4 xr[8], hr[2];
  u32x2 ep2;
  {
    const char* xb = (const char*)x + (size_t)(64 * rb + xrow8) * 1024 + (l & 7) * 16;
    asm volatile("global_load_dwordx4 %0, %9, off\n\tglobal_load_dwordx4 %1, %9, off offset:128\n\tglobal_load_dwordx4 %2, %9, off offset:256\n\t"
                 "global_load_dwordx4 %3, %9, off offset:384\n\tglobal_load_dwordx4 %4, %9, off offset:512\n\tglobal_load_dwordx4 %5, %9, off offset:640\n\t"
                 "global_load_dwordx4 %6, %9, off offset:768\n\tglobal_load_dwordx4 %7, %9, off offset:896\n\tglobal_load_dwordx2 %8, %10, off sc1"
                 : "=&v"(xr[0]), "=&v"(xr[1]), "=&v"(xr[2]), "=&v"(xr[3]), "=&v"(xr[4]), "=&v"(xr[5]), "=&v"(xr[6]), "=&v"(xr[7]), "=&v"(ep2)
                 : "v"(xb), "v"(ws + WS_EPOCH) : "memory");
    if (!DW) asm volatile("global_load_dwordx4 %0, %2, off\n\tglobal_load_dwordx4 %1, %2, off offset:512"
                 : "=&v"(hr[0]), "=&v"(hr[1]) : "v"((const char*)x + (size_t)hrow * 1024 + r * 16) : "memory");
  }
  const int rq = idx >> 1, rt = (idx >> 2) & 7, ro = 2 * (idx >> 2);
  const int pidx = ((idx >> 2) << 1) | (idx & 1);
  const bool do_g2 = (rb & 1) == 0, do_g1 = !do_g2 && pidx < 14;
  const int ft1 = 7 * (xcd >> 1) + (pidx >> 1), dp1 = 2 * (xcd & 1) + (pidx & 1);
  const int pp2 = 4 * (xcd >> 1) + (pidx >> 2), dt2 = 4 * (xcd & 1) + (pidx & 3);
  const int r1 = (16 * (pidx & 1) + 4 * (pidx >> 1) + (pidx >> 2)) & 31;
  const int r2 = (7 * (pidx & 3) + 7 * (pidx >> 2) / 4 + 2 * (pidx >> 2)) % 28;
  const unsigned wsw = (unsigned)((l ^ (wd << 2)) << 4);
  const unsigned gsw = (unsigned)(((l & 7) ^ (((l >> 4) & 1) << 2)) << 4);
  const unsigned grow = 8 * wd + (l >> 3);
  Desc DA;
  auto d_lds = [&](Desc& D, unsigned slot) { D.lds[0] = slot + wd * 1024; D.lds[1] = D.lds[0] + 8192; D.lds[2] = D.lds[0] + 16384; D.lds[3] = D.lds[0] + 24576; };
  auto d_ldsg = [&](Desc& D, int j) { const unsigned b0 = j < 3 ? j * 32768u : 24576u, st = j < 3 ? 8192u : 32768u;
                                      D.lds[0] = D.lds[1] = b0 + wd * 1024; D.lds[2] = D.lds[0] + st; D.lds[3] = D.lds[0] + 2 * st; };
  auto d_wq = [&](Desc& D, int s, unsigned slot) {
    D.p[0] = (const char*)A.wq + (size_t)(32 * ((s + rq) & 15)) * 8192 + g * 1024;
    D.p[1] = D.p[0] + 8 * 8192; D.p[2] = D.p[0] + 16 * 8192; D.p[3] = D.p[0] + 24 * 8192;
    D.v[0] = D.v[2] = wd * 8192u + wsw; D.v[1] = D.v[3] = D.v[0] + 4 * 8192; d_lds(D, slot);
  };
  auto d_wo = [&](Desc& D, int s, unsigned slot) {
    D.p[0] = (const char*)A.wo + (size_t)(512 * g + 32 * ((s + ro) & 15)) * 1024;
    D.p[1] = D.p[0] + 8192; D.p[2] = D.p[0] + 16384; D.p[3] = D.p[0] + 24576;
    D.v[0] = D.v[2] = wd * 1024u + wsw; D.v[1] = D.v[3] = D.v[0] + 4096; d_lds(D, slot);
  };
  auto d_kv = [&](Desc& D, int u, unsigned slot) {
    const size_t o = ((size_t)g * 2048 + 1536) * 256;
    D.p[0] = (const char*)A.kc + o + (size_t)((u + 1 + rt) & 7) * 16384; D.p[1] = D.p[0] + 8192;
    D.p[2] = (const char*)A.vc + o + (size_t)((u + rt) & 7) * 16384 + 8192;
    D.p[3] = (const char*)A.vc + o + (size_t)((u + 1 + rt) & 7) * 16384;
    D.v[0] = (4 * wd + l4) * 256 + (((l & 15) ^ (4 * wd + l4)) << 4); D.v[1] = D.v[0] + 4096;
    D.v[2] = (4 * wd + l4) * 256 + (((l & 15) ^ (l4 << 2)) << 4); D.v[3] = D.v[2] + 4096; d_lds(D, slot);
  };
  auto d_g1 = [&](Desc& D, int t, int j) {
    const int tt = (t + r1) & 31;
    D.p[0] = D.p[1] = (const char*)A.w1 + (size_t)(64 * tt) * 3584 + ft1 * 128;
    D.p[2] = ws + WS_H + (size_t)dp1 * 524288 + (size_t)(64 * tt) * 256; D.p[3] = D.p[2] + 128;
    D.v[0] = grow * 3584u + gsw; D.v[1] = D.v[0] + 32 * 3584; D.v[2] = grow * 256u + gsw; D.v[3] = D.v[2] + 32 * 256; d_ldsg(D, j);
  };
  auto d_g2 = [&](Desc& D, int t, int j) {
    int tt = t + r2; if (tt >= 28) tt -= 28;
    D.p[0] = D.p[1] = ws + WS_U + (size_t)dt2 * (224 * 1024) + (size_t)tt * 8192;
    D.p[2] = (const char*)A.w2 + (size_t)(64 * tt) * 4096 + pp2 * 256; D.p[3] = D.p[2] + 128;
    D.v[0] = wd * 1024u + l * 16u; D.v[1] = D.v[0] + 4096; D.v[2] = grow * 4096u + gsw; D.v[3] = D.v[2] + 32 * 4096; d_ldsg(D, j);
  };
#define DWX DW
#define DMX(D, i) do { if (DWX) dma16o<((i) & 1) * 4096>(D.p[(i) >> 1], D.v[((i) >> 2) * 2 + ((i) & 1)], D.lds[(i) >> 1]); } while (0)
#define DMA(i) do { if (DWX) dma16p<((i) & 1) * 4096>(DA.p[(i) >> 1], DA.v[((i) >> 2) * 2 + ((i) & 1)], DA.lds[(i) >> 1]); } while (0)
#define DMX_ALL(D) do { DMX(D, 0); DMX(D, 1); DMX(D, 2); DMX(D, 3); DMX(D, 4); DMX(D, 5); DMX(D, 6); DMX(D, 7); } while (0)
#define DMX_G(D) do { DMX(D, 0); DMX(D, 1); DMX(D, 4); DMX(D, 5); DMX(D, 6); DMX(D, 7); } while (0)
  auto nextslot = [](unsigned s) { return s >= 2 * SLOT ? 0u : s + SLOT; };
  auto next5 = [](unsigned s) { return (s == 2 * SLOT || s == 4 * SLOT) ? 0u : s + SLOT; };

  if (DW) { d_wq(DA, 0, 0); DMX_ALL(DA); d_wq(DA, 1, SLOT); }
#define XOPS "+v"(xr[0]), "+v"(xr[1]), "+v"(xr[2]), "+v"(xr[3]), "+v"(xr[4]), "+v"(xr[5]), "+v"(xr[6]), "+v"(xr[7]), "+v"(ep2)
  if (DWX) asm volatile("s_waitcnt vmcnt(8) ; REL" : XOPS :: "memory");
  else if (DW) asm volatile("s_waitcnt vmcnt(0)" : XOPS :: "memory");
  else asm volatile("s_waitcnt vmcnt(0)" : XOPS, "+v"(hr[0]), "+v"(hr[1]) :: "memory");
  const u64 tag = (((u64)__builtin_amdgcn_readfirstlane(ep2[1]) << 32) | (u64)__builtin_amdgcn_readfirstlane(ep2[0])) + 0x9E3779B97F4A7C15ull;
  const unsigned tagl = (unsigned)tag, tagh = (unsigned)(tag >> 32);

  {
    float sm = 0.f, sq = 0.f, sm2 = 0.f, sq2 = 0.f;
    #pragma unroll
    for (int i = 0; i < 8; ++i)
      #pragma unroll
      for (int j = 0; j < 4; j += 2) { sm = dot_one(xr[i][j], sm); sq = dot_sq(xr[i][j], sq); sm2 = dot_one(xr[i][j + 1], sm2); sq2 = dot_sq(xr[i][j + 1], sq2); }
    sm += sm2; sq += sq2;
    sm = dadd<0xB1>(sm); sq = dadd<0xB1>(sq); sm = dadd<0x4E>(sm); sq = dadd<0x4E>(sq); sm = dadd<0x141>(sm); sq = dadd<0x141>(sq);
    const float m = sm * (1.f / 512.f);
    const float var = fmaxf(sq * (1.f / 512.f) - m * m, 0.f);
    const float rs = __builtin_amdgcn_rsqf(var + 1e-5f);
    const float mb = -m * rs;
    const unsigned xw = XO + (l & 7) * 1024 + ((xrow8 ^ (l & 7)) << 4);
#define LNROW(i) do { u32x4 o; _Pragma("unroll") for (int j = 0; j < 4; ++j) o[j] = pk2(bflo(xr[i][j]) * rs + mb, bfhi(xr[i][j]) * rs + mb); \
      *(LDS3 u32x4*)(xw + (i) * 8192) = o; DMA(i); } while (0)
    LNROW(0); LNROW(1); LNROW(2); LNROW(3); LNROW(4); LNROW(5); LNROW(6); LNROW(7);
  }
  auto hpub = [&]() { if (!DW) {
    float sm = 0.f, sq = 0.f;
    #pragma unroll
    for (int i = 0; i < 2; ++i)
      #pragma unroll
      for (int j = 0; j < 4; ++j) { sm = dot_one(hr[i][j], sm); sq = dot_sq(hr[i][j], sq); }
    sm = dadd<0xB1>(sm); sq = dadd<0xB1>(sq); sm = dadd<0x4E>(sm); sq = dadd<0x4E>(sq);
    sm = dadd<0x141>(sm); sq = dadd<0x141>(sq); sm = dadd<0x140>(sm); sq = dadd<0x140>(sq);
    { auto a = __builtin_amdgcn_permlane16_swap(__float_as_uint(sm), __float_as_uint(sm), false, false); sm = __uint_as_float(a[0]) + __uint_as_float(a[1]);
      auto c = __builtin_amdgcn_permlane16_swap(__float_as_uint(sq), __float_as_uint(sq), false, false); sq = __uint_as_float(c[0]) + __uint_as_float(c[1]); }
    const float m = sm * (1.f / 512.f);
    const float var = fmaxf(sq * (1.f / 512.f) - m * m, 0.f);
    const float rs = __builtin_amdgcn_rsqf(var + 1e-5f);
    const float rs2 = rs * __builtin_amdgcn_rsqf(var * rs * rs + 1e-5f), mb2 = -m * rs2;
    char* hp = ws + WS_H + (size_t)(r >> 4) * 524288 + (size_t)hrow * 256 + (r & 15) * 16;
    #pragma unroll
    for (int i = 0; i < 2; ++i) {
      u32x4 o;
      #pragma unroll
      for (int j = 0; j < 4; ++j) o[j] = pk2(bflo(hr[i][j]) * rs2 + mb2, bfhi(hr[i][j]) * rs2 + mb2);
      store16_sc1(hp + i * 1048576, o);
    }
  } };
  STAMP(1);
  unsigned cur = 0;
  unsigned fre = 2 * SLOT + wd * 1024;

#define PMM(P, ks_, i_, j_) do { if (MM) acc[i_][j_] = mfma(fa[P][ks_][j_], fb[P][ks_][i_], acc[i_][j_]); SB(); } while (0)
#define PDM(FAST, i) do { if (FAST) { if (DWX) dma16p<(i) * 4096>(pb_, pv8[i], fre); } else DMX(DA, i); SB(); } while (0)
#define PSTEP(P, N, s_, MM_, FAST, MID) do { constexpr bool MM = MM_; STEPBAR(); PRIO(1); \
      PMM(P, 0, 1, 0); fb[N][0][1] = PLB(1, s_, 0); const char* pb_ = PBASE((s_) + 2); PX(P, 0); SB(); PDM(FAST, 0); \
      PMM(P, 0, 0, 0); fa[N][0][0] = PLA(0, 0); PX(P, 1); SB(); PDM(FAST, 1); \
      PMM(P, 0, 1, 1); fb[N][0][0] = PLB(0, s_, 0); fa[N][0][1] = PLA(1, 0); SB(); PDM(FAST, 2); \
      PMM(P, 0, 0, 1); fb[N][1][1] = PLB(1, s_, 1); PX(P, 2); SB(); PDM(FAST, 3); PRIO(0); \
      PMM(P, 1, 1, 0); fa[N][1][0] = PLA(0, 1); fb[N][1][0] = PLB(0, s_, 1); PX(P, 3); SB(); PDM(FAST, 4); \
      PMM(P, 1, 1, 1); fa[N][1][1] = PLA(1, 1); PX(P, 4); SB(); PDM(FAST, 5); \
      PMM(P, 1, 0, 0); PX(P, 5); PDM(FAST, 6); PDM(FAST, 7); MID; fre = cur + wd * 1024; cur = nextslot(cur); SB(); \
      PMM(P, 1, 0, 1); } while (0)
#define PLOOP(MID0) do { PSTEP(1, 0, 0, false, true, MID0); \
    for (int s = 1; s < 11; s += 2) { PSTEP(0, 1, s, true, true, auxmid(s)); PSTEP(1, 0, s + 1, true, true, (void)0); } \
    PSTEP(0, 1, 11, true, true, POLL_ISSUE); PSTEP(1, 0, 12, true, true, (void)0); \
    PSTEP(0, 1, 13, true, true, { POLL_CHECK; nextd(0); }); PSTEP(1, 0, 14, true, false, nextd(1)); PSTEP(0, 1, 15, true, false, nextd(2)); } while (0)

#define GMM(P, k_, j_) do { if (MM) acc[j_] = mfma(fa[P][k_][j_], fb[P][k_], acc[j_]); SB(); } while (0)
#define GSTEP(P, N, J, MM_, DD, MID) do { constexpr bool MM = MM_; \
    if (DW) { if ((DD) == 1) DMX_G(DA); if ((DD) == 2) DMX_ALL(DA); MID; } else { \
    asm volatile("" : "+v"(acc[0]), "+v"(acc[1])); \
    const unsigned ga_ = (J) < 3 ? gbase[(J) % 3] : 24576u, gb_ = (J) < 3 ? gbase[(J) % 3] + gsub8 : 24576u + gsub32; \
    GMM(P, 0, 0); GLB(N, 0); GLA(N, 0, 0); SB(); GMM(P, 0, 1); GLA(N, 0, 1); GLB(N, 1); SB(); \
    GMM(P, 1, 0); GLA(N, 1, 0); GLA(N, 1, 1); SB(); GMM(P, 1, 1); GLB(N, 2); GLA(N, 2, 0); SB(); \
    GMM(P, 2, 0); GLA(N, 2, 1); GLB(N, 3); SB(); GMM(P, 2, 1); GLA(N, 3, 0); GLA(N, 3, 1); SB(); \
    GMM(P, 3, 0); MID; SB(); GMM(P, 3, 1); } } while (0)
#define GLAST() do { if (!DW) { constexpr bool MM = true; GMM(1, 0, 0); GMM(1, 0, 1); GMM(1, 1, 0); GMM(1, 1, 1); GMM(1, 2, 0); GMM(1, 2, 1); GMM(1, 3, 0); GMM(1, 3, 1); } } while (0)
#define GBAR(N) do { if (DW) VMWAIT(N); LBAR(); } while (0)

  const int hh = w >> 1, rh = w & 1;
  bf16x8 qf[8];
  {
    f32x16 acc[2][2];
    #pragma unroll
    for (int i = 0; i < 2; ++i)
      #pragma unroll
      for (int j = 0; j < 2; ++j) acc[i][j] = f32x16{};
    unsigned aq[2], xrow[2];
    #pragma unroll
    for (int j = 0; j < 2; ++j) {
      aq[j] = (8 * h + q) * 1024 + (16 * hh + 4 * ((2 * rh + j) ^ q) + 2 * gq + (p & 1)) * 16 + (p >> 1) * 8;
      xrow[j] = (unsigned)(((32 * (rh ^ j) + r) ^ h) << 4) + h * 1024 + XO;
    }
    bf16x8 fa[2][2][2], fb[2][2][2];
    u32x4 pf0, pf1;
#define PLB(i_, s_, ks_) lds_b128((xrow[i_] ^ (unsigned)((4 * (((s_) + rq) & 1) + 2 * (ks_)) << 4)) + (4 * (((s_) + rq) & 15) + 2 * (ks_)) * 1024)
#define PX(P, n_) do { } while (0)
#define PLA(j_, ks_) lds_tr2(cur + aq[j_] + (16 * (ks_)) * 1024, cur + aq[j_] + (16 * (ks_) + 4) * 1024)
#define PBASE(s_) ((const char*)A.wq + (size_t)(((s_) + rq) & 15) * (32 * 8192) + g * 1024)
    unsigned pv8[8];
    #pragma unroll
    for (int i = 0; i < 8; ++i) pv8[i] = (4 * i + wd) * 8192u + wsw;
    auto auxmid = [&](int s) {
      if (!DW) { if (s == 3) VMWAIT(0); if (s == 5 && w == 4 && l == 0) flag_store(ws + WS_HFLAG + (rb * 8 + g) * 8, tag); }
    };
#define POLL_ISSUE do { if (!DW) asm volatile("global_load_dwordx4 %0, %2, off sc1\n\tglobal_load_dwordx4 %1, %2, off offset:16 sc1" \
                          : "=&v"(pf0), "=&v"(pf1) : "v"(ws + WS_HFLAG + l * 32) : "memory"); } while (0)
#define POLL_CHECK do { if (!DW) { \
      asm volatile("s_waitcnt vmcnt(0)" : "+v"(pf0), "+v"(pf1) :: "memory"); \
      const bool ok = pf0[0] == tagl && pf0[1] == tagh && pf0[2] == tagl && pf0[3] == tagh && pf1[0] == tagl && pf1[1] == tagh && pf1[2] == tagl && pf1[3] == tagh; \
      if (do_g1 && w == 4) { if (!__all(ok)) poll_flags((const u64*)(ws + WS_HFLAG), 256, tag, l); \
        if (b == 16 && l == 0) flag_store(ws + WS_EPOCH, tag); } } } while (0)
    auto nextd = [&](int n) { if (DW) { if (do_g1) d_g1(DA, n, (1 + n) % 3); else d_kv(DA, n - 1, ((1 + n) % 3) * SLOT); } };
    PLOOP(hpub());
#undef PLB
#undef PLA
#undef PBASE
#undef POLL_ISSUE
#undef POLL_CHECK
    { constexpr bool MM = true;
      PMM(1, 0, 1, 0); PMM(1, 0, 1, 1); PMM(1, 1, 1, 0); PMM(1, 1, 1, 1);
      LBAR();
      PMM(1, 0, 0, 0); PMM(1, 0, 0, 1);
      const unsigned xa = XO + w * 4096 + l * 16;
      *(LDS3 bf16x8*)(xa) = pack8<0>(acc[1][0], QSCALE); *(LDS3 bf16x8*)(xa + 1024) = pack8<1>(acc[1][0], QSCALE); SB();
      PMM(1, 1, 0, 0);
      *(LDS3 bf16x8*)(xa + 2048) = pack8<0>(acc[1][1], QSCALE); *(LDS3 bf16x8*)(xa + 3072) = pack8<1>(acc[1][1], QSCALE); SB();
      PMM(1, 1, 0, 1);
      STEPBAR();
      const unsigned xb2 = XO + (w ^ 1) * 4096 + l * 16;
      qf[4] = lds_b128(xb2); qf[5] = lds_b128(xb2 + 1024); qf[6] = lds_b128(xb2 + 2048); qf[7] = lds_b128(xb2 + 3072);
      qf[0] = pack8<0>(acc[0][0], QSCALE); qf[1] = pack8<1>(acc[0][0], QSCALE); qf[2] = pack8<0>(acc[0][1], QSCALE); qf[3] = pack8<1>(acc[0][1], QSCALE);
    }
#undef PX
    asm volatile("" : "+v"(qf[0]), "+v"(qf[1]), "+v"(qf[2]), "+v"(qf[3]), "+v"(qf[4]), "+v"(qf[5]), "+v"(qf[6]), "+v"(qf[7]));
  }
  STAMP(2);
  unsigned fill = 0;

  if (do_g1) {
    f32x16 acc[2] = {f32x16{}, f32x16{}};
    const unsigned tb = (8 * h + q) * 128 + (2 * gq + (p >> 1)) * 16 + (p & 1) * 8;
    const unsigned aa[2] = {tb + 64 * (q >> 1), tb + 64 * (1 ^ (q >> 1))};
    const unsigned ba = tb + 64 * ((wd & 1) ^ (q >> 1));
    const unsigned gbase[3] = {32768u, 65536u, 0u};
    const unsigned gsub8 = 8192u * (1 + (wd >> 1)), gsub32 = 32768u * (1 + (wd >> 1));
    bf16x8 fa[2][4][2], fb[2][4];
#define GLA(N, kk, j_) fa[N][kk][j_] = lds_tr2(ga_ + aa[j_] + (16 * (kk)) * 128, ga_ + aa[j_] + (16 * (kk) + 4) * 128)
#define GLB(N, kk) fb[N][kk] = lds_tr2(gb_ + ba + (16 * (kk)) * 128, gb_ + ba + (16 * (kk) + 4) * 128)
#define G1D(t_, j_) do { if (DW) d_g1(DA, t_, j_); } while (0)
    if (DW) { DMX_G(DA); d_g1(DA, 3, 3); }
    GSTEP(1, 0, 0, false, 1, G1D(4, 1));
    GBAR(12); GSTEP(0, 1, 1, true, 1, G1D(5, 2));
    GBAR(12); GSTEP(1, 0, 2, true, 1, G1D(6, 0));
    GBAR(12); GSTEP(0, 1, 3, true, 1, G1D(7, 3));
    for (int t = 4; t < 28; t += 4) {
      GBAR(12); GSTEP(1, 0, 0, true, 1, G1D(t + 4, 1));
      GBAR(12); GSTEP(0, 1, 1, true, 1, G1D(t + 5, 2));
      GBAR(12); GSTEP(1, 0, 2, true, 1, G1D(t + 6, 0));
      GBAR(12); GSTEP(0, 1, 3, true, 1, G1D(t + 7, 3));
    }
    GBAR(12); GSTEP(1, 0, 0, true, 1, if (DW) d_kv(DA, -1, XO));
    GBAR(12); GSTEP(0, 1, 1, true, 2, if (DW) d_kv(DA, 0, XO + SLOT));
    GBAR(14); GSTEP(1, 0, 2, true, 2, if (DW) d_kv(DA, 1, 0));
    constexpr int f2 = DW ? 1 : 0;
    u32x2 bb[4];
    asm volatile("global_load_dwordx2 %0, %4, off\n\tglobal_load_dwordx2 %1, %4, off offset:16\n\tglobal_load_dwordx2 %2, %4, off offset:32\n\tglobal_load_dwordx2 %3, %4, off offset:48"
                 : "=&v"(bb[0]), "=&v"(bb[1]), "=&v"(bb[2]), "=&v"(bb[3]) : "v"(A.b1 + 64 * ft1 + 32 * f2 + 4 * h) : "memory");
    GBAR(20); GSTEP(0, 1, 3, true, 0, (void)0);
    if (!DW) {
      constexpr bool MM = true;
      GMM(1, 0, 1); GMM(1, 1, 1); GMM(1, 2, 1); GMM(1, 3, 1); GMM(1, 0, 0); GMM(1, 1, 0);
      #pragma unroll
      for (int m = 0; m < 4; ++m) {
        const f32x4 v4 = {acc[1][4 * m], acc[1][4 * m + 1], acc[1][4 * m + 2], acc[1][4 * m + 3]};
        *(LDS3 f32x4*)(SLOT + wd * 4096 + m * 1024 + l * 16) = v4;
      }
      SB(); GMM(1, 2, 0); GMM(1, 3, 0);
    }
    asm volatile("s_waitcnt vmcnt(0)" : "+v"(bb[0]), "+v"(bb[1]), "+v"(bb[2]), "+v"(bb[3]) :: "memory");
    LBAR();
#undef GLA
#undef GLB
    if (DW) {
      #pragma unroll
      for (int m = 0; m < 4; ++m) {
        const f32x4 v4 = *(LDS3 f32x4*)(SLOT + wd * 4096 + m * 1024 + l * 16);
        acc[0][4 * m] = v4[0]; acc[0][4 * m + 1] = v4[1]; acc[0][4 * m + 2] = v4[2]; acc[0][4 * m + 3] = v4[3];
      }
    }
    #pragma unroll
    for (int m = 0; m < 4; ++m) {
      acc[0][4 * m] += bflo(bb[m][0]); acc[0][4 * m + 1] += bfhi(bb[m][0]); acc[0][4 * m + 2] += bflo(bb[m][1]); acc[0][4 * m + 3] += bfhi(bb[m][1]);
    }
    #pragma unroll
    for (int e = 0; e < 16; ++e) {
      const float u = acc[0][e];
      const float z = u * (1.f + 0.044715f * u * u) * (-2.f * 0.7978845608028654f * 1.4426950408889634f);
      acc[0][e] = u * __builtin_amdgcn_rcpf(1.f + __builtin_amdgcn_exp2f(z));
    }
    char* up = ws + WS_U + (size_t)(2 * dp1 + (wd >> 1)) * (224 * 1024) + (size_t)(ft1 * 8 + f2 * 4 + h) * 1024 + (32 * (wd & 1) + r) * 16;
    store16_sc1(up, __builtin_bit_cast(u32x4, pack8n<0>(acc[0])));
    store16_sc1(up + 2048, __builtin_bit_cast(u32x4, pack8n<1>(acc[0])));
    cur = XO;
  }
  STAMP(3);

  {
    f32x16 o[4];
    f32x16 negm, sA, sB;
    float lsum = 0.f;
    unsigned ka[8];
    #pragma unroll
    for (int kk = 0; kk < 8; ++kk) ka[kk] = (r * 256 + ((h ^ (r & 15)) << 4)) ^ (unsigned)((kk ^ (4 * rh)) << 5);
    unsigned va[4];
    #pragma unroll
    for (int d = 0; d < 4; ++d) va[d] = 16384 + (4 * h + q) * 256 + (4 * (d ^ q) + 2 * gq + (p >> 1)) * 16 + (p & 1) * 8;
    bf16x8 kf[8], vf[8];
    u32x4 pw0 = {}, pw1 = {};
    float mx;
    unsigned kc_[8], vc_[4];
#define SETADDR() do { _Pragma("unroll") for (int i_ = 0; i_ < 8; ++i_) { kc_[i_] = cur + ka[i_]; } _Pragma("unroll") for (int i_ = 0; i_ < 4; ++i_) { vc_[i_] = cur + va[i_]; } } while (0)
#define LDK(i, KB) kf[i] = lds_b128(kc_[i] + (KB) * 8192)
#define LDV(i, VP) vf[i] = lds_tr2(vc_[(i) & 3] + ((VP) * 32 + 16 * ((i) >> 2)) * 256, vc_[(i) & 3] + ((VP) * 32 + 16 * ((i) >> 2) + 8) * 256)
#define EXP(Y, e) do { Y[e] = __builtin_amdgcn_exp2f(Y[e]); } while (0)
#define ADD(Y, e) do { lsum += Y[e]; } while (0)
#define CVT(P, Y, c) do { P[(c) & 3] = pk2(Y[2 * (c)], Y[2 * (c) + 1]); } while (0)
#define MX(X, e) do { mx = fmaxf(fmaxf(mx, X[2 * (e)]), X[2 * (e) + 1]); } while (0)
#define QK(X, i) do { X = mfma(kf[i], qf[i], (i) == 0 ? negm : X); } while (0)
#define PV(i) do { o[(i) & 3] = mfma(vf[i], __builtin_bit_cast(bf16x8, (i) < 4 ? pw0 : pw1), o[(i) & 3]); } while (0)
#define DMAC(C, i) do { if (C) DMA(i); } while (0)
#define STAGE_A(X, Y, KB, VP, DC) do { \
      QK(X, 0); LDK(6, KB); EXP(Y, 0); EXP(Y, 1); DMAC(DC, 3); SB(); \
      QK(X, 1); LDK(7, KB); EXP(Y, 2); CVT(pw0, Y, 0); DMAC(DC, 4); SB(); \
      QK(X, 2); LDV(0, VP); EXP(Y, 3); EXP(Y, 4); DMAC(DC, 5); SB(); \
      QK(X, 3); LDV(1, VP); EXP(Y, 5); ADD(Y, 0); CVT(pw0, Y, 1); DMAC(DC, 6); SB(); \
      QK(X, 4); LDV(2, VP); EXP(Y, 6); EXP(Y, 7); CVT(pw0, Y, 2); DMAC(DC, 7); SB(); \
      QK(X, 5); LDV(3, VP); EXP(Y, 8); ADD(Y, 1); ADD(Y, 2); CVT(pw0, Y, 3); SB(); \
      QK(X, 6); LDV(4, VP); EXP(Y, 9); EXP(Y, 10); ADD(Y, 3); SB(); \
      QK(X, 7); LDV(5, VP); EXP(Y, 11); ADD(Y, 4); CVT(pw1, Y, 4); SB(); \
    } while (0)
#define RESCALE(X) do { \
      if (__any(mx > THR)) { \
        const float delta = fmaxf(mx, 0.f); \
        const float alpha = __builtin_amdgcn_exp2f(-delta); \
        _Pragma("unroll") for (int e = 0; e < 16; ++e) { X[e] -= delta; negm[e] -= delta; } \
        _Pragma("unroll") for (int d = 0; d < 4; ++d) _Pragma("unroll") for (int e = 0; e < 16; ++e) o[d][e] *= alpha; \
        lsum *= alpha; \
      } } while (0)
#define STAGE_B(X, Y, VP, KBN) do { \
      PV(0); LDV(6, VP); EXP(Y, 12); EXP(Y, 13); CVT(pw1, Y, 5); SB(); \
      PV(1); LDV(7, VP); EXP(Y, 14); EXP(Y, 15); ADD(Y, 5); SB(); \
      PV(2); LDK(0, KBN); CVT(pw1, Y, 6); CVT(pw1, Y, 7); ADD(Y, 6); mx = -3.0e38f; MX(X, 0); SB(); \
      PV(3); LDK(1, KBN); MX(X, 1); MX(X, 2); ADD(Y, 7); ADD(Y, 8); SB(); \
      PV(4); LDK(2, KBN); MX(X, 3); MX(X, 4); ADD(Y, 9); ADD(Y, 10); SB(); \
      PV(5); LDK(3, KBN); MX(X, 5); MX(X, 6); ADD(Y, 11); ADD(Y, 12); SB(); \
      PV(6); LDK(4, KBN); MX(X, 7); ADD(Y, 13); ADD(Y, 14); SB(); \
      PV(7); LDK(5, KBN); mx = half_max(mx); ADD(Y, 15); SB(); \
      RESCALE(X); SB(); \
    } while (0)
#define STAGE_B_BAR(X, Y, VP, WAITS, SETD, POST) do { \
      PV(0); LDV(6, VP); EXP(Y, 12); EXP(Y, 13); CVT(pw1, Y, 5); SB(); \
      PV(1); LDV(7, VP); EXP(Y, 14); EXP(Y, 15); ADD(Y, 5); SB(); \
      PV(2); CVT(pw1, Y, 6); CVT(pw1, Y, 7); ADD(Y, 6); mx = -3.0e38f; MX(X, 0); SB(); \
      cur = next5(cur); \
      PV(3); SETADDR(); SETD; SB(); \
      PV(4); SB(); \
      WAITS; LBAR(); PRIO(3); \
      PV(5); SB(); LDK(0, 0); LDK(1, 0); LDK(2, 0); LDK(3, 0); MX(X, 1); MX(X, 2); MX(X, 3); MX(X, 4); ADD(Y, 7); ADD(Y, 8); ADD(Y, 9); POST; DMA(0); SB(); \
      PV(6); LDK(4, 0); MX(X, 5); MX(X, 6); MX(X, 7); ADD(Y, 10); ADD(Y, 11); ADD(Y, 12); DMA(1); SB(); \
      PV(7); LDK(5, 0); mx = half_max(mx); ADD(Y, 13); ADD(Y, 14); ADD(Y, 15); DMA(2); SB(); \
      RESCALE(X); SB(); \
    } while (0)

    SETADDR();
    #pragma unroll
    for (int i = 0; i < 8; ++i) LDK(i, 0);
    negm = f32x16{};
    #pragma unroll
    for (int d = 0; d < 4; ++d) o[d] = f32x16{};
    SB();
    QK(sA, 0); LDK(0, 1); DMA(0); SB(); QK(sA, 1); LDK(1, 1); DMA(1); SB(); QK(sA, 2); LDK(2, 1); DMA(2); SB(); QK(sA, 3); LDK(3, 1); SB();
    QK(sA, 4); LDK(4, 1); SB(); QK(sA, 5); LDK(5, 1); SB(); QK(sA, 6); SB(); QK(sA, 7); SB();
    mx = -3.0e38f;
    #pragma unroll
    for (int e = 0; e < 8; ++e) MX(sA, e);
    mx = half_max(mx);
    #pragma unroll
    for (int e = 0; e < 16; ++e) { sA[e] -= mx; negm[e] = -mx; }
    SB();
    STAGE_A(sB, sA, 1, 1, true);
    fill = SLOT;
    STAGE_B_BAR(sB, sA, 1, VMWAIT(0), if (DW) d_kv(DA, 2, fill),
                if (!DW && do_g1 && w == 4 && l == 0) flag_store(ws + WS_UFLAG + (dp1 * 28 + ft1) * 8, tag));
    for (int u = 0; u < 7; ++u) {
      STAGE_A(sA, sB, 0, 0, true);
      PRIO(2);
      STAGE_B(sA, sB, 0, 1);
      PRIO(1);
      STAGE_A(sB, sA, 1, 1, false);
      PRIO(0);
      fill = nextslot(fill);
      STAGE_B_BAR(sB, sA, 1, if (DW) VMWAIT(8), if (DW) { if (u + 3 < 8) d_kv(DA, u + 3, fill); else d_wo(DA, u - 5, fill); }, (void)0);
    }
    PRIO(0);
    #pragma unroll
    for (int i = 0; i < 8; ++i) LDV(i, 0);
    DMA(3); DMA(4);
    #pragma unroll
    for (int e = 0; e < 16; ++e) { EXP(sB, e); ADD(sB, e); }
    #pragma unroll
    for (int c = 0; c < 4; ++c) { CVT(pw0, sB, c); CVT(pw1, sB, 4 + c); }
    SB();
    fre = cur + wd * 1024; cur = nextslot(cur);
    const float inv = __builtin_amdgcn_rcpf(half_sum(lsum));
    const unsigned ob = XO + (hh * 16 + h) * 1024 + (32 * rh + r) * 16;
#define OWR(d) do { *(LDS3 bf16x8*)(ob + (4 * (d)) * 1024) = pack8<0>(o[d], inv); *(LDS3 bf16x8*)(ob + (4 * (d) + 2) * 1024) = pack8<1>(o[d], inv); } while (0)
    PV(0); PV(4); DMA(5); SB(); PV(1); PV(5); DMA(6); SB(); PV(2); OWR(0); SB(); PV(6); DMA(7); SB(); PV(3); OWR(1); SB(); PV(7); SB(); OWR(2); OWR(3);
  }
  STAMP(4);

  {
    f32x16 acc[2][2];
    #pragma unroll
    for (int i = 0; i < 2; ++i)
      #pragma unroll
      for (int j = 0; j < 2; ++j) acc[i][j] = f32x16{};
    unsigned oa[2];
    #pragma unroll
    for (int j = 0; j < 2; ++j) oa[j] = (4 * h + q) * 1024 + (16 * (w >> 1) + 4 * ((2 * (w & 1) + j) ^ q) + 2 * gq + (p >> 1)) * 16 + (p & 1) * 8;
    const unsigned ob = XO + h * 1024 + r * 16;
    bf16x8 fa[2][2][2], fb[2][2][2];
    u32x2 pfl;
#define PLB(i_, s_, ks_) lds_b128(ob + (4 * (((s_) + ro) & 15) + 2 * (ks_)) * 1024 + 512 * (i_))
#define PLA(j_, ks_) lds_tr2(cur + oa[j_] + (16 * (ks_)) * 1024, cur + oa[j_] + (16 * (ks_) + 8) * 1024)
#define PBASE(s_) ((const char*)A.wo + (size_t)(16 * g + (((s_) + ro) & 15)) * 32768)
    unsigned pv8[8];
    #pragma unroll
    for (int i = 0; i < 8; ++i) pv8[i] = (4 * i + wd) * 1024u + wsw;
    auto auxmid = [&](int) {};
#define PX(P, n_) do { } while (0)
#define POLL_ISSUE do { if (!DW) asm volatile("global_load_dwordx2 %0, %1, off sc1" : "=&v"(pfl) : "v"(ws + WS_UFLAG + ((dt2 >> 1) * 28 + (l < 28 ? l : 0)) * 8) : "memory"); } while (0)
#define POLL_CHECK do { if (!DW) { asm volatile("s_waitcnt vmcnt(0)" : "+v"(pfl) :: "memory"); \
      if (do_g2 && w == 4 && !__all(pfl[0] == tagl && pfl[1] == tagh)) poll_flags((const u64*)(ws + WS_UFLAG) + (dt2 >> 1) * 28, 28, tag, l); } } while (0)
    auto nextd = [&](int n) { if (DW) { if (do_g2) d_g2(DA, n, (2 + n) % 3); else d_wo(DA, n, ((2 + n) % 3) * SLOT); } };
    PLOOP((void)0);
#undef PLB
#undef PLA
#undef PBASE
#undef POLL_ISSUE
#undef POLL_CHECK
    char* pp = ws + WS_PART + ((size_t)(rb * 8 + w) * 8 + g) * 8192 + l * 16;
    { constexpr bool MM = true;
      PMM(1, 0, 0, 0); PMM(1, 1, 0, 0); PMM(1, 0, 0, 1); PMM(1, 1, 0, 1); PMM(1, 0, 1, 0);
      store16_sc1(pp, __builtin_bit_cast(u32x4, pack8n<0>(acc[0][0]))); store16_sc1(pp + 4096, __builtin_bit_cast(u32x4, pack8n<1>(acc[0][0]))); SB();
      PMM(1, 1, 1, 0);
      store16_sc1(pp + 1024, __builtin_bit_cast(u32x4, pack8n<0>(acc[0][1]))); store16_sc1(pp + 5120, __builtin_bit_cast(u32x4, pack8n<1>(acc[0][1]))); SB();
      PMM(1, 0, 1, 1); PMM(1, 1, 1, 1);
      store16_sc1(pp + 2048, __builtin_bit_cast(u32x4, pack8n<0>(acc[1][0]))); store16_sc1(pp + 6144, __builtin_bit_cast(u32x4, pack8n<1>(acc[1][0])));
      store16_sc1(pp + 3072, __builtin_bit_cast(u32x4, pack8n<0>(acc[1][1]))); store16_sc1(pp + 7168, __builtin_bit_cast(u32x4, pack8n<1>(acc[1][1])));
    }
    STAMP(5);
    VMWAIT(0);
    if (l == 0) flag_store(ws + WS_SFLAG + ((rb * 8 + w) * 8 + g) * 8, tag);
    if (do_g2) LBAR();
  }
  STAMP(6);

  if (do_g2) {
    const int wi = (w >> 1) & 1, wj = w & 1, sb2 = DW ? 1 : 0;
    f32x16 acc[2] = {f32x16{}, f32x16{}};
    const unsigned aa = h * 1024 + r * 16;
    const unsigned ba = (4 * h + q) * 128 + (4 * ((wd & 1) ^ (q >> 1)) + 2 * gq + (p >> 1)) * 16 + (p & 1) * 8;
    const unsigned gbase[3] = {65536u, 0u, 32768u};
    const unsigned gsub8 = 8192u * (1 + (wd >> 1)), gsub32 = 32768u * (1 + (wd >> 1));
    bf16x8 fa[2][4][2], fb[2][4];
    const int srow = 64 * (2 * pp2 + sb2) + 32 * wi + r;
    const size_t xo = (size_t)srow * 512 + 64 * dt2 + 32 * wj + 8 * h;
    const char* pp = ws + WS_PART + (size_t)((2 * pp2 + sb2) * 8 + dt2) * 65536 + (wi * 2 + wj) * 1024 + l * 16;
    const u64* sfl = (const u64*)(ws + WS_SFLAG) + ((2 * pp2 + sb2) * 8 + dt2) * 8;
    u32x4 pv[16];
    u32x2 pfl;
#define GLA(N, kk, j_) fa[N][kk][j_] = lds_b128(ga_ + aa + 2 * (kk) * 1024 + 512 * (j_))
#define GLB(N, kk) fb[N][kk] = lds_tr2(gb_ + ba + (16 * (kk)) * 128, gb_ + ba + (16 * (kk) + 8) * 128)
#define PLD(c) asm volatile("global_load_dwordx4 %0, %1, off sc1" : "=v"(pv[c]) : "v"(pp + ((c) >> 1) * 8192 + ((c) & 1) * 4096) : "memory")
#define G2D(t_, j_, AUX) do { if (DW) d_g2(DA, t_, j_); else { AUX; } } while (0)
    if (DW) { DMX_G(DA); d_g2(DA, 3, 3); }
    GSTEP(1, 0, 0, false, 1, G2D(4, 2, (void)0));
    GBAR(12); GSTEP(0, 1, 1, true, 1, G2D(5, 0, (void)0));
    GBAR(12); GSTEP(1, 0, 2, true, 1, G2D(6, 1, (void)0));
    GBAR(12); GSTEP(0, 1, 3, true, 1, G2D(7, 3, (void)0));
    for (int t = 4; t < 12; t += 4) {
      GBAR(12); GSTEP(1, 0, 0, true, 1, G2D(t + 4, 2, (void)0));
      GBAR(12); GSTEP(0, 1, 1, true, 1, G2D(t + 5, 0, (void)0));
      GBAR(12); GSTEP(1, 0, 2, true, 1, G2D(t + 6, 1, (void)0));
      GBAR(12); GSTEP(0, 1, 3, true, 1, G2D(t + 7, 3, (void)0));
    }
    GBAR(12); GSTEP(1, 0, 0, true, 1, G2D(16, 2, asm volatile("global_load_dwordx2 %0, %1, off sc1" : "=&v"(pfl) : "v"(sfl + (l & 7)) : "memory")));
    GBAR(12); GSTEP(0, 1, 1, true, 1, G2D(17, 0, (void)0));
    GBAR(12); GSTEP(1, 0, 2, true, 1, G2D(18, 1, (void)0));
    GBAR(12); GSTEP(0, 1, 3, true, 1, G2D(19, 3, { asm volatile("s_waitcnt vmcnt(0)" : "+v"(pfl) :: "memory");
                                                  if (!__all(pfl[0] == tagl && pfl[1] == tagh)) poll_flags(sfl, 8, tag, l); PLD(0); PLD(1); }));
    GBAR(12); GSTEP(1, 0, 0, true, 1, G2D(20, 2, { PLD(2); PLD(3); }));
    GBAR(12); GSTEP(0, 1, 1, true, 1, G2D(21, 0, { PLD(4); PLD(5); }));
    GBAR(12); GSTEP(1, 0, 2, true, 1, G2D(22, 1, { PLD(6); PLD(7); }));
    GBAR(12); GSTEP(0, 1, 3, true, 1, G2D(23, 3, { PLD(8); PLD(9); }));
    GBAR(12); GSTEP(1, 0, 0, true, 1, G2D(24, 2, { PLD(10); PLD(11); }));
    GBAR(12); GSTEP(0, 1, 1, true, 1, G2D(25, 0, { PLD(12); PLD(13); }));
    GBAR(12); GSTEP(1, 0, 2, true, 1, G2D(26, 1, { PLD(14); PLD(15); }));
    GBAR(12); GSTEP(0, 1, 3, true, 1, G2D(27, 3, (void)0));
    GBAR(12); GSTEP(1, 0, 0, true, 1, (void)0);
    u32x4 xv0, xv1; unsigned b2r;
    asm volatile("global_load_dwordx4 %0, %3, off\n\tglobal_load_dwordx4 %1, %3, off offset:32\n\tglobal_load_ushort %2, %4, off"
                 : "=&v"(xv0), "=&v"(xv1), "=&v"(b2r) : "v"(x + xo), "v"(A.b2 + srow) : "memory");
    GBAR(15); GSTEP(0, 1, 1, true, 0, (void)0);
    GBAR(9); GSTEP(1, 0, 2, true, 0, (void)0);
    GBAR(3); GSTEP(0, 1, 3, true, 0, (void)0);
    GLAST();
#undef GLA
#undef GLB
    if (!DW) {
      #pragma unroll
      for (int dh = 0; dh < 2; ++dh)
        #pragma unroll
        for (int m = 0; m < 4; ++m) {
          const f32x4 v4 = {acc[dh][4 * m], acc[dh][4 * m + 1], acc[dh][4 * m + 2], acc[dh][4 * m + 3]};
          *(LDS3 f32x4*)(XO + (wd * 2 + dh) * 4096 + m * 1024 + l * 16) = v4;
        }
    }
    LBAR();
    f32x16 yv;
    #pragma unroll
    for (int m = 0; m < 4; ++m) {
      const f32x4 v4 = *(LDS3 f32x4*)(XO + ((sb2 * 2 + wi) * 2 + wj) * 4096 + m * 1024 + l * 16);
      yv[4 * m] = v4[0]; yv[4 * m + 1] = v4[1]; yv[4 * m + 2] = v4[2]; yv[4 * m + 3] = v4[3];
    }
    STAMP(7);
    asm volatile("s_waitcnt vmcnt(0)" : "+v"(xv0), "+v"(xv1), "+v"(b2r) :: "memory");
    if (!DW) asm volatile("" : "+v"(pv[0]), "+v"(pv[1]), "+v"(pv[2]), "+v"(pv[3]), "+v"(pv[4]), "+v"(pv[5]), "+v"(pv[6]), "+v"(pv[7]),
                 "+v"(pv[8]), "+v"(pv[9]), "+v"(pv[10]), "+v"(pv[11]), "+v"(pv[12]), "+v"(pv[13]), "+v"(pv[14]), "+v"(pv[15]) :: "memory");
    if (DW) {
      unsigned done = 0;
      const char* pb = ws + WS_PART + (size_t)((2 * pp2 + 1) * 8 + dt2) * 65536 + (wi * 2 + wj) * 1024;
      for (unsigned spins = 0; done != 0xffu && spins < (1u << 20); ++spins) {
        const u64 f = __hip_atomic_load((GAS const u64*)(sfl + (l & 7)), __ATOMIC_RELAXED, __HIP_MEMORY_SCOPE_AGENT);
        unsigned nw = (unsigned)__ballot(f == tag) & 0xffu & ~done;
        done |= nw;
        if (!nw) __builtin_amdgcn_s_sleep(1);
        while (nw) {
          const int gg = __builtin_ctz(nw); nw &= nw - 1;
          dma16o<0>(pb + gg * 8192, l * 16u, wd * 16384 + gg * 2048);
          dma16o<1024>(pb + gg * 8192 + 4096, l * 16u, wd * 16384 + gg * 2048);
        }
      }
    }
    const float bias2 = bflo(b2r);
    #pragma unroll
    for (int e = 0; e < 16; ++e) {
      const float yy = yv[e] + bias2;
      yv[e] = yy * __builtin_amdgcn_rcpf(1.f + __builtin_amdgcn_exp2f(-1.4426950408889634f * yy));
    }
    if (DW) {
      VMWAIT(0);
      #pragma unroll
      for (int c = 0; c < 16; ++c) pv[c] = *(LDS3 u32x4*)(wd * 16384 + c * 1024 + l * 16);
    }
    #pragma unroll
    for (int c = 0; c < 2; ++c) {
      float t[8];
      #pragma unroll
      for (int e = 0; e < 8; ++e) t[e] = 0.f;
      #pragma unroll
      for (int gg = 0; gg < 8; ++gg)
        #pragma unroll
        for (int e = 0; e < 4; ++e) { t[2 * e] += bflo(pv[2 * gg + c][e]); t[2 * e + 1] += bfhi(pv[2 * gg + c][e]); }
      #pragma unroll
      for (int e = 0; e < 8; ++e) t[e] *= yv[8 * c + e];
      #pragma unroll
      for (int e = 0; e < 4; ++e) {
        auto rr = __builtin_amdgcn_permlane32_swap(__float_as_uint(t[e]), __float_as_uint(t[4 + e]), false, false);
        t[e] = __uint_as_float(rr[0]); t[4 + e] = __uint_as_float(rr[1]);
      }
      const u32x4 xv = c ? xv1 : xv0;
      u32x4 ov;
      #pragma unroll
      for (int e = 0; e < 4; ++e) ov[e] = pk2(t[2 * e] + bflo(xv[e]), t[2 * e + 1] + bfhi(xv[e]));
      *(u32x4*)(A.out + xo + 16 * c) = ov;
    }
  } else { STAMP(7); }
  STAMP(8);
  TS();
}

__global__ __launch_bounds__(512, 2) void fused_kernel(Args A) {
  extern __shared__ __attribute__((aligned(16))) char lds_raw[];
  const int l = threadIdx.x & 63;
  const int w = __builtin_amdgcn_readfirstlane(threadIdx.x >> 6);
  if (w < 4) body<true>(A, w, l); else body<false>(A, w, l);
}

extern "C" void kernel_launch(void* const* d_in, const int* in_sizes, int n_in, void* d_out, int out_size, void* d_ws, size_t ws_size, hipStream_t stream) {
  (void)hipFuncSetAttribute((const void*)fused_kernel, hipFuncAttributeMaxDynamicSharedMemorySize, LDS_BYTES);
  Args A;
  A.x = (const __bf16*)d_in[0]; A.kc = (const __bf16*)d_in[1]; A.vc = (const __bf16*)d_in[2]; A.wq = (const __bf16*)d_in[3]; A.wo = (const __bf16*)d_in[4];
  A.w1 = (const __bf16*)d_in[6]; A.b1 = (const __bf16*)d_in[7]; A.w2 = (const __bf16*)d_in[8]; A.b2 = (const __bf16*)d_in[9];
  A.out = (__bf16*)d_out; A.ws = (char*)d_ws;
  fused_kernel<<<256, 512, LDS_BYTES, stream>>>(A);
}
```

```cpp
#include <hip/hip_runtime.h>
#include <stdint.h>
#include <type_traits>
#pragma clang diagnostic ignored "-Wint-to-pointer-cast"

typedef __bf16 bf16x8 __attribute__((ext_vector_type(8)));
typedef __bf16 bf16x4 __attribute__((ext_vector_type(4)));
typedef __bf16 bf16x2 __attribute__((ext_vector_type(2)));
typedef float f32x16 __attribute__((ext_vector_type(16)));
typedef float f32x4 __attribute__((ext_vector_type(4)));
typedef float f32x2 __attribute__((ext_vector_type(2)));
typedef unsigned u32x4 __attribute__((ext_vector_type(4)));
typedef unsigned u32x2 __attribute__((ext_vector_type(2)));
typedef unsigned long long u64;
#define LDS3 __attribute__((address_space(3)))
#define GAS __attribute__((address_space(1)))
#define DI __device__ __forceinline__
#define SB() __builtin_amdgcn_sched_barrier(0)
#define PLAINPOL ""
#define PRIO(n) __builtin_amdgcn_s_setprio(n)

constexpr int SLOT = 32768;
constexpr int XO = 98304;
constexpr int LDS_BYTES = 163840;
constexpr size_t WS_EPOCH = 0;
constexpr size_t WS_HFLAG = 4096;
constexpr size_t WS_UFLAG = 8192;
constexpr size_t WS_SFLAG = 16384;
constexpr size_t WS_DAG   = 65536;
constexpr size_t WS_H     = 1u << 20;
constexpr size_t WS_U     = 4u << 20;
constexpr size_t WS_PART  = 8u << 20;

constexpr int CODE_KB = 64;
constexpr float THR = 16.0f;
constexpr float QSCALE = 0.08838834764831845f * 1.4426950408889634f;

DI void dma16(const void* sbase, unsigned voff, unsigned lds) {
  asm volatile("s_mov_b32 m0, %2\n\ts_nop 0\n\tglobal_load_lds_dwordx4 %0, %1" :: "v"(voff), "s"(sbase), "s"(lds) : "memory");
}
DI void dma16_sc1(const void* sbase, unsigned voff, unsigned lds) {
  asm volatile("s_mov_b32 m0, %2\n\ts_nop 0\n\tglobal_load_lds_dwordx4 %0, %1 sc1" :: "v"(voff), "s"(sbase), "s"(lds) : "memory");
}
DI void dma16x4_sc1(const void* sbase, unsigned voff, unsigned lds) {
  asm volatile("s_mov_b32 m0, %2\n\ts_nop 0\n\tglobal_load_lds_dwordx4 %0, %1 sc1\n\tglobal_load_lds_dwordx4 %0, %1 offset:1024 sc1\n\t"
               "global_load_lds_dwordx4 %0, %1 offset:2048 sc1\n\tglobal_load_lds_dwordx4 %0, %1 offset:3072 sc1" :: "v"(voff), "s"(sbase), "s"(lds) : "memory");
}
DI bf16x8 lds_b128(unsigned a) { return *(LDS3 bf16x8*)a; }
DI bf16x8 lds_tr2(unsigned a0, unsigned a1) {
  bf16x4 lo = __builtin_amdgcn_ds_read_tr16_b64_v4bf16((LDS3 bf16x4*)a0);
  bf16x4 hi = __builtin_amdgcn_ds_read_tr16_b64_v4bf16((LDS3 bf16x4*)a1);
  return __builtin_shufflevector(lo, hi, 0, 1, 2, 3, 4, 5, 6, 7);
}
DI f32x16 mfma(bf16x8 a, bf16x8 b, f32x16 c) { return __builtin_amdgcn_mfma_f32_32x32x16_bf16(a, b, c, 0, 0, 0); }
DI unsigned pk2(float lo, float hi) { f32x2 v = {lo, hi}; bf16x2 b = __builtin_convertvector(v, bf16x2); return __builtin_bit_cast(unsigned, b); }
template <int S> DI bf16x8 pack8(const f32x16& x, float sc) {
  u32x4 p;
  p[0] = pk2(x[8 * S + 0] * sc, x[8 * S + 1] * sc); p[1] = pk2(x[8 * S + 2] * sc, x[8 * S + 3] * sc);
  p[2] = pk2(x[8 * S + 4] * sc, x[8 * S + 5] * sc); p[3] = pk2(x[8 * S + 6] * sc, x[8 * S + 7] * sc);
  return __builtin_bit_cast(bf16x8, p);
}
template <int S> DI bf16x8 pack8n(const f32x16& x) {
  u32x4 p;
  p[0] = pk2(x[8 * S + 0], x[8 * S + 1]); p[1] = pk2(x[8 * S + 2], x[8 * S + 3]);
  p[2] = pk2(x[8 * S + 4], x[8 * S + 5]); p[3] = pk2(x[8 * S + 6], x[8 * S + 7]);
  return __builtin_bit_cast(bf16x8, p);
}
DI float bflo(unsigned u) { return __uint_as_float(u << 16); }
DI float bfhi(unsigned u) { return __uint_as_float(u & 0xffff0000u); }
template <int C> DI float dadd(float v) { return v + __builtin_bit_cast(float, __builtin_amdgcn_update_dpp(0, __builtin_bit_cast(int, v), C, 0xf, 0xf, true)); }
DI float dot_one(unsigned u, float c) { return __builtin_amdgcn_fdot2_f32_bf16(__builtin_bit_cast(bf16x2, u), __builtin_bit_cast(bf16x2, 0x3f803f80u), c, false); }
DI float dot_sq(unsigned u, float c) { return __builtin_amdgcn_fdot2_f32_bf16(__builtin_bit_cast(bf16x2, u), __builtin_bit_cast(bf16x2, u), c, false); }
DI void frag_sum(bf16x8 f, float& c) { const u32x4 u = __builtin_bit_cast(u32x4, f); const unsigned a = u[0], b = u[1], d = u[2], e = u[3]; c = dot_one(e, dot_one(d, dot_one(b, dot_one(a, c)))); }
DI void frag_sq(bf16x8 f, float& c) { const u32x4 u = __builtin_bit_cast(u32x4, f); const unsigned a = u[0], b = u[1], d = u[2], e = u[3]; c = dot_sq(e, dot_sq(d, dot_sq(b, dot_sq(a, c)))); }
DI float half_sum(float v) {
  auto b = __builtin_amdgcn_permlane32_swap(__float_as_uint(v), __float_as_uint(v), false, false);
  return __uint_as_float(b[0]) + __uint_as_float(b[1]);
}
DI float half_max(float v) {
  auto b = __builtin_amdgcn_permlane32_swap(__float_as_uint(v), __float_as_uint(v), false, false);
  return fmaxf(fmaxf(-3.0e38f, __uint_as_float(b[0])), __uint_as_float(b[1]));
}
DI float wave_sum(float v) {
  v = dadd<0xB1>(v); v = dadd<0x4E>(v); v = dadd<0x141>(v); v = dadd<0x140>(v);
  auto a = __builtin_amdgcn_permlane16_swap(__float_as_uint(v), __float_as_uint(v), false, false);
  v = __uint_as_float(a[0]) + __uint_as_float(a[1]);
  return half_sum(v);
}
DI void store16_sc1(void* p, u32x4 v) {
  asm volatile("global_store_dwordx4 %0, %1, off sc1\n\ts_nop 1" :: "v"(p), "v"(v) : "memory");
}
#define VMWAIT(N) asm volatile("s_waitcnt vmcnt(" #N ")" ::: "memory")
#define LBAR() do { asm volatile("" ::: "memory"); __builtin_amdgcn_s_waitcnt(0xC07F); __builtin_amdgcn_s_barrier(); asm volatile("" ::: "memory"); } while (0)
#define STEPBAR() do { if (DW) VMWAIT(8); LBAR(); } while (0)

DI void poll_flags(const u64* f, int nflags, u64 tag, int lane) {
  for (unsigned spins = 0; spins < (1u << 20); ++spins) {
    bool ok = true;
    for (int i = lane; i < nflags; i += 64)
      ok &= (__hip_atomic_load((GAS const u64*)(f + i), __ATOMIC_RELAXED, __HIP_MEMORY_SCOPE_AGENT) == tag);
    if (__all(ok)) break;
    __builtin_amdgcn_s_sleep(2);
  }
  asm volatile("" ::: "memory");
}
DI void flag_store(char* p, u64 tag) { __hip_atomic_store((GAS u64*)p, tag, __ATOMIC_RELAXED, __HIP_MEMORY_SCOPE_AGENT); }

#define TS()
#define STAMP(i)

struct Args {
  const __bf16* x; const __bf16* kc; const __bf16* vc; const __bf16* wq; const __bf16* wo; const __bf16* w1;
  const __bf16* b1; const __bf16* w2; const __bf16* b2; __bf16* out; char* ws;
};
struct Desc { const char* p[4]; unsigned lds[4]; unsigned v[4]; };

template <int OFF> DI void dma16p(const void* sbase, unsigned voff, unsigned lds) {
  asm volatile("s_add_u32 m0, %2, %3\n\ts_nop 0\n\tglobal_load_lds_dwordx4 %0, %1" PLAINPOL :: "v"(voff), "s"(sbase), "s"(__builtin_amdgcn_readfirstlane(lds)), "i"(OFF) : "memory", "scc");
}
template <int OFF> DI void dma16o(const void* sbase, unsigned voff, unsigned lds) {
  asm volatile("s_add_u32 m0, %2, %3\n\ts_nop 0\n\tglobal_load_lds_dwordx4 %0, %1 sc1" :: "v"(voff), "s"(sbase), "s"(__builtin_amdgcn_readfirstlane(lds)), "i"(OFF) : "memory", "scc");
}
DI void warm_line(const void* sbase, unsigned voff) {
  asm volatile("global_load_dword v255, %0, %1" :: "v"(voff), "s"(sbase) : "v255", "memory");
}

template <bool DW>
DI void body(const Args& A, const int w, const int l) {
  const __bf16* __restrict__ x = A.x; char* __restrict__ ws = A.ws;
  STAMP(0);
  TS();
  const int wd = w & 3;
  const int b = blockIdx.x, xcd = b & 7, idx = b >> 3;
  const int g = 2 * (xcd & 3) + (idx & 1), rb = 16 * (xcd >> 2) + (idx >> 1);
  const int r = l & 31, h = l >> 5, gq = (l >> 4) & 1, q = (l >> 2) & 3, p = l & 3;
  const unsigned l4 = l >> 4;

  const int xrow8 = 8 * w + (l >> 3);
  const int hrow = 64 * rb + 8 * g + 2 * wd + h;
  u32x4 xr[8], hr[2];
  u32x2 ep2;
  {
    const char* xb = (const char*)x + (size_t)(64 * rb + xrow8) * 1024 + (l & 7) * 16;
    asm volatile("global_load_dwordx4 %0, %9, off\n\tglobal_load_dwordx4 %1, %9, off offset:128\n\tglobal_load_dwordx4 %2, %9, off offset:256\n\t"
                 "global_load_dwordx4 %3, %9, off offset:384\n\tglobal_load_dwordx4 %4, %9, off offset:512\n\tglobal_load_dwordx4 %5, %9, off offset:640\n\t"
                 "global_load_dwordx4 %6, %9, off offset:768\n\tglobal_load_dwordx4 %7, %9, off offset:896\n\tglobal_load_dwordx2 %8, %10, off sc1"
                 : "=&v"(xr[0]), "=&v"(xr[1]), "=&v"(xr[2]), "=&v"(xr[3]), "=&v"(xr[4]), "=&v"(xr[5]), "=&v"(xr[6]), "=&v"(xr[7]), "=&v"(ep2)
                 : "v"(xb), "v"(ws + WS_EPOCH) : "memory");
    if (!DW) asm volatile("global_load_dwordx4 %0, %2, off\n\tglobal_load_dwordx4 %1, %2, off offset:512"
                 : "=&v"(hr[0]), "=&v"(hr[1]) : "v"((const char*)x + (size_t)hrow * 1024 + r * 16) : "memory");
  }
  const int rq = idx >> 1, rt = (idx >> 2) & 7, ro = 2 * (idx >> 2);
  const int pidx = ((idx >> 2) << 1) | (idx & 1);
  const bool do_g2 = (rb & 1) == 0, do_g1 = !do_g2 && pidx < 14;
  const int ft1 = 7 * (xcd >> 1) + (pidx >> 1), dp1 = 2 * (xcd & 1) + (pidx & 1);
  const int pp2 = 4 * (xcd >> 1) + (pidx >> 2), dt2 = 4 * (xcd & 1) + (pidx & 3);
  const int r1 = (16 * (pidx & 1) + 4 * (pidx >> 1) + (pidx >> 2)) & 31;
  const int r2 = (7 * (pidx & 3) + 7 * (pidx >> 2) / 4 + 2 * (pidx >> 2)) % 28;
  const unsigned wsw = (unsigned)((l ^ (wd << 2)) << 4);
  const unsigned gsw = (unsigned)(((l & 7) ^ (((l >> 4) & 1) << 2)) << 4);
  const unsigned grow = 8 * wd + (l >> 3);
  Desc DA;
  auto d_lds = [&](Desc& D, unsigned slot) { D.lds[0] = slot + wd * 1024; D.lds[1] = D.lds[0] + 8192; D.lds[2] = D.lds[0] + 16384; D.lds[3] = D.lds[0] + 24576; };
  auto d_ldsg = [&](Desc& D, int j) { const unsigned b0 = j < 3 ? j * 32768u : 24576u, st = j < 3 ? 8192u : 32768u;
                                      D.lds[0] = D.lds[1] = b0 + wd * 1024; D.lds[2] = D.lds[0] + st; D.lds[3] = D.lds[0] + 2 * st; };
  auto d_wq = [&](Desc& D, int s, unsigned slot) {
    D.p[0] = (const char*)A.wq + (size_t)(32 * ((s + rq) & 15)) * 8192 + g * 1024;
    D.p[1] = D.p[0] + 8 * 8192; D.p[2] = D.p[0] + 16 * 8192; D.p[3] = D.p[0] + 24 * 8192;
    D.v[0] = D.v[2] = wd * 8192u + wsw; D.v[1] = D.v[3] = D.v[0] + 4 * 8192; d_lds(D, slot);
  };
  auto d_wo = [&](Desc& D, int s, unsigned slot) {
    D.p[0] = (const char*)A.wo + (size_t)(512 * g + 32 * ((s + ro) & 15)) * 1024;
    D.p[1] = D.p[0] + 8192; D.p[2] = D.p[0] + 16384; D.p[3] = D.p[0] + 24576;
    D.v[0] = D.v[2] = wd * 1024u + wsw; D.v[1] = D.v[3] = D.v[0] + 4096; d_lds(D, slot);
  };
  auto d_kv = [&](Desc& D, int u, unsigned slot) {
    const size_t o = ((size_t)g * 2048 + 1536) * 256;
    D.p[0] = (const char*)A.kc + o + (size_t)((u + 1 + rt) & 7) * 16384; D.p[1] = D.p[0] + 8192;
    D.p[2] = (const char*)A.vc + o + (size_t)((u + rt) & 7) * 16384 + 8192;
    D.p[3] = (const char*)A.vc + o + (size_t)((u + 1 + rt) & 7) * 16384;
    D.v[0] = (4 * wd + l4) * 256 + (((l & 15) ^ (4 * wd + l4)) << 4); D.v[1] = D.v[0] + 4096;
    D.v[2] = (4 * wd + l4) * 256 + (((l & 15) ^ (l4 << 2)) << 4); D.v[3] = D.v[2] + 4096; d_lds(D, slot);
  };
  auto d_g1 = [&](Desc& D, int t, int j) {
    const int tt = (t + r1) & 31;
    D.p[0] = D.p[1] = (const char*)A.w1 + (size_t)(64 * tt) * 3584 + ft1 * 128;
    D.p[2] = ws + WS_H + (size_t)dp1 * 524288 + (size_t)(64 * tt) * 256; D.p[3] = D.p[2] + 128;
    D.v[0] = grow * 3584u + gsw; D.v[1] = D.v[0] + 32 * 3584; D.v[2] = grow * 256u + gsw; D.v[3] = D.v[2] + 32 * 256; d_ldsg(D, j);
  };
  auto d_g2 = [&](Desc& D, int t, int j) {
    int tt = t + r2; if (tt >= 28) tt -= 28;
    D.p[0] = D.p[1] = ws + WS_U + (size_t)dt2 * (224 * 1024) + (size_t)tt * 8192;
    D.p[2] = (const char*)A.w2 + (size_t)(64 * tt) * 4096 + pp2 * 256; D.p[3] = D.p[2] + 128;
    D.v[0] = wd * 1024u + l * 16u; D.v[1] = D.v[0] + 4096; D.v[2] = grow * 4096u + gsw; D.v[3] = D.v[2] + 32 * 4096; d_ldsg(D, j);
  };
#define DWX DW
#define DMX(D, i) do { if (DWX) dma16o<((i) & 1) * 4096>(D.p[(i) >> 1], D.v[((i) >> 2) * 2 + ((i) & 1)], D.lds[(i) >> 1]); } while (0)
#define DMA(i) do { if (DWX) dma16p<((i) & 1) * 4096>(DA.p[(i) >> 1], DA.v[((i) >> 2) * 2 + ((i) & 1)], DA.lds[(i) >> 1]); } while (0)
#define DMX_ALL(D) do { DMX(D, 0); DMX(D, 1); DMX(D, 2); DMX(D, 3); DMX(D, 4); DMX(D, 5); DMX(D, 6); DMX(D, 7); } while (0)
#define DMX_G(D) do { DMX(D, 0); DMX(D, 1); DMX(D, 4); DMX(D, 5); DMX(D, 6); DMX(D, 7); } while (0)
  auto nextslot = [](unsigned s) { return s >= 2 * SLOT ? 0u : s + SLOT; };
  auto next5 = [](unsigned s) { return (s == 2 * SLOT || s == 4 * SLOT) ? 0u : s + SLOT; };

  if (DW) { d_wq(DA, 0, 0); DMX_ALL(DA); d_wq(DA, 1, SLOT); }
#define XOPS "+v"(xr[0]), "+v"(xr[1]), "+v"(xr[2]), "+v"(xr[3]), "+v"(xr[4]), "+v"(xr[5]), "+v"(xr[6]), "+v"(xr[7]), "+v"(ep2)
  if (DWX) asm volatile("s_waitcnt vmcnt(8) ; REL" : XOPS :: "memory");
  else if (DW) asm volatile("s_waitcnt vmcnt(0)" : XOPS :: "memory");
  else asm volatile("s_waitcnt vmcnt(0)" : XOPS, "+v"(hr[0]), "+v"(hr[1]) :: "memory");
  const u64 tag = (((u64)__builtin_amdgcn_readfirstlane(ep2[1]) << 32) | (u64)__builtin_amdgcn_readfirstlane(ep2[0])) + 0x9E3779B97F4A7C15ull;
  const unsigned tagl = (unsigned)tag, tagh = (unsigned)(tag >> 32);

  {
    float sm = 0.f, sq = 0.f, sm2 = 0.f, sq2 = 0.f;
    #pragma unroll
    for (int i = 0; i < 8; ++i)
      #pragma unroll
      for (int j = 0; j < 4; j += 2) { sm = dot_one(xr[i][j], sm); sq = dot_sq(xr[i][j], sq); sm2 = dot_one(xr[i][j + 1], sm2); sq2 = dot_sq(xr[i][j + 1], sq2); }
    sm += sm2; sq += sq2;
    sm = dadd<0xB1>(sm); sq = dadd<0xB1>(sq); sm = dadd<0x4E>(sm); sq = dadd<0x4E>(sq); sm = dadd<0x141>(sm); sq = dadd<0x141>(sq);
    const float m = sm * (1.f / 512.f);
    const float var = fmaxf(sq * (1.f / 512.f) - m * m, 0.f);
    const float rs = __builtin_amdgcn_rsqf(var + 1e-5f);
    const float mb = -m * rs;
    const unsigned xw = XO + (l & 7) * 1024 + ((xrow8 ^ (l & 7)) << 4);
#define LNROW(i) do { u32x4 o; _Pragma("unroll") for (int j = 0; j < 4; ++j) o[j] = pk2(bflo(xr[i][j]) * rs + mb, bfhi(xr[i][j]) * rs + mb); \
      *(LDS3 u32x4*)(xw + (i) * 8192) = o; DMA(i); } while (0)
    LNROW(0); LNROW(1); LNROW(2); LNROW(3); LNROW(4); LNROW(5); LNROW(6); LNROW(7);
  }
  auto hpub = [&]() { if (!DW) {
    float sm = 0.f, sq = 0.f;
    #pragma unroll
    for (int i = 0; i < 2; ++i)
      #pragma unroll
      for (int j = 0; j < 4; ++j) { sm = dot_one(hr[i][j], sm); sq = dot_sq(hr[i][j], sq); }
    sm = dadd<0xB1>(sm); sq = dadd<0xB1>(sq); sm = dadd<0x4E>(sm); sq = dadd<0x4E>(sq);
    sm = dadd<0x141>(sm); sq = dadd<0x141>(sq); sm = dadd<0x140>(sm); sq = dadd<0x140>(sq);
    { auto a = __builtin_amdgcn_permlane16_swap(__float_as_uint(sm), __float_as_uint(sm), false, false); sm = __uint_as_float(a[0]) + __uint_as_float(a[1]);
      auto c = __builtin_amdgcn_permlane16_swap(__float_as_uint(sq), __float_as_uint(sq), false, false); sq = __uint_as_float(c[0]) + __uint_as_float(c[1]); }
    const float m = sm * (1.f / 512.f);
    const float var = fmaxf(sq * (1.f / 512.f) - m * m, 0.f);
    const float rs = __builtin_amdgcn_rsqf(var + 1e-5f);
    const float rs2 = rs * __builtin_amdgcn_rsqf(var * rs * rs + 1e-5f), mb2 = -m * rs2;
    char* hp = ws + WS_H + (size_t)(r >> 4) * 524288 + (size_t)hrow * 256 + (r & 15) * 16;
    #pragma unroll
    for (int i = 0; i < 2; ++i) {
      u32x4 o;
      #pragma unroll
      for (int j = 0; j < 4; ++j) o[j] = pk2(bflo(hr[i][j]) * rs2 + mb2, bfhi(hr[i][j]) * rs2 + mb2);
      store16_sc1(hp + i * 1048576, o);
    }
  } };
  STAMP(1);
  unsigned cur = 0;
  unsigned fre = 2 * SLOT + wd * 1024;

#define PMM(P, ks_, i_, j_) do { if (MM) acc[i_][j_] = mfma(fa[P][ks_][j_], fb[P][ks_][i_], acc[i_][j_]); SB(); } while (0)
#define PDM(FAST, i) do { if (FAST) { if (DWX) dma16p<(i) * 4096>(pb_, pv8[i], fre); } else DMX(DA, i); SB(); } while (0)
#define PSTEP(P, N, s_, MM_, FAST, MID) do { constexpr bool MM = MM_; STEPBAR(); PRIO(1); \
      PMM(P, 0, 1, 0); fb[N][0][1] = PLB(1, s_, 0); const char* pb_ = PBASE((s_) + 2); PX(P, 0); SB(); PDM(FAST, 0); \
      PMM(P, 0, 0, 0); fa[N][0][0] = PLA(0, 0); PX(P, 1); SB(); PDM(FAST, 1); \
      PMM(P, 0, 1, 1); fb[N][0][0] = PLB(0, s_, 0); fa[N][0][1] = PLA(1, 0); SB(); PDM(FAST, 2); \
      PMM(P, 0, 0, 1); fb[N][1][1] = PLB(1, s_, 1); PX(P, 2); SB(); PDM(FAST, 3); PRIO(0); \
      PMM(P, 1, 1, 0); fa[N][1][0] = PLA(0, 1); fb[N][1][0] = PLB(0, s_, 1); PX(P, 3); SB(); PDM(FAST, 4); \
      PMM(P, 1, 1, 1); fa[N][1][1] = PLA(1, 1); PX(P, 4); SB(); PDM(FAST, 5); \
      PMM(P, 1, 0, 0); PX(P, 5); PDM(FAST, 6); PDM(FAST, 7); MID; fre = cur + wd * 1024; cur = nextslot(cur); SB(); \
      PMM(P, 1, 0, 1); } while (0)
#define PZERO() do { _Pragma("unroll") for (int a_ = 0; a_ < 2; ++a_) _Pragma("unroll") for (int b_ = 0; b_ < 2; ++b_) { fa[1][a_][b_] = bf16x8{}; fb[1][a_][b_] = bf16x8{}; } } while (0)
#define PLOOP() do { PZERO(); \
    _Pragma("nounroll") for (int s = 0; s < 12; s += 2) { PSTEP(1, 0, s, true, true, auxmid(s)); PSTEP(0, 1, s + 1, true, true, (void)0); } \
    POLL_ISSUE; PSTEP(1, 0, 12, true, true, (void)0); \
    PSTEP(0, 1, 13, true, true, { POLL_CHECK; nextd(0); }); PSTEP(1, 0, 14, true, false, nextd(1)); PSTEP(0, 1, 15, true, false, nextd(2)); } while (0)

#define GMM(P, k_, j_) do { if (MM) acc[j_] = mfma(fa[P][k_][j_], fb[P][k_], acc[j_]); SB(); } while (0)
#define GSTEP(P, N, J, MM_, DD, MID) do { constexpr bool MM = MM_; \
    if (DW) { if ((DD) == 1) DMX_G(DA); if ((DD) == 2) DMX_ALL(DA); MID; } else { \
    asm volatile("" : "+v"(acc[0]), "+v"(acc[1])); \
    const unsigned ga_ = (J) < 3 ? gbase[(J) % 3] : 24576u, gb_ = (J) < 3 ? gbase[(J) % 3] + gsub8 : 24576u + gsub32; \
    GMM(P, 0, 0); GLB(N, 0); GLA(N, 0, 0); SB(); GMM(P, 0, 1); GLA(N, 0, 1); GLB(N, 1); SB(); \
    GMM(P, 1, 0); GLA(N, 1, 0); GLA(N, 1, 1); SB(); GMM(P, 1, 1); GLB(N, 2); GLA(N, 2, 0); SB(); \
    GMM(P, 2, 0); GLA(N, 2, 1); GLB(N, 3); SB(); GMM(P, 2, 1); GLA(N, 3, 0); GLA(N, 3, 1); SB(); \
    GMM(P, 3, 0); MID; SB(); GMM(P, 3, 1); } } while (0)
#define GLAST() do { if (!DW) { constexpr bool MM = true; GMM(1, 0, 0); GMM(1, 0, 1); GMM(1, 1, 0); GMM(1, 1, 1); GMM(1, 2, 0); GMM(1, 2, 1); GMM(1, 3, 0); GMM(1, 3, 1); } } while (0)
#define GZERO() do { _Pragma("unroll") for (int a_ = 0; a_ < 4; ++a_) { fa[1][a_][0] = bf16x8{}; fa[1][a_][1] = bf16x8{}; fb[1][a_] = bf16x8{}; } } while (0)
#define GBAR(N) do { if (DW) VMWAIT(N); LBAR(); } while (0)

  const int hh = w >> 1, rh = w & 1;
  bf16x8 qf[8];
  {
    f32x16 acc[2][2];
    #pragma unroll
    for (int i = 0; i < 2; ++i)
      #pragma unroll
      for (int j = 0; j < 2; ++j) acc[i][j] = f32x16{};
    unsigned aq[2], xrow[2];
    #pragma unroll
    for (int j = 0; j < 2; ++j) {
      aq[j] = (8 * h + q) * 1024 + (16 * hh + 4 * ((2 * rh + j) ^ q) + 2 * gq + (p & 1)) * 16 + (p >> 1) * 8;
      xrow[j] = (unsigned)(((32 * (rh ^ j) + r) ^ h) << 4) + h * 1024 + XO;
    }
    bf16x8 fa[2][2][2], fb[2][2][2];
    u32x4 pf0, pf1;
#define PLB(i_, s_, ks_) lds_b128((xrow[i_] ^ (unsigned)((4 * (((s_) + rq) & 1) + 2 * (ks_)) << 4)) + (4 * (((s_) + rq) & 15) + 2 * (ks_)) * 1024)
#define PX(P, n_) do { } while (0)
#define PLA(j_, ks_) lds_tr2(cur + aq[j_] + (16 * (ks_)) * 1024, cur + aq[j_] + (16 * (ks_) + 4) * 1024)
#define PBASE(s_) ((const char*)A.wq + (size_t)(((s_) + rq) & 15) * (32 * 8192) + g * 1024)
    unsigned pv8[8];
    #pragma unroll
    for (int i = 0; i < 8; ++i) pv8[i] = (4 * i + wd) * 8192u + wsw;
    auto auxmid = [&](int s) {
      if (!DW) { if (s == 2) VMWAIT(0); if (s == 4 && w == 4 && l == 0) flag_store(ws + WS_HFLAG + (rb * 8 + g) * 8, tag); }
    };
#define POLL_ISSUE do { if (!DW) asm volatile("global_load_dwordx4 %0, %2, off sc1\n\tglobal_load_dwordx4 %1, %2, off offset:16 sc1" \
                          : "=&v"(pf0), "=&v"(pf1) : "v"(ws + WS_HFLAG + l * 32) : "memory"); } while (0)
#define POLL_CHECK do { if (!DW) { \
      asm volatile("s_waitcnt vmcnt(0)" : "+v"(pf0), "+v"(pf1) :: "memory"); \
      const bool ok = pf0[0] == tagl && pf0[1] == tagh && pf0[2] == tagl && pf0[3] == tagh && pf1[0] == tagl && pf1[1] == tagh && pf1[2] == tagl && pf1[3] == tagh; \
      if (do_g1 && w == 4) { if (!__all(ok)) poll_flags((const u64*)(ws + WS_HFLAG), 256, tag, l); \
        if (b == 16 && l == 0) flag_store(ws + WS_EPOCH, tag); } } } while (0)
    auto nextd = [&](int n) { if (DW) { if (do_g1) d_g1(DA, n, (1 + n) % 3); else d_kv(DA, n - 1, ((1 + n) % 3) * SLOT); } };
    hpub();
    PLOOP();
#undef PLB
#undef PLA
#undef PBASE
#undef POLL_ISSUE
#undef POLL_CHECK
    { constexpr bool MM = true;
      PMM(1, 0, 1, 0); PMM(1, 0, 1, 1); PMM(1, 1, 1, 0); PMM(1, 1, 1, 1);
      LBAR();
      PMM(1, 0, 0, 0); PMM(1, 0, 0, 1);
      const unsigned xa = XO + w * 4096 + l * 16;
      *(LDS3 bf16x8*)(xa) = pack8<0>(acc[1][0], QSCALE); *(LDS3 bf16x8*)(xa + 1024) = pack8<1>(acc[1][0], QSCALE); SB();
      PMM(1, 1, 0, 0);
      *(LDS3 bf16x8*)(xa + 2048) = pack8<0>(acc[1][1], QSCALE); *(LDS3 bf16x8*)(xa + 3072) = pack8<1>(acc[1][1], QSCALE); SB();
      PMM(1, 1, 0, 1);
      STEPBAR();
      const unsigned xb2 = XO + (w ^ 1) * 4096 + l * 16;
      qf[4] = lds_b128(xb2); qf[5] = lds_b128(xb2 + 1024); qf[6] = lds_b128(xb2 + 2048); qf[7] = lds_b128(xb2 + 3072);
      qf[0] = pack8<0>(acc[0][0], QSCALE); qf[1] = pack8<1>(acc[0][0], QSCALE); qf[2] = pack8<0>(acc[0][1], QSCALE); qf[3] = pack8<1>(acc[0][1], QSCALE);
    }
#undef PX
    asm volatile("" : "+v"(qf[0]), "+v"(qf[1]), "+v"(qf[2]), "+v"(qf[3]), "+v"(qf[4]), "+v"(qf[5]), "+v"(qf[6]), "+v"(qf[7]));
  }
  STAMP(2);
  unsigned fill = 0;

  if (do_g1) {
    f32x16 acc[2] = {f32x16{}, f32x16{}};
    const unsigned tb = (8 * h + q) * 128 + (2 * gq + (p >> 1)) * 16 + (p & 1) * 8;
    const unsigned aa[2] = {tb + 64 * (q >> 1), tb + 64 * (1 ^ (q >> 1))};
    const unsigned ba = tb + 64 * ((wd & 1) ^ (q >> 1));
    const unsigned gbase[3] = {32768u, 65536u, 0u};
    const unsigned gsub8 = 8192u * (1 + (wd >> 1)), gsub32 = 32768u * (1 + (wd >> 1));
    bf16x8 fa[2][4][2], fb[2][4];
#define GLA(N, kk, j_) fa[N][kk][j_] = lds_tr2(ga_ + aa[j_] + (16 * (kk)) * 128, ga_ + aa[j_] + (16 * (kk) + 4) * 128)
#define GLB(N, kk) fb[N][kk] = lds_tr2(gb_ + ba + (16 * (kk)) * 128, gb_ + ba + (16 * (kk) + 4) * 128)
#define G1D(t_, j_) do { if (DW) d_g1(DA, t_, j_); } while (0)
    if (DW) { DMX_G(DA); d_g1(DA, 3, 3); }
    GZERO();
    _Pragma("nounroll") for (int t = 0; t < 28; t += 4) {
      GBAR(12); GSTEP(1, 0, 0, true, 1, G1D(t + 4, 1));
      GBAR(12); GSTEP(0, 1, 1, true, 1, G1D(t + 5, 2));
      GBAR(12); GSTEP(1, 0, 2, true, 1, G1D(t + 6, 0));
      GBAR(12); GSTEP(0, 1, 3, true, 1, G1D(t + 7, 3));
    }
    GBAR(12); GSTEP(1, 0, 0, true, 1, if (DW) d_kv(DA, -1, XO));
    GBAR(12); GSTEP(0, 1, 1, true, 2, if (DW) d_kv(DA, 0, XO + SLOT));
    GBAR(14); GSTEP(1, 0, 2, true, 2, if (DW) d_kv(DA, 1, 0));
    constexpr int f2 = DW ? 1 : 0;
    u32x2 bb[4];
    asm volatile("global_load_dwordx2 %0, %4, off\n\tglobal_load_dwordx2 %1, %4, off offset:16\n\tglobal_load_dwordx2 %2, %4, off offset:32\n\tglobal_load_dwordx2 %3, %4, off offset:48"
                 : "=&v"(bb[0]), "=&v"(bb[1]), "=&v"(bb[2]), "=&v"(bb[3]) : "v"(A.b1 + 64 * ft1 + 32 * f2 + 4 * h) : "memory");
    GBAR(20); GSTEP(0, 1, 3, true, 0, (void)0);
    if (!DW) {
      constexpr bool MM = true;
      GMM(1, 0, 1); GMM(1, 1, 1); GMM(1, 2, 1); GMM(1, 3, 1); GMM(1, 0, 0); GMM(1, 1, 0);
      #pragma unroll
      for (int m = 0; m < 4; ++m) {
        const f32x4 v4 = {acc[1][4 * m], acc[1][4 * m + 1], acc[1][4 * m + 2], acc[1][4 * m + 3]};
        *(LDS3 f32x4*)(SLOT + wd * 4096 + m * 1024 + l * 16) = v4;
      }
      SB(); GMM(1, 2, 0); GMM(1, 3, 0);
    }
    asm volatile("s_waitcnt vmcnt(0)" : "+v"(bb[0]), "+v"(bb[1]), "+v"(bb[2]), "+v"(bb[3]) :: "memory");
    LBAR();
#undef GLA
#undef GLB
    if (DW) {
      #pragma unroll
      for (int m = 0; m < 4; ++m) {
        const f32x4 v4 = *(LDS3 f32x4*)(SLOT + wd * 4096 + m * 1024 + l * 16);
        acc[0][4 * m] = v4[0]; acc[0][4 * m + 1] = v4[1]; acc[0][4 * m + 2] = v4[2]; acc[0][4 * m + 3] = v4[3];
      }
    }
    #pragma unroll
    for (int m = 0; m < 4; ++m) {
      acc[0][4 * m] += bflo(bb[m][0]); acc[0][4 * m + 1] += bfhi(bb[m][0]); acc[0][4 * m + 2] += bflo(bb[m][1]); acc[0][4 * m + 3] += bfhi(bb[m][1]);
    }
    #pragma unroll
    for (int e = 0; e < 16; ++e) {
      const float u = acc[0][e];
      const float z = u * (1.f + 0.044715f * u * u) * (-2.f * 0.7978845608028654f * 1.4426950408889634f);
      acc[0][e] = u * __builtin_amdgcn_rcpf(1.f + __builtin_amdgcn_exp2f(z));
    }
    char* up = ws + WS_U + (size_t)(2 * dp1 + (wd >> 1)) * (224 * 1024) + (size_t)(ft1 * 8 + f2 * 4 + h) * 1024 + (32 * (wd & 1) + r) * 16;
    store16_sc1(up, __builtin_bit_cast(u32x4, pack8n<0>(acc[0])));
    store16_sc1(up + 2048, __builtin_bit_cast(u32x4, pack8n<1>(acc[0])));
    cur = XO;
  }
  STAMP(3);

  {
    f32x16 o[4];
    f32x16 negm, sA, sB;
    float lsum = 0.f;
    unsigned ka[8];
    #pragma unroll
    for (int kk = 0; kk < 8; ++kk) ka[kk] = (r * 256 + ((h ^ (r & 15)) << 4)) ^ (unsigned)((kk ^ (4 * rh)) << 5);
    unsigned va[4];
    #pragma unroll
    for (int d = 0; d < 4; ++d) va[d] = 16384 + (4 * h + q) * 256 + (4 * (d ^ q) + 2 * gq + (p >> 1)) * 16 + (p & 1) * 8;
    bf16x8 kf[8], vf[8];
    u32x4 pw0 = {}, pw1 = {};
    float mx;
    unsigned kc_[8], vc_[4];
#define SETADDR() do { _Pragma("unroll") for (int i_ = 0; i_ < 8; ++i_) { kc_[i_] = cur + ka[i_]; } _Pragma("unroll") for (int i_ = 0; i_ < 4; ++i_) { vc_[i_] = cur + va[i_]; } } while (0)
#define LDK(i, KB) kf[i] = lds_b128(kc_[i] + (KB) * 8192)
#define LDV(i, VP) vf[i] = lds_tr2(vc_[(i) & 3] + ((VP) * 32 + 16 * ((i) >> 2)) * 256, vc_[(i) & 3] + ((VP) * 32 + 16 * ((i) >> 2) + 8) * 256)
#define EXP(Y, e) do { Y[e] = __builtin_amdgcn_exp2f(Y[e]); } while (0)
#define ADD(Y, e) do { lsum += Y[e]; } while (0)
#define CVT(P, Y, c) do { P[(c) & 3] = pk2(Y[2 * (c)], Y[2 * (c) + 1]); } while (0)
#define MX(X, e) do { mx = fmaxf(fmaxf(mx, X[2 * (e)]), X[2 * (e) + 1]); } while (0)
#define QK(X, i) do { X = mfma(kf[i], qf[i], (i) == 0 ? negm : X); } while (0)
#define PV(i) do { o[(i) & 3] = mfma(vf[i], __builtin_bit_cast(bf16x8, (i) < 4 ? pw0 : pw1), o[(i) & 3]); } while (0)
#define DMAC(C, i) do { if (C) DMA(i); } while (0)
#define STAGE_A(X, Y, KB, VP, DC) do { \
      QK(X, 0); LDK(6, KB); EXP(Y, 0); EXP(Y, 1); DMAC(DC, 3); SB(); \
      QK(X, 1); LDK(7, KB); EXP(Y, 2); CVT(pw0, Y, 0); DMAC(DC, 4); SB(); \
      QK(X, 2); LDV(0, VP); EXP(Y, 3); EXP(Y, 4); DMAC(DC, 5); SB(); \
      QK(X, 3); LDV(1, VP); EXP(Y, 5); ADD(Y, 0); CVT(pw0, Y, 1); DMAC(DC, 6); SB(); \
      QK(X, 4); LDV(2, VP); EXP(Y, 6); EXP(Y, 7); CVT(pw0, Y, 2); DMAC(DC, 7); SB(); \
      QK(X, 5); LDV(3, VP); EXP(Y, 8); ADD(Y, 1); ADD(Y, 2); CVT(pw0, Y, 3); SB(); \
      QK(X, 6); LDV(4, VP); EXP(Y, 9); EXP(Y, 10); ADD(Y, 3); SB(); \
      QK(X, 7); LDV(5, VP); EXP(Y, 11); ADD(Y, 4); CVT(pw1, Y, 4); SB(); \
    } while (0)
#define RESCALE(X) do { \
      if (__builtin_expect(__any(mx > THR), 0)) { \
        const float delta = fmaxf(mx, 0.f); \
        const float alpha = __builtin_amdgcn_exp2f(-delta); \
        _Pragma("unroll") for (int e = 0; e < 16; ++e) { X[e] -= delta; negm[e] -= delta; } \
        _Pragma("unroll") for (int d = 0; d < 4; ++d) _Pragma("unroll") for (int e = 0; e < 16; ++e) o[d][e] *= alpha; \
        lsum *= alpha; \
      } } while (0)
#define STAGE_B(X, Y, VP, KBN) do { \
      PV(0); LDV(6, VP); EXP(Y, 12); EXP(Y, 13); CVT(pw1, Y, 5); SB(); \
      PV(1); LDV(7, VP); EXP(Y, 14); EXP(Y, 15); ADD(Y, 5); SB(); \
      PV(2); LDK(0, KBN); CVT(pw1, Y, 6); CVT(pw1, Y, 7); ADD(Y, 6); mx = -3.0e38f; MX(X, 0); SB(); \
      PV(3); LDK(1, KBN); MX(X, 1); MX(X, 2); ADD(Y, 7); ADD(Y, 8); SB(); \
      PV(4); LDK(2, KBN); MX(X, 3); MX(X, 4); ADD(Y, 9); ADD(Y, 10); SB(); \
      PV(5); LDK(3, KBN); MX(X, 5); MX(X, 6); ADD(Y, 11); ADD(Y, 12); SB(); \
      PV(6); LDK(4, KBN); MX(X, 7); ADD(Y, 13); ADD(Y, 14); SB(); \
      PV(7); LDK(5, KBN); mx = half_max(mx); ADD(Y, 15); SB(); \
      RESCALE(X); SB(); \
    } while (0)
#define STAGE_B_BAR(X, Y, VP, WAITS, SETD, POST) do { \
      PV(0); LDV(6, VP); EXP(Y, 12); EXP(Y, 13); CVT(pw1, Y, 5); SB(); \
      PV(1); LDV(7, VP); EXP(Y, 14); EXP(Y, 15); ADD(Y, 5); SB(); \
      PV(2); CVT(pw1, Y, 6); CVT(pw1, Y, 7); ADD(Y, 6); mx = -3.0e38f; MX(X, 0); SB(); \
      cur = next5(cur); \
      PV(3); SETADDR(); SETD; SB(); \
      PV(4); SB(); \
      WAITS; LBAR(); PRIO(3); \
      PV(5); SB(); LDK(0, 0); LDK(1, 0); LDK(2, 0); LDK(3, 0); MX(X, 1); MX(X, 2); MX(X, 3); MX(X, 4); ADD(Y, 7); ADD(Y, 8); ADD(Y, 9); POST; DMA(0); SB(); \
      PV(6); LDK(4, 0); MX(X, 5); MX(X, 6); MX(X, 7); ADD(Y, 10); ADD(Y, 11); ADD(Y, 12); DMA(1); SB(); \
      PV(7); LDK(5, 0); mx = half_max(mx); ADD(Y, 13); ADD(Y, 14); ADD(Y, 15); DMA(2); SB(); \
      RESCALE(X); SB(); \
    } while (0)

    SETADDR();
    #pragma unroll
    for (int i = 0; i < 8; ++i) LDK(i, 0);
    negm = f32x16{};
    #pragma unroll
    for (int d = 0; d < 4; ++d) o[d] = f32x16{};
    SB();
    QK(sA, 0); LDK(0, 1); DMA(0); SB(); QK(sA, 1); LDK(1, 1); DMA(1); SB(); QK(sA, 2); LDK(2, 1); DMA(2); SB(); QK(sA, 3); LDK(3, 1); DMA(3); SB();
    QK(sA, 4); LDK(4, 1); DMA(4); SB(); QK(sA, 5); LDK(5, 1); DMA(5); SB(); QK(sA, 6); DMA(6); SB(); QK(sA, 7); DMA(7); SB();
    mx = -3.0e38f;
    #pragma unroll
    for (int e = 0; e < 8; ++e) MX(sA, e);
    mx = half_max(mx);
    #pragma unroll
    for (int e = 0; e < 16; ++e) { sA[e] -= mx; negm[e] = -mx; }
    SB();
    STAGE_A(sB, sA, 1, 1, false);
    fill = SLOT;
    STAGE_B_BAR(sB, sA, 1, VMWAIT(0), if (DW) d_kv(DA, 2, fill),
                if (!DW && do_g1 && w == 4 && l == 0) flag_store(ws + WS_UFLAG + (dp1 * 28 + ft1) * 8, tag));
    _Pragma("nounroll") for (int u = 0; u < 7; ++u) {
      STAGE_A(sA, sB, 0, 0, true);
      PRIO(2);
      STAGE_B(sA, sB, 0, 1);
      PRIO(1);
      STAGE_A(sB, sA, 1, 1, false);
      PRIO(0);
      fill = nextslot(fill);
      STAGE_B_BAR(sB, sA, 1, if (DW) VMWAIT(8), if (DW) { if (u + 3 < 8) d_kv(DA, u + 3, fill); else d_wo(DA, u - 5, fill); }, (void)0);
    }
    PRIO(0);
    #pragma unroll
    for (int i = 0; i < 8; ++i) LDV(i, 0);
    DMA(3); DMA(4);
    #pragma unroll
    for (int e = 0; e < 16; ++e) { EXP(sB, e); ADD(sB, e); }
    #pragma unroll
    for (int c = 0; c < 4; ++c) { CVT(pw0, sB, c); CVT(pw1, sB, 4 + c); }
    SB();
    fre = cur + wd * 1024; cur = nextslot(cur);
    const float inv = __builtin_amdgcn_rcpf(half_sum(lsum));
    const unsigned ob = XO + (hh * 16 + h) * 1024 + (32 * rh + r) * 16;
#define OWR(d) do { *(LDS3 bf16x8*)(ob + (4 * (d)) * 1024) = pack8<0>(o[d], inv); *(LDS3 bf16x8*)(ob + (4 * (d) + 2) * 1024) = pack8<1>(o[d], inv); } while (0)
    PV(0); PV(4); DMA(5); SB(); PV(1); PV(5); DMA(6); SB(); PV(2); OWR(0); SB(); PV(6); DMA(7); SB(); PV(3); OWR(1); SB(); PV(7); SB(); OWR(2); OWR(3);
  }
  STAMP(4);

  {
    f32x16 acc[2][2];
    #pragma unroll
    for (int i = 0; i < 2; ++i)
      #pragma unroll
      for (int j = 0; j < 2; ++j) acc[i][j] = f32x16{};
    unsigned oa[2];
    #pragma unroll
    for (int j = 0; j < 2; ++j) oa[j] = (4 * h + q) * 1024 + (16 * (w >> 1) + 4 * ((2 * (w & 1) + j) ^ q) + 2 * gq + (p >> 1)) * 16 + (p & 1) * 8;
    const unsigned ob = XO + h * 1024 + r * 16;
    bf16x8 fa[2][2][2], fb[2][2][2];
    u32x2 pfl;
#define PLB(i_, s_, ks_) lds_b128(ob + (4 * (((s_) + ro) & 15) + 2 * (ks_)) * 1024 + 512 * (i_))
#define PLA(j_, ks_) lds_tr2(cur + oa[j_] + (16 * (ks_)) * 1024, cur + oa[j_] + (16 * (ks_) + 8) * 1024)
#define PBASE(s_) ((const char*)A.wo + (size_t)(16 * g + (((s_) + ro) & 15)) * 32768)
    unsigned pv8[8];
    #pragma unroll
    for (int i = 0; i < 8; ++i) pv8[i] = (4 * i + wd) * 1024u + wsw;
    auto auxmid = [&](int) {};
#define PX(P, n_) do { } while (0)
#define POLL_ISSUE do { if (!DW) asm volatile("global_load_dwordx2 %0, %1, off sc1" : "=&v"(pfl) : "v"(ws + WS_UFLAG + ((dt2 >> 1) * 28 + (l < 28 ? l : 0)) * 8) : "memory"); } while (0)
#define POLL_CHECK do { if (!DW) { asm volatile("s_waitcnt vmcnt(0)" : "+v"(pfl) :: "memory"); \
      if (do_g2 && w == 4 && !__all(pfl[0] == tagl && pfl[1] == tagh)) poll_flags((const u64*)(ws + WS_UFLAG) + (dt2 >> 1) * 28, 28, tag, l); } } while (0)
    auto nextd = [&](int n) { if (DW) { if (do_g2) d_g2(DA, n, (2 + n) % 3); else d_wo(DA, n, ((2 + n) % 3) * SLOT); } };
    PLOOP();
#undef PLB
#undef PLA
#undef PBASE
#undef POLL_ISSUE
#undef POLL_CHECK
    char* pp = ws + WS_PART + ((size_t)(rb * 8 + w) * 8 + g) * 8192 + l * 16;
    { constexpr bool MM = true;
      PMM(1, 0, 0, 0); PMM(1, 1, 0, 0); PMM(1, 0, 0, 1); PMM(1, 1, 0, 1); PMM(1, 0, 1, 0);
      store16_sc1(pp, __builtin_bit_cast(u32x4, pack8n<0>(acc[0][0]))); store16_sc1(pp + 4096, __builtin_bit_cast(u32x4, pack8n<1>(acc[0][0]))); SB();
      PMM(1, 1, 1, 0);
      store16_sc1(pp + 1024, __builtin_bit_cast(u32x4, pack8n<0>(acc[0][1]))); store16_sc1(pp + 5120, __builtin_bit_cast(u32x4, pack8n<1>(acc[0][1]))); SB();
      PMM(1, 0, 1, 1); PMM(1, 1, 1, 1);
      store16_sc1(pp + 2048, __builtin_bit_cast(u32x4, pack8n<0>(acc[1][0]))); store16_sc1(pp + 6144, __builtin_bit_cast(u32x4, pack8n<1>(acc[1][0])));
      store16_sc1(pp + 3072, __builtin_bit_cast(u32x4, pack8n<0>(acc[1][1]))); store16_sc1(pp + 7168, __builtin_bit_cast(u32x4, pack8n<1>(acc[1][1])));
    }
    STAMP(5);
    VMWAIT(0);
    if (l == 0) flag_store(ws + WS_SFLAG + ((rb * 8 + w) * 8 + g) * 8, tag);
    if (do_g2) LBAR();
  }
  STAMP(6);

  if (do_g2) {
    const int wi = (w >> 1) & 1, wj = w & 1, sb2 = DW ? 1 : 0;
    f32x16 acc[2] = {f32x16{}, f32x16{}};
    const unsigned aa = h * 1024 + r * 16;
    const unsigned ba = (4 * h + q) * 128 + (4 * ((wd & 1) ^ (q >> 1)) + 2 * gq + (p >> 1)) * 16 + (p & 1) * 8;
    const unsigned gbase[3] = {65536u, 0u, 32768u};
    const unsigned gsub8 = 8192u * (1 + (wd >> 1)), gsub32 = 32768u * (1 + (wd >> 1));
    bf16x8 fa[2][4][2], fb[2][4];
    const int srow = 64 * (2 * pp2 + sb2) + 32 * wi + r;
    const size_t xo = (size_t)srow * 512 + 64 * dt2 + 32 * wj + 8 * h;
    const char* pp = ws + WS_PART + (size_t)((2 * pp2 + sb2) * 8 + dt2) * 65536 + (wi * 2 + wj) * 1024 + l * 16;
    const u64* sfl = (const u64*)(ws + WS_SFLAG) + ((2 * pp2 + sb2) * 8 + dt2) * 8;
    u32x4 pv[16];
    u32x2 pfl;
#define GLA(N, kk, j_) fa[N][kk][j_] = lds_b128(ga_ + aa + 2 * (kk) * 1024 + 512 * (j_))
#define GLB(N, kk) fb[N][kk] = lds_tr2(gb_ + ba + (16 * (kk)) * 128, gb_ + ba + (16 * (kk) + 8) * 128)
#define PLD(c) asm volatile("global_load_dwordx4 %0, %1, off sc1" : "=v"(pv[c]) : "v"(pp + ((c) >> 1) * 8192 + ((c) & 1) * 4096) : "memory")
#define G2D(t_, j_, AUX) do { if (DW) d_g2(DA, t_, j_); else { AUX; } } while (0)
    if (DW) { DMX_G(DA); d_g2(DA, 3, 3); }
    GZERO();
    _Pragma("nounroll") for (int t = 0; t < 12; t += 4) {
      GBAR(12); GSTEP(1, 0, 0, true, 1, G2D(t + 4, 2, (void)0));
      GBAR(12); GSTEP(0, 1, 1, true, 1, G2D(t + 5, 0, (void)0));
      GBAR(12); GSTEP(1, 0, 2, true, 1, G2D(t + 6, 1, (void)0));
      GBAR(12); GSTEP(0, 1, 3, true, 1, G2D(t + 7, 3, (void)0));
    }
    GBAR(12); GSTEP(1, 0, 0, true, 1, G2D(16, 2, asm volatile("global_load_dwordx2 %0, %1, off sc1" : "=&v"(pfl) : "v"(sfl + (l & 7)) : "memory")));
    GBAR(12); GSTEP(0, 1, 1, true, 1, G2D(17, 0, (void)0));
    GBAR(12); GSTEP(1, 0, 2, true, 1, G2D(18, 1, (void)0));
    GBAR(12); GSTEP(0, 1, 3, true, 1, G2D(19, 3, { asm volatile("s_waitcnt vmcnt(0)" : "+v"(pfl) :: "memory");
                                                  if (!__all(pfl[0] == tagl && pfl[1] == tagh)) poll_flags(sfl, 8, tag, l); PLD(0); PLD(1); }));
    GBAR(12); GSTEP(1, 0, 0, true, 1, G2D(20, 2, { PLD(2); PLD(3); }));
    GBAR(12); GSTEP(0, 1, 1, true, 1, G2D(21, 0, { PLD(4); PLD(5); }));
    GBAR(12); GSTEP(1, 0, 2, true, 1, G2D(22, 1, { PLD(6); PLD(7); }));
    GBAR(12); GSTEP(0, 1, 3, true, 1, G2D(23, 3, { PLD(8); PLD(9); }));
    GBAR(12); GSTEP(1, 0, 0, true, 1, G2D(24, 2, { PLD(10); PLD(11); }));
    GBAR(12); GSTEP(0, 1, 1, true, 1, G2D(25, 0, { PLD(12); PLD(13); }));
    GBAR(12); GSTEP(1, 0, 2, true, 1, G2D(26, 1, { PLD(14); PLD(15); }));
    GBAR(12); GSTEP(0, 1, 3, true, 1, G2D(27, 3, (void)0));
    GBAR(12); GSTEP(1, 0, 0, true, 1, (void)0);
    u32x4 xv0, xv1; unsigned b2r;
    asm volatile("global_load_dwordx4 %0, %3, off\n\tglobal_load_dwordx4 %1, %3, off offset:32\n\tglobal_load_ushort %2, %4, off"
                 : "=&v"(xv0), "=&v"(xv1), "=&v"(b2r) : "v"(x + xo), "v"(A.b2 + srow) : "memory");
    GBAR(15); GSTEP(0, 1, 1, true, 0, (void)0);
    GBAR(9); GSTEP(1, 0, 2, true, 0, (void)0);
    GBAR(3); GSTEP(0, 1, 3, true, 0, (void)0);
    GLAST();
#undef GLA
#undef GLB
    if (!DW) {
      #pragma unroll
      for (int dh = 0; dh < 2; ++dh)
        #pragma unroll
        for (int m = 0; m < 4; ++m) {
          const f32x4 v4 = {acc[dh][4 * m], acc[dh][4 * m + 1], acc[dh][4 * m + 2], acc[dh][4 * m + 3]};
          *(LDS3 f32x4*)(XO + (wd * 2 + dh) * 4096 + m * 1024 + l * 16) = v4;
        }
    }
    LBAR();
    f32x16 yv;
    #pragma unroll
    for (int m = 0; m < 4; ++m) {
      const f32x4 v4 = *(LDS3 f32x4*)(XO + ((sb2 * 2 + wi) * 2 + wj) * 4096 + m * 1024 + l * 16);
      yv[4 * m] = v4[0]; yv[4 * m + 1] = v4[1]; yv[4 * m + 2] = v4[2]; yv[4 * m + 3] = v4[3];
    }
    STAMP(7);
    asm volatile("s_waitcnt vmcnt(0)" : "+v"(xv0), "+v"(xv1), "+v"(b2r) :: "memory");
    if (!DW) asm volatile("" : "+v"(pv[0]), "+v"(pv[1]), "+v"(pv[2]), "+v"(pv[3]), "+v"(pv[4]), "+v"(pv[5]), "+v"(pv[6]), "+v"(pv[7]),
                 "+v"(pv[8]), "+v"(pv[9]), "+v"(pv[10]), "+v"(pv[11]), "+v"(pv[12]), "+v"(pv[13]), "+v"(pv[14]), "+v"(pv[15]) :: "memory");
    if (DW) {
      unsigned done = 0;
      const char* pb = ws + WS_PART + (size_t)((2 * pp2 + 1) * 8 + dt2) * 65536 + (wi * 2 + wj) * 1024;
      for (unsigned spins = 0; done != 0xffu && spins < (1u << 20); ++spins) {
        const u64 f = __hip_atomic_load((GAS const u64*)(sfl + (l & 7)), __ATOMIC_RELAXED, __HIP_MEMORY_SCOPE_AGENT);
        unsigned nw = (unsigned)__ballot(f == tag) & 0xffu & ~done;
        done |= nw;
        if (!nw) __builtin_amdgcn_s_sleep(1);
        while (nw) {
          const int gg = __builtin_ctz(nw); nw &= nw - 1;
          dma16o<0>(pb + gg * 8192, l * 16u, wd * 16384 + gg * 2048);
          dma16o<1024>(pb + gg * 8192 + 4096, l * 16u, wd * 16384 + gg * 2048);
        }
      }
    }
    const float bias2 = bflo(b2r);
    #pragma unroll
    for (int e = 0; e < 16; ++e) {
      const float yy = yv[e] + bias2;
      yv[e] = yy * __builtin_amdgcn_rcpf(1.f + __builtin_amdgcn_exp2f(-1.4426950408889634f * yy));
    }
    if (DW) {
      VMWAIT(0);
      #pragma unroll
      for (int c = 0; c < 16; ++c) pv[c] = *(LDS3 u32x4*)(wd * 16384 + c * 1024 + l * 16);
    }
    #pragma unroll
    for (int c = 0; c < 2; ++c) {
      float t[8];
      #pragma unroll
      for (int e = 0; e < 8; ++e) t[e] = 0.f;
      #pragma unroll
      for (int gg = 0; gg < 8; ++gg)
        #pragma unroll
        for (int e = 0; e < 4; ++e) { t[2 * e] += bflo(pv[2 * gg + c][e]); t[2 * e + 1] += bfhi(pv[2 * gg + c][e]); }
      #pragma unroll
      for (int e = 0; e < 8; ++e) t[e] *= yv[8 * c + e];
      #pragma unroll
      for (int e = 0; e < 4; ++e) {
        auto rr = __builtin_amdgcn_permlane32_swap(__float_as_uint(t[e]), __float_as_uint(t[4 + e]), false, false);
        t[e] = __uint_as_float(rr[0]); t[4 + e] = __uint_as_float(rr[1]);
      }
      const u32x4 xv = c ? xv1 : xv0;
      u32x4 ov;
      #pragma unroll
      for (int e = 0; e < 4; ++e) ov[e] = pk2(t[2 * e] + bflo(xv[e]), t[2 * e + 1] + bfhi(xv[e]));
      *(u32x4*)(A.out + xo + 16 * c) = ov;
    }
  } else { STAMP(7); }
  STAMP(8);
  TS();
}

__global__ __launch_bounds__(512, 2) void fused_kernel(Args A) {
  extern __shared__ __attribute__((aligned(16))) char lds_raw[];
  const int l = threadIdx.x & 63;
  const int w = __builtin_amdgcn_readfirstlane(threadIdx.x >> 6);
  if (w < 4) body<true>(A, w, l); else body<false>(A, w, l);
}

extern "C" void kernel_launch(void* const* d_in, const int* in_sizes, int n_in, void* d_out, int out_size, void* d_ws, size_t ws_size, hipStream_t stream) {
  (void)hipFuncSetAttribute((const void*)fused_kernel, hipFuncAttributeMaxDynamicSharedMemorySize, LDS_BYTES);
  Args A;
  A.x = (const __bf16*)d_in[0]; A.kc = (const __bf16*)d_in[1]; A.vc = (const __bf16*)d_in[2]; A.wq = (const __bf16*)d_in[3]; A.wo = (const __bf16*)d_in[4];
  A.w1 = (const __bf16*)d_in[6]; A.b1 = (const __bf16*)d_in[7]; A.w2 = (const __bf16*)d_in[8]; A.b2 = (const __bf16*)d_in[9];
  A.out = (__bf16*)d_out; A.ws = (char*)d_ws;
  fused_kernel<<<256, 512, LDS_BYTES, stream>>>(A);
}
```

```cpp
#include <hip/hip_runtime.h>
#include <stdint.h>
#pragma clang diagnostic ignored "-Wint-to-pointer-cast"

typedef __bf16 bf16x8 __attribute__((ext_vector_type(8)));
typedef __bf16 bf16x4 __attribute__((ext_vector_type(4)));
typedef __bf16 bf16x2 __attribute__((ext_vector_type(2)));
typedef float f32x16 __attribute__((ext_vector_type(16)));
typedef float f32x4 __attribute__((ext_vector_type(4)));
typedef float f32x2 __attribute__((ext_vector_type(2)));
typedef unsigned u32x4 __attribute__((ext_vector_type(4)));
typedef unsigned u32x2 __attribute__((ext_vector_type(2)));
typedef unsigned long long u64;
#define LDS3 __attribute__((address_space(3)))
#define GAS __attribute__((address_space(1)))
#define DI __device__ __forceinline__
#define SB() __builtin_amdgcn_sched_barrier(0)
#define PRIO(n) __builtin_amdgcn_s_setprio(n)

constexpr int SLOT = 32768;
constexpr int XO = 98304;
constexpr int LDS_BYTES = 163840;
constexpr size_t WS_EPOCH = 0;
constexpr size_t WS_HFLAG = 4096;
constexpr size_t WS_UFLAG = 8192;
constexpr size_t WS_SFLAG = 16384;
constexpr size_t WS_H     = 1u << 20;
constexpr size_t WS_U     = 4u << 20;
constexpr size_t WS_PART  = 8u << 20;

constexpr float THR = 16.0f;
constexpr float QSCALE = 0.08838834764831845f * 1.4426950408889634f;

DI bf16x8 lds_b128(unsigned a) { return *(LDS3 bf16x8*)a; }
DI bf16x8 lds_tr2(unsigned a0, unsigned a1) {
  bf16x4 lo = __builtin_amdgcn_ds_read_tr16_b64_v4bf16((LDS3 bf16x4*)a0);
  bf16x4 hi = __builtin_amdgcn_ds_read_tr16_b64_v4bf16((LDS3 bf16x4*)a1);
  return __builtin_shufflevector(lo, hi, 0, 1, 2, 3, 4, 5, 6, 7);
}
DI f32x16 mfma(bf16x8 a, bf16x8 b, f32x16 c) { return __builtin_amdgcn_mfma_f32_32x32x16_bf16(a, b, c, 0, 0, 0); }
DI unsigned pk2(float lo, float hi) { f32x2 v = {lo, hi}; bf16x2 b = __builtin_convertvector(v, bf16x2); return __builtin_bit_cast(unsigned, b); }
template <int S> DI bf16x8 pack8(const f32x16& x, float sc) {
  u32x4 p;
  p[0] = pk2(x[8 * S + 0] * sc, x[8 * S + 1] * sc); p[1] = pk2(x[8 * S + 2] * sc, x[8 * S + 3] * sc);
  p[2] = pk2(x[8 * S + 4] * sc, x[8 * S + 5] * sc); p[3] = pk2(x[8 * S + 6] * sc, x[8 * S + 7] * sc);
  return __builtin_bit_cast(bf16x8, p);
}
template <int S> DI bf16x8 pack8n(const f32x16& x) {
  u32x4 p;
  p[0] = pk2(x[8 * S + 0], x[8 * S + 1]); p[1] = pk2(x[8 * S + 2], x[8 * S + 3]);
  p[2] = pk2(x[8 * S + 4], x[8 * S + 5]); p[3] = pk2(x[8 * S + 6], x[8 * S + 7]);
  return __builtin_bit_cast(bf16x8, p);
}
DI float bflo(unsigned u) { return __uint_as_float(u << 16); }
DI float bfhi(unsigned u) { return __uint_as_float(u & 0xffff0000u); }
template <int C> DI float dadd(float v) { return v + __builtin_bit_cast(float, __builtin_amdgcn_update_dpp(0, __builtin_bit_cast(int, v), C, 0xf, 0xf, true)); }
DI float dot_one(unsigned u, float c) { return __builtin_amdgcn_fdot2_f32_bf16(__builtin_bit_cast(bf16x2, u), __builtin_bit_cast(bf16x2, 0x3f803f80u), c, false); }
DI float dot_sq(unsigned u, float c) { return __builtin_amdgcn_fdot2_f32_bf16(__builtin_bit_cast(bf16x2, u), __builtin_bit_cast(bf16x2, u), c, false); }
DI float half_sum(float v) {
  auto b = __builtin_amdgcn_permlane32_swap(__float_as_uint(v), __float_as_uint(v), false, false);
  return __uint_as_float(b[0]) + __uint_as_float(b[1]);
}
DI float half_max(float v) {
  auto b = __builtin_amdgcn_permlane32_swap(__float_as_uint(v), __float_as_uint(v), false, false);
  return fmaxf(fmaxf(-3.0e38f, __uint_as_float(b[0])), __uint_as_float(b[1]));
}
DI void store16_sc1(void* p, u32x4 v) {
  asm volatile("global_store_dwordx4 %0, %1, off sc1\n\ts_nop 1" :: "v"(p), "v"(v) : "memory");
}
#define VMWAIT(N) asm volatile("s_waitcnt vmcnt(" #N ")" ::: "memory")
#define LBAR() do { asm volatile("" ::: "memory"); __builtin_amdgcn_s_waitcnt(0xC07F); __builtin_amdgcn_s_barrier(); asm volatile("" ::: "memory"); } while (0)
#define STEPBAR() do { if (DW) VMWAIT(8); LBAR(); } while (0)

DI void poll_flags(const u64* f, int nflags, u64 tag, int lane) {
  for (unsigned spins = 0; spins < (1u << 20); ++spins) {
    bool ok = true;
    for (int i = lane; i < nflags; i += 64)
      ok &= (__hip_atomic_load((GAS const u64*)(f + i), __ATOMIC_RELAXED, __HIP_MEMORY_SCOPE_AGENT) == tag);
    if (__all(ok)) break;
    __builtin_amdgcn_s_sleep(2);
  }
  asm volatile("" ::: "memory");
}
DI void flag_store(char* p, u64 tag) { __hip_atomic_store((GAS u64*)p, tag, __ATOMIC_RELAXED, __HIP_MEMORY_SCOPE_AGENT); }


struct Args {
  const __bf16* x; const __bf16* kc; const __bf16* vc; const __bf16* wq; const __bf16* wo; const __bf16* w1;
  const __bf16* b1; const __bf16* w2; const __bf16* b2; __bf16* out; char* ws;
};
struct Desc { const char* p[4]; unsigned lds[4]; unsigned v[4]; };

template <int OFF> DI void dma16p(const void* sbase, unsigned voff, unsigned lds) {
  asm volatile("s_add_u32 m0, %2, %3\n\ts_nop 0\n\tglobal_load_lds_dwordx4 %0, %1" :: "v"(voff), "s"(sbase), "s"(__builtin_amdgcn_readfirstlane(lds)), "i"(OFF) : "memory", "scc");
}
template <int OFF> DI void dma16o(const void* sbase, unsigned voff, unsigned lds) {
  asm volatile("s_add_u32 m0, %2, %3\n\ts_nop 0\n\tglobal_load_lds_dwordx4 %0, %1 sc1" :: "v"(voff), "s"(sbase), "s"(__builtin_amdgcn_readfirstlane(lds)), "i"(OFF) : "memory", "scc");
}
template <bool DW>
DI void body(const Args& A, const int w, const int l) {
  const __bf16* __restrict__ x = A.x; char* __restrict__ ws = A.ws;
  const int wd = w & 3;
  const int b = blockIdx.x, xcd = b & 7, idx = b >> 3;
  const int pidx = ((idx >> 2) << 1) | (idx & 1);
  const int g = 2 * (xcd & 3) + (idx & 1), rb = 16 * (xcd >> 2) + (idx >> 1);
  const int r = l & 31, h = l >> 5, gq = (l >> 4) & 1, q = (l >> 2) & 3, p = l & 3;
  const unsigned l4 = l >> 4;

  const int xrow8 = 8 * w + (l >> 3);
  const int hrow = 64 * rb + 8 * g + 2 * wd + h;
  u32x4 xr[8], hr[2];
  u32x2 ep2;
  {
    const char* xb = (const char*)x + (size_t)(64 * rb + xrow8) * 1024 + (l & 7) * 16;
    asm volatile("global_load_dwordx4 %0, %9, off\n\tglobal_load_dwordx4 %1, %9, off offset:128\n\tglobal_load_dwordx4 %2, %9, off offset:256\n\t"
                 "global_load_dwordx4 %3, %9, off offset:384\n\tglobal_load_dwordx4 %4, %9, off offset:512\n\tglobal_load_dwordx4 %5, %9, off offset:640\n\t"
                 "global_load_dwordx4 %6, %9, off offset:768\n\tglobal_load_dwordx4 %7, %9, off offset:896\n\tglobal_load_dwordx2 %8, %10, off sc1"
                 : "=&v"(xr[0]), "=&v"(xr[1]), "=&v"(xr[2]), "=&v"(xr[3]), "=&v"(xr[4]), "=&v"(xr[5]), "=&v"(xr[6]), "=&v"(xr[7]), "=&v"(ep2)
                 : "v"(xb), "v"(ws + WS_EPOCH) : "memory");
    if (!DW) asm volatile("global_load_dwordx4 %0, %2, off\n\tglobal_load_dwordx4 %1, %2, off offset:512"
                 : "=&v"(hr[0]), "=&v"(hr[1]) : "v"((const char*)x + (size_t)hrow * 1024 + r * 16) : "memory");
  }
  const int rq = rb & 15, rt = pidx >> 1, ro = pidx & 14;
  const bool do_g2 = (rb & 1) == 0, do_g1 = !do_g2 && pidx < 14;
  const int ft1 = 7 * (xcd >> 1) + (pidx >> 1), dp1 = 2 * (xcd & 1) + (pidx & 1);
  const int pp2 = 4 * (xcd >> 1) + (pidx >> 2), dt2 = 4 * (xcd & 1) + (pidx & 3);
  const int r1 = (16 * (pidx & 1) + 4 * (pidx >> 1) + (pidx >> 2)) & 31;
  const int r2 = (7 * (pidx & 3) + 7 * (pidx >> 2) / 4 + 2 * (pidx >> 2)) % 28;
  const unsigned wsw = (unsigned)((l ^ (wd << 2)) << 4);
  const unsigned gsw = (unsigned)(((l & 7) ^ (((l >> 4) & 1) << 2)) << 4);
  const unsigned grow = 8 * wd + (l >> 3);
  Desc DA;
  auto d_lds = [&](Desc& D, unsigned slot) { D.lds[0] = slot + wd * 1024; D.lds[1] = D.lds[0] + 8192; D.lds[2] = D.lds[0] + 16384; D.lds[3] = D.lds[0] + 24576; };
  auto d_ldsg = [&](Desc& D, int j) { const unsigned b0 = j < 3 ? j * 32768u : 24576u, st = j < 3 ? 8192u : 32768u;
                                      D.lds[0] = D.lds[1] = b0 + wd * 1024; D.lds[2] = D.lds[0] + st; D.lds[3] = D.lds[0] + 2 * st; };
  auto d_wq = [&](Desc& D, int s, unsigned slot) {
    D.p[0] = (const char*)A.wq + (size_t)(32 * ((s + rq) & 15)) * 8192 + g * 1024;
    D.p[1] = D.p[0] + 8 * 8192; D.p[2] = D.p[0] + 16 * 8192; D.p[3] = D.p[0] + 24 * 8192;
    D.v[0] = D.v[2] = wd * 8192u + wsw; D.v[1] = D.v[3] = D.v[0] + 4 * 8192; d_lds(D, slot);
  };
  auto d_wo = [&](Desc& D, int s, unsigned slot) {
    D.p[0] = (const char*)A.wo + (size_t)(512 * g + 32 * ((s + ro) & 15)) * 1024;
    D.p[1] = D.p[0] + 8192; D.p[2] = D.p[0] + 16384; D.p[3] = D.p[0] + 24576;
    D.v[0] = D.v[2] = wd * 1024u + wsw; D.v[1] = D.v[3] = D.v[0] + 4096; d_lds(D, slot);
  };
  auto d_kv = [&](Desc& D, int u, unsigned slot) {
    const size_t o = ((size_t)g * 2048 + 1536) * 256;
    D.p[0] = (const char*)A.kc + o + (size_t)((u + 1 + rt) & 7) * 16384; D.p[1] = D.p[0] + 8192;
    D.p[2] = (const char*)A.vc + o + (size_t)((u + rt) & 7) * 16384 + 8192;
    D.p[3] = (const char*)A.vc + o + (size_t)((u + 1 + rt) & 7) * 16384;
    D.v[0] = (4 * wd + l4) * 256 + (((l & 15) ^ (4 * wd + l4)) << 4); D.v[1] = D.v[0] + 4096;
    D.v[2] = (4 * wd + l4) * 256 + (((l & 15) ^ (l4 << 2)) << 4); D.v[3] = D.v[2] + 4096; d_lds(D, slot);
  };
  auto d_g1 = [&](Desc& D, int t, int j) {
    const int tt = (t + r1) & 31;
    D.p[0] = D.p[1] = (const char*)A.w1 + (size_t)(64 * tt) * 3584 + ft1 * 128;
    D.p[2] = ws + WS_H + (size_t)dp1 * 524288 + (size_t)(64 * tt) * 256; D.p[3] = D.p[2] + 128;
    D.v[0] = grow * 3584u + gsw; D.v[1] = D.v[0] + 32 * 3584; D.v[2] = grow * 256u + gsw; D.v[3] = D.v[2] + 32 * 256; d_ldsg(D, j);
  };
  auto d_g2 = [&](Desc& D, int t, int j) {
    int tt = t + r2; if (tt >= 28) tt -= 28;
    D.p[0] = D.p[1] = ws + WS_U + (size_t)dt2 * (224 * 1024) + (size_t)tt * 8192;
    D.p[2] = (const char*)A.w2 + (size_t)(64 * tt) * 4096 + pp2 * 256; D.p[3] = D.p[2] + 128;
    D.v[0] = wd * 1024u + l * 16u; D.v[1] = D.v[0] + 4096; D.v[2] = grow * 4096u + gsw; D.v[3] = D.v[2] + 32 * 4096; d_ldsg(D, j);
  };
#define DMX(D, i) do { if (DW) dma16o<((i) & 1) * 4096>(D.p[(i) >> 1], D.v[((i) >> 2) * 2 + ((i) & 1)], D.lds[(i) >> 1]); } while (0)
#define DMA(i) do { if (DW) dma16p<((i) & 1) * 4096>(DA.p[(i) >> 1], DA.v[((i) >> 2) * 2 + ((i) & 1)], DA.lds[(i) >> 1]); } while (0)
#define DMX_ALL(D) do { DMX(D, 0); DMX(D, 1); DMX(D, 2); DMX(D, 3); DMX(D, 4); DMX(D, 5); DMX(D, 6); DMX(D, 7); } while (0)
#define DMX_G(D) do { DMX(D, 0); DMX(D, 1); DMX(D, 4); DMX(D, 5); DMX(D, 6); DMX(D, 7); } while (0)
  auto nextslot = [](unsigned s) { return s >= 2 * SLOT ? 0u : s + SLOT; };
  auto next5 = [](unsigned s) { return (s == 2 * SLOT || s == 4 * SLOT) ? 0u : s + SLOT; };

  if (DW) { d_wq(DA, 0, 0); DMX_ALL(DA); d_wq(DA, 1, SLOT); }
#define XOPS "+v"(xr[0]), "+v"(xr[1]), "+v"(xr[2]), "+v"(xr[3]), "+v"(xr[4]), "+v"(xr[5]), "+v"(xr[6]), "+v"(xr[7]), "+v"(ep2)
  if (DW) asm volatile("s_waitcnt vmcnt(8) ; REL" : XOPS :: "memory");
  else asm volatile("s_waitcnt vmcnt(0)" : XOPS, "+v"(hr[0]), "+v"(hr[1]) :: "memory");
  const u64 tag = (((u64)(unsigned)__builtin_amdgcn_readfirstlane(ep2[1]) << 32) | (u64)(unsigned)__builtin_amdgcn_readfirstlane(ep2[0])) + 0x9E3779B97F4A7C15ull;
  const unsigned tagl = (unsigned)tag, tagh = (unsigned)(tag >> 32);

  {
    float sm = 0.f, sq = 0.f, sm2 = 0.f, sq2 = 0.f;
    #pragma unroll
    for (int i = 0; i < 8; ++i)
      #pragma unroll
      for (int j = 0; j < 4; j += 2) { sm = dot_one(xr[i][j], sm); sq = dot_sq(xr[i][j], sq); sm2 = dot_one(xr[i][j + 1], sm2); sq2 = dot_sq(xr[i][j + 1], sq2); }
    sm += sm2; sq += sq2;
    sm = dadd<0xB1>(sm); sq = dadd<0xB1>(sq); sm = dadd<0x4E>(sm); sq = dadd<0x4E>(sq); sm = dadd<0x141>(sm); sq = dadd<0x141>(sq);
    const float m = sm * (1.f / 512.f);
    const float var = fmaxf(sq * (1.f / 512.f) - m * m, 0.f);
    const float rs = __builtin_amdgcn_rsqf(var + 1e-5f);
    const float mb = -m * rs;
    const unsigned xw = XO + (l & 7) * 1024 + ((xrow8 ^ (l & 7)) << 4);
#define LNROW(i) do { u32x4 o; _Pragma("unroll") for (int j = 0; j < 4; ++j) o[j] = pk2(bflo(xr[i][j]) * rs + mb, bfhi(xr[i][j]) * rs + mb); \
      *(LDS3 u32x4*)(xw + (i) * 8192) = o; DMA(i); } while (0)
    LNROW(0); LNROW(1); LNROW(2); LNROW(3); LNROW(4); LNROW(5); LNROW(6); LNROW(7);
  }
  auto hpub = [&]() { if (!DW) {
    float sm = 0.f, sq = 0.f;
    #pragma unroll
    for (int i = 0; i < 2; ++i)
      #pragma unroll
      for (int j = 0; j < 4; ++j) { sm = dot_one(hr[i][j], sm); sq = dot_sq(hr[i][j], sq); }
    sm = dadd<0xB1>(sm); sq = dadd<0xB1>(sq); sm = dadd<0x4E>(sm); sq = dadd<0x4E>(sq);
    sm = dadd<0x141>(sm); sq = dadd<0x141>(sq); sm = dadd<0x140>(sm); sq = dadd<0x140>(sq);
    { auto a = __builtin_amdgcn_permlane16_swap(__float_as_uint(sm), __float_as_uint(sm), false, false); sm = __uint_as_float(a[0]) + __uint_as_float(a[1]);
      auto c = __builtin_amdgcn_permlane16_swap(__float_as_uint(sq), __float_as_uint(sq), false, false); sq = __uint_as_float(c[0]) + __uint_as_float(c[1]); }
    const float m = sm * (1.f / 512.f);
    const float var = fmaxf(sq * (1.f / 512.f) - m * m, 0.f);
    const float rs = __builtin_amdgcn_rsqf(var + 1e-5f);
    const float rs2 = rs * __builtin_amdgcn_rsqf(var * rs * rs + 1e-5f), mb2 = -m * rs2;
    char* hp = ws + WS_H + (size_t)(r >> 4) * 524288 + (size_t)hrow * 256 + (r & 15) * 16;
    #pragma unroll
    for (int i = 0; i < 2; ++i) {
      u32x4 o;
      #pragma unroll
      for (int j = 0; j < 4; ++j) o[j] = pk2(bflo(hr[i][j]) * rs2 + mb2, bfhi(hr[i][j]) * rs2 + mb2);
      store16_sc1(hp + i * 1048576, o);
    }
  } };
  unsigned cur = 0;
  unsigned fre = 2 * SLOT + wd * 1024;

#define PMM(P, ks_, i_, j_) do { if (MM) acc[i_][j_] = mfma(fa[P][ks_][j_], fb[P][ks_][i_], acc[i_][j_]); SB(); } while (0)
#define PDM(FAST, i) do { if (FAST) { if (DW) dma16p<(i) * 4096>(pb_, pv8[i], fre); } else DMX(DA, i); SB(); } while (0)
#define PSTEP(P, N, s_, MM_, FAST, MID) do { constexpr bool MM = MM_; STEPBAR(); PRIO(1); \
      PMM(P, 0, 1, 0); fb[N][0][1] = PLB(1, s_, 0); const char* pb_ = PBASE((s_) + 2); PX(P, 0); SB(); PDM(FAST, 0); \
      PMM(P, 0, 0, 0); fa[N][0][0] = PLA(0, 0); PX(P, 1); SB(); PDM(FAST, 1); \
      PMM(P, 0, 1, 1); fb[N][0][0] = PLB(0, s_, 0); fa[N][0][1] = PLA(1, 0); SB(); PDM(FAST, 2); \
      PMM(P, 0, 0, 1); fb[N][1][1] = PLB(1, s_, 1); PX(P, 2); SB(); PDM(FAST, 3); PRIO(0); \
      PMM(P, 1, 1, 0); fa[N][1][0] = PLA(0, 1); fb[N][1][0] = PLB(0, s_, 1); PX(P, 3); SB(); PDM(FAST, 4); \
      PMM(P, 1, 1, 1); fa[N][1][1] = PLA(1, 1); PX(P, 4); SB(); PDM(FAST, 5); \
      PMM(P, 1, 0, 0); PX(P, 5); PDM(FAST, 6); PDM(FAST, 7); MID; fre = cur + wd * 1024; cur = nextslot(cur); SB(); \
      PMM(P, 1, 0, 1); } while (0)
#define PLOOP(MID0) do { PSTEP(1, 0, 0, false, true, MID0); \
    for (int s = 1; s < 11; s += 2) { PSTEP(0, 1, s, true, true, auxmid(s)); PSTEP(1, 0, s + 1, true, true, (void)0); } \
    PSTEP(0, 1, 11, true, true, POLL_ISSUE); PSTEP(1, 0, 12, true, true, (void)0); \
    PSTEP(0, 1, 13, true, true, { POLL_CHECK; nextd(0); }); PSTEP(1, 0, 14, true, false, nextd(1)); PSTEP(0, 1, 15, true, false, nextd(2)); } while (0)

#define GMM(P, k_, j_) do { if (MM) acc[j_] = mfma(fa[P][k_][j_], fb[P][k_], acc[j_]); SB(); } while (0)
#define GSTEP(P, N, J, MM_, DD, MID) do { constexpr bool MM = MM_; \
    if (DW) { if ((DD) == 1) DMX_G(DA); if ((DD) == 2) DMX_ALL(DA); MID; } else { \
    asm volatile("" : "+v"(acc[0]), "+v"(acc[1])); \
    const unsigned ga_ = (J) < 3 ? gbase[(J) % 3] : 24576u, gb_ = (J) < 3 ? gbase[(J) % 3] + gsub8 : 24576u + gsub32; \
    GMM(P, 0, 0); GLB(N, 0); GLA(N, 0, 0); SB(); GMM(P, 0, 1); GLA(N, 0, 1); GLB(N, 1); SB(); \
    GMM(P, 1, 0); GLA(N, 1, 0); GLA(N, 1, 1); SB(); GMM(P, 1, 1); GLB(N, 2); GLA(N, 2, 0); SB(); \
    GMM(P, 2, 0); GLA(N, 2, 1); GLB(N, 3); SB(); GMM(P, 2, 1); GLA(N, 3, 0); GLA(N, 3, 1); SB(); \
    GMM(P, 3, 0); MID; SB(); GMM(P, 3, 1); } } while (0)
#define GLAST() do { if (!DW) { constexpr bool MM = true; GMM(1, 0, 0); GMM(1, 0, 1); GMM(1, 1, 0); GMM(1, 1, 1); GMM(1, 2, 0); GMM(1, 2, 1); GMM(1, 3, 0); GMM(1, 3, 1); } } while (0)
#define GBAR(N) do { if (DW) VMWAIT(N); LBAR(); } while (0)

  const int hh = w >> 1, rh = w & 1;
  bf16x8 qf[8];
  {
    f32x16 acc[2][2];
    #pragma unroll
    for (int i = 0; i < 2; ++i)
      #pragma unroll
      for (int j = 0; j < 2; ++j) acc[i][j] = f32x16{};
    unsigned aq[2], xrow[2];
    #pragma unroll
    for (int j = 0; j < 2; ++j) {
      aq[j] = (8 * h + q) * 1024 + (16 * hh + 4 * ((2 * rh + j) ^ q) + 2 * gq + (p & 1)) * 16 + (p >> 1) * 8;
      xrow[j] = (unsigned)(((32 * (rh ^ j) + r) ^ h) << 4) + h * 1024 + XO;
    }
    bf16x8 fa[2][2][2], fb[2][2][2];
    u32x4 pf0, pf1;
#define PLB(i_, s_, ks_) lds_b128((xrow[i_] ^ (unsigned)((4 * (((s_) + rq) & 1) + 2 * (ks_)) << 4)) + (4 * (((s_) + rq) & 15) + 2 * (ks_)) * 1024)
#define PX(P, n_) do { } while (0)
#define PLA(j_, ks_) lds_tr2(cur + aq[j_] + (16 * (ks_)) * 1024, cur + aq[j_] + (16 * (ks_) + 4) * 1024)
#define PBASE(s_) ((const char*)A.wq + (size_t)(((s_) + rq) & 15) * (32 * 8192) + g * 1024)
    unsigned pv8[8];
    #pragma unroll
    for (int i = 0; i < 8; ++i) pv8[i] = (4 * i + wd) * 8192u + wsw;
    auto auxmid = [&](int s) {
      if (!DW) { if (s == 3) VMWAIT(0); if (s == 5 && w == 4 && l == 0) flag_store(ws + WS_HFLAG + (rb * 8 + g) * 8, tag); }
    };
#define POLL_ISSUE do { if (!DW) asm volatile("global_load_dwordx4 %0, %2, off sc1\n\tglobal_load_dwordx4 %1, %2, off offset:16 sc1" \
                          : "=&v"(pf0), "=&v"(pf1) : "v"(ws + WS_HFLAG + l * 32) : "memory"); } while (0)
#define POLL_CHECK do { if (!DW) { \
      asm volatile("s_waitcnt vmcnt(0)" : "+v"(pf0), "+v"(pf1) :: "memory"); \
      const bool ok = pf0[0] == tagl && pf0[1] == tagh && pf0[2] == tagl && pf0[3] == tagh && pf1[0] == tagl && pf1[1] == tagh && pf1[2] == tagl && pf1[3] == tagh; \
      if (do_g1 && w == 4) { if (!__all(ok)) poll_flags((const u64*)(ws + WS_HFLAG), 256, tag, l); \
        if (b == 16 && l == 0) flag_store(ws + WS_EPOCH, tag); } } } while (0)
    auto nextd = [&](int n) { if (DW) { if (do_g1) d_g1(DA, n, (1 + n) % 3); else d_kv(DA, n - 1, ((1 + n) % 3) * SLOT); } };
    PLOOP(hpub());
#undef PLB
#undef PLA
#undef PBASE
#undef POLL_ISSUE
#undef POLL_CHECK
    { constexpr bool MM = true;
      PMM(1, 0, 1, 0); PMM(1, 0, 1, 1); PMM(1, 1, 1, 0); PMM(1, 1, 1, 1);
      LBAR();
      PMM(1, 0, 0, 0); PMM(1, 0, 0, 1);
      const unsigned xa = XO + w * 4096 + l * 16;
      *(LDS3 bf16x8*)(xa) = pack8<0>(acc[1][0], QSCALE); *(LDS3 bf16x8*)(xa + 1024) = pack8<1>(acc[1][0], QSCALE); SB();
      PMM(1, 1, 0, 0);
      *(LDS3 bf16x8*)(xa + 2048) = pack8<0>(acc[1][1], QSCALE); *(LDS3 bf16x8*)(xa + 3072) = pack8<1>(acc[1][1], QSCALE); SB();
      PMM(1, 1, 0, 1);
      STEPBAR();
      const unsigned xb2 = XO + (w ^ 1) * 4096 + l * 16;
      qf[4] = lds_b128(xb2); qf[5] = lds_b128(xb2 + 1024); qf[6] = lds_b128(xb2 + 2048); qf[7] = lds_b128(xb2 + 3072);
      qf[0] = pack8<0>(acc[0][0], QSCALE); qf[1] = pack8<1>(acc[0][0], QSCALE); qf[2] = pack8<0>(acc[0][1], QSCALE); qf[3] = pack8<1>(acc[0][1], QSCALE);
    }
#undef PX
    asm volatile("" : "+v"(qf[0]), "+v"(qf[1]), "+v"(qf[2]), "+v"(qf[3]), "+v"(qf[4]), "+v"(qf[5]), "+v"(qf[6]), "+v"(qf[7]));
  }
  unsigned fill = 0;

  if (do_g1) {
    f32x16 acc[2] = {f32x16{}, f32x16{}};
    const unsigned tb = (8 * h + q) * 128 + (2 * gq + (p >> 1)) * 16 + (p & 1) * 8;
    const unsigned aa[2] = {tb + 64 * (q >> 1), tb + 64 * (1 ^ (q >> 1))};
    const unsigned ba = tb + 64 * ((wd & 1) ^ (q >> 1));
    const unsigned gbase[3] = {32768u, 65536u, 0u};
    const unsigned gsub8 = 8192u * (1 + (wd >> 1)), gsub32 = 32768u * (1 + (wd >> 1));
    bf16x8 fa[2][4][2], fb[2][4];
#define GLA(N, kk, j_) fa[N][kk][j_] = lds_tr2(ga_ + aa[j_] + (16 * (kk)) * 128, ga_ + aa[j_] + (16 * (kk) + 4) * 128)
#define GLB(N, kk) fb[N][kk] = lds_tr2(gb_ + ba + (16 * (kk)) * 128, gb_ + ba + (16 * (kk) + 4) * 128)
#define G1D(t_, j_) do { if (DW) d_g1(DA, t_, j_); } while (0)
    if (DW) { DMX_G(DA); d_g1(DA, 3, 3); }
    GSTEP(1, 0, 0, false, 1, G1D(4, 1));
    GBAR(12); GSTEP(0, 1, 1, true, 1, G1D(5, 2));
    GBAR(12); GSTEP(1, 0, 2, true, 1, G1D(6, 0));
    GBAR(12); GSTEP(0, 1, 3, true, 1, G1D(7, 3));
    for (int t = 4; t < 28; t += 4) {
      GBAR(12); GSTEP(1, 0, 0, true, 1, G1D(t + 4, 1));
      GBAR(12); GSTEP(0, 1, 1, true, 1, G1D(t + 5, 2));
      GBAR(12); GSTEP(1, 0, 2, true, 1, G1D(t + 6, 0));
      GBAR(12); GSTEP(0, 1, 3, true, 1, G1D(t + 7, 3));
    }
    GBAR(12); GSTEP(1, 0, 0, true, 1, if (DW) d_kv(DA, -1, XO));
    GBAR(12); GSTEP(0, 1, 1, true, 2, if (DW) d_kv(DA, 0, XO + SLOT));
    GBAR(14); GSTEP(1, 0, 2, true, 2, if (DW) d_kv(DA, 1, 0));
    constexpr int f2 = DW ? 1 : 0;
    u32x2 bb[4];
    asm volatile("global_load_dwordx2 %0, %4, off\n\tglobal_load_dwordx2 %1, %4, off offset:16\n\tglobal_load_dwordx2 %2, %4, off offset:32\n\tglobal_load_dwordx2 %3, %4, off offset:48"
                 : "=&v"(bb[0]), "=&v"(bb[1]), "=&v"(bb[2]), "=&v"(bb[3]) : "v"(A.b1 + 64 * ft1 + 32 * f2 + 4 * h) : "memory");
    GBAR(20); GSTEP(0, 1, 3, true, 0, (void)0);
    if (!DW) {
      constexpr bool MM = true;
      GMM(1, 0, 1); GMM(1, 1, 1); GMM(1, 2, 1); GMM(1, 3, 1); GMM(1, 0, 0); GMM(1, 1, 0);
      #pragma unroll
      for (int m = 0; m < 4; ++m) {
        const f32x4 v4 = {acc[1][4 * m], acc[1][4 * m + 1], acc[1][4 * m + 2], acc[1][4 * m + 3]};
        *(LDS3 f32x4*)(SLOT + wd * 4096 + m * 1024 + l * 16) = v4;
      }
      SB(); GMM(1, 2, 0); GMM(1, 3, 0);
    }
    asm volatile("s_waitcnt vmcnt(0)" : "+v"(bb[0]), "+v"(bb[1]), "+v"(bb[2]), "+v"(bb[3]) :: "memory");
    LBAR();
#undef GLA
#undef GLB
    if (DW) {
      #pragma unroll
      for (int m = 0; m < 4; ++m) {
        const f32x4 v4 = *(LDS3 f32x4*)(SLOT + wd * 4096 + m * 1024 + l * 16);
        acc[0][4 * m] = v4[0]; acc[0][4 * m + 1] = v4[1]; acc[0][4 * m + 2] = v4[2]; acc[0][4 * m + 3] = v4[3];
      }
    }
    #pragma unroll
    for (int m = 0; m < 4; ++m) {
      acc[0][4 * m] += bflo(bb[m][0]); acc[0][4 * m + 1] += bfhi(bb[m][0]); acc[0][4 * m + 2] += bflo(bb[m][1]); acc[0][4 * m + 3] += bfhi(bb[m][1]);
    }
    #pragma unroll
    for (int e = 0; e < 16; ++e) {
      const float u = acc[0][e];
      const float z = u * (1.f + 0.044715f * u * u) * (-2.f * 0.7978845608028654f * 1.4426950408889634f);
      acc[0][e] = u * __builtin_amdgcn_rcpf(1.f + __builtin_amdgcn_exp2f(z));
    }
    char* up = ws + WS_U + (size_t)(2 * dp1 + (wd >> 1)) * (224 * 1024) + (size_t)(ft1 * 8 + f2 * 4 + h) * 1024 + (32 * (wd & 1) + r) * 16;
    store16_sc1(up, __builtin_bit_cast(u32x4, pack8n<0>(acc[0])));
    store16_sc1(up + 2048, __builtin_bit_cast(u32x4, pack8n<1>(acc[0])));
    cur = XO;
  }

  {
    f32x16 o[4];
    f32x16 negm, sA, sB;
    float lsum = 0.f;
    unsigned ka[8];
    #pragma unroll
    for (int kk = 0; kk < 8; ++kk) ka[kk] = (r * 256 + ((h ^ (r & 15)) << 4)) ^ (unsigned)((kk ^ (4 * rh)) << 5);
    unsigned va[4];
    #pragma unroll
    for (int d = 0; d < 4; ++d) va[d] = 16384 + (4 * h + q) * 256 + (4 * (d ^ q) + 2 * gq + (p >> 1)) * 16 + (p & 1) * 8;
    bf16x8 kf[8], vf[8];
    u32x4 pw0 = {}, pw1 = {};
    float mx;
    unsigned kc_[8], vc_[4];
#define SETADDR() do { _Pragma("unroll") for (int i_ = 0; i_ < 8; ++i_) { kc_[i_] = cur + ka[i_]; } _Pragma("unroll") for (int i_ = 0; i_ < 4; ++i_) { vc_[i_] = cur + va[i_]; } } while (0)
#define LDK(i, KB) kf[i] = lds_b128(kc_[i] + (KB) * 8192)
#define LDV(i, VP) vf[i] = lds_tr2(vc_[(i) & 3] + ((VP) * 32 + 16 * ((i) >> 2)) * 256, vc_[(i) & 3] + ((VP) * 32 + 16 * ((i) >> 2) + 8) * 256)
#define EXP(Y, e) do { Y[e] = __builtin_amdgcn_exp2f(Y[e]); } while (0)
#define ADD(Y, e) do { lsum += Y[e]; } while (0)
#define CVT(P, Y, c) do { P[(c) & 3] = pk2(Y[2 * (c)], Y[2 * (c) + 1]); } while (0)
#define MX(X, e) do { mx = fmaxf(fmaxf(mx, X[2 * (e)]), X[2 * (e) + 1]); } while (0)
#define QK(X, i) do { X = mfma(kf[i], qf[i], (i) == 0 ? negm : X); } while (0)
#define PV(i) do { o[(i) & 3] = mfma(vf[i], __builtin_bit_cast(bf16x8, (i) < 4 ? pw0 : pw1), o[(i) & 3]); } while (0)
#define DMAC(C, i) do { if (C) DMA(i); } while (0)
#define STAGE_A(X, Y, KB, VP, DC) do { \
      QK(X, 0); LDK(6, KB); EXP(Y, 0); EXP(Y, 1); DMAC(DC, 3); SB(); \
      QK(X, 1); LDK(7, KB); EXP(Y, 2); CVT(pw0, Y, 0); DMAC(DC, 4); SB(); \
      QK(X, 2); LDV(0, VP); EXP(Y, 3); EXP(Y, 4); DMAC(DC, 5); SB(); \
      QK(X, 3); LDV(1, VP); EXP(Y, 5); ADD(Y, 0); CVT(pw0, Y, 1); DMAC(DC, 6); SB(); \
      QK(X, 4); LDV(2, VP); EXP(Y, 6); EXP(Y, 7); CVT(pw0, Y, 2); DMAC(DC, 7); SB(); \
      QK(X, 5); LDV(3, VP); EXP(Y, 8); ADD(Y, 1); ADD(Y, 2); CVT(pw0, Y, 3); SB(); \
      QK(X, 6); LDV(4, VP); EXP(Y, 9); EXP(Y, 10); ADD(Y, 3); SB(); \
      QK(X, 7); LDV(5, VP); EXP(Y, 11); ADD(Y, 4); CVT(pw1, Y, 4); SB(); \
    } while (0)
#define RESCALE(X) do { \
      if (__any(mx > THR)) { \
        const float delta = fmaxf(mx, 0.f); \
        const float alpha = __builtin_amdgcn_exp2f(-delta); \
        _Pragma("unroll") for (int e = 0; e < 16; ++e) { X[e] -= delta; negm[e] -= delta; } \
        _Pragma("unroll") for (int d = 0; d < 4; ++d) _Pragma("unroll") for (int e = 0; e < 16; ++e) o[d][e] *= alpha; \
        lsum *= alpha; \
      } } while (0)
#define STAGE_B(X, Y, VP, KBN) do { \
      PV(0); LDV(6, VP); EXP(Y, 12); EXP(Y, 13); CVT(pw1, Y, 5); SB(); \
      PV(1); LDV(7, VP); EXP(Y, 14); EXP(Y, 15); ADD(Y, 5); SB(); \
      PV(2); LDK(0, KBN); CVT(pw1, Y, 6); CVT(pw1, Y, 7); ADD(Y, 6); mx = -3.0e38f; MX(X, 0); SB(); \
      PV(3); LDK(1, KBN); MX(X, 1); MX(X, 2); ADD(Y, 7); ADD(Y, 8); SB(); \
      PV(4); LDK(2, KBN); MX(X, 3); MX(X, 4); ADD(Y, 9); ADD(Y, 10); SB(); \
      PV(5); LDK(3, KBN); MX(X, 5); MX(X, 6); ADD(Y, 11); ADD(Y, 12); SB(); \
      PV(6); LDK(4, KBN); MX(X, 7); ADD(Y, 13); ADD(Y, 14); SB(); \
      PV(7); LDK(5, KBN); mx = half_max(mx); ADD(Y, 15); SB(); \
      RESCALE(X); SB(); \
    } while (0)
#define STAGE_B_BAR(X, Y, VP, WAITS, SETD, POST) do { \
      PV(0); LDV(6, VP); EXP(Y, 12); EXP(Y, 13); CVT(pw1, Y, 5); SB(); \
      PV(1); LDV(7, VP); EXP(Y, 14); EXP(Y, 15); ADD(Y, 5); SB(); \
      PV(2); CVT(pw1, Y, 6); CVT(pw1, Y, 7); ADD(Y, 6); mx = -3.0e38f; MX(X, 0); SB(); \
      cur = next5(cur); \
      PV(3); SETADDR(); SETD; SB(); \
      PV(4); SB(); \
      WAITS; LBAR(); PRIO(3); \
      PV(5); SB(); LDK(0, 0); LDK(1, 0); LDK(2, 0); LDK(3, 0); MX(X, 1); MX(X, 2); MX(X, 3); MX(X, 4); ADD(Y, 7); ADD(Y, 8); ADD(Y, 9); POST; DMA(0); SB(); \
      PV(6); LDK(4, 0); MX(X, 5); MX(X, 6); MX(X, 7); ADD(Y, 10); ADD(Y, 11); ADD(Y, 12); DMA(1); SB(); \
      PV(7); LDK(5, 0); mx = half_max(mx); ADD(Y, 13); ADD(Y, 14); ADD(Y, 15); DMA(2); SB(); \
      RESCALE(X); SB(); \
    } while (0)


    SETADDR();
    #pragma unroll
    for (int i = 0; i < 8; ++i) LDK(i, 0);
    negm = f32x16{};
    #pragma unroll
    for (int d = 0; d < 4; ++d) o[d] = f32x16{};
    SB();
    QK(sA, 0); LDK(0, 1); DMA(0); SB(); QK(sA, 1); LDK(1, 1); DMA(1); SB(); QK(sA, 2); LDK(2, 1); DMA(2); SB(); QK(sA, 3); LDK(3, 1); SB();
    QK(sA, 4); LDK(4, 1); SB(); QK(sA, 5); LDK(5, 1); SB(); QK(sA, 6); SB(); QK(sA, 7); SB();
    mx = -3.0e38f;
    #pragma unroll
    for (int e = 0; e < 8; ++e) MX(sA, e);
    mx = half_max(mx);
    #pragma unroll
    for (int e = 0; e < 16; ++e) { sA[e] -= mx; negm[e] = -mx; }
    SB();
    STAGE_A(sB, sA, 1, 1, true);
    fill = SLOT;
    STAGE_B_BAR(sB, sA, 1, VMWAIT(0), if (DW) d_kv(DA, 2, fill),
                if (!DW && do_g1 && w == 4 && l == 0) flag_store(ws + WS_UFLAG + (dp1 * 28 + ft1) * 8, tag));
    for (int u = 0; u < 7; ++u) {
      STAGE_A(sA, sB, 0, 0, true);
      PRIO(2);
      STAGE_B(sA, sB, 0, 1);
      PRIO(1);
      STAGE_A(sB, sA, 1, 1, false);
      PRIO(0);
      fill = nextslot(fill);
      STAGE_B_BAR(sB, sA, 1, if (DW) VMWAIT(8), if (DW) { if (u + 3 < 8) d_kv(DA, u + 3, fill); else d_wo(DA, u - 5, fill); }, (void)0);
    }
    PRIO(0);
    #pragma unroll
    for (int i = 0; i < 8; ++i) LDV(i, 0);
    DMA(3); DMA(4);
    #pragma unroll
    for (int e = 0; e < 16; ++e) { EXP(sB, e); ADD(sB, e); }
    #pragma unroll
    for (int c = 0; c < 4; ++c) { CVT(pw0, sB, c); CVT(pw1, sB, 4 + c); }
    SB();
    fre = cur + wd * 1024; cur = nextslot(cur);
    const float inv = __builtin_amdgcn_rcpf(half_sum(lsum));
    const unsigned ob = XO + (hh * 16 + h) * 1024 + (32 * rh + r) * 16;
#define OWR(d) do { *(LDS3 bf16x8*)(ob + (4 * (d)) * 1024) = pack8<0>(o[d], inv); *(LDS3 bf16x8*)(ob + (4 * (d) + 2) * 1024) = pack8<1>(o[d], inv); } while (0)
    PV(0); PV(4); DMA(5); SB(); PV(1); PV(5); DMA(6); SB(); PV(2); OWR(0); SB(); PV(6); DMA(7); SB(); PV(3); OWR(1); SB(); PV(7); SB(); OWR(2); OWR(3);
  }

  {
    f32x16 acc[2][2];
    #pragma unroll
    for (int i = 0; i < 2; ++i)
      #pragma unroll
      for (int j = 0; j < 2; ++j) acc[i][j] = f32x16{};
    unsigned oa[2];
    #pragma unroll
    for (int j = 0; j < 2; ++j) oa[j] = (4 * h + q) * 1024 + (16 * (w >> 1) + 4 * ((2 * (w & 1) + j) ^ q) + 2 * gq + (p >> 1)) * 16 + (p & 1) * 8;
    const unsigned ob = XO + h * 1024 + r * 16;
    bf16x8 fa[2][2][2], fb[2][2][2];
    u32x2 pfl;
#define PLB(i_, s_, ks_) lds_b128(ob + (4 * (((s_) + ro) & 15) + 2 * (ks_)) * 1024 + 512 * (i_))
#define PLA(j_, ks_) lds_tr2(cur + oa[j_] + (16 * (ks_)) * 1024, cur + oa[j_] + (16 * (ks_) + 8) * 1024)
#define PBASE(s_) ((const char*)A.wo + (size_t)(16 * g + (((s_) + ro) & 15)) * 32768)
    unsigned pv8[8];
    #pragma unroll
    for (int i = 0; i < 8; ++i) pv8[i] = (4 * i + wd) * 1024u + wsw;
    auto auxmid = [&](int) {};
#define PX(P, n_) do { } while (0)
#define POLL_ISSUE do { if (!DW) asm volatile("global_load_dwordx2 %0, %1, off sc1" : "=&v"(pfl) : "v"(ws + WS_UFLAG + ((dt2 >> 1) * 28 + (l < 28 ? l : 0)) * 8) : "memory"); } while (0)
#define POLL_CHECK do { if (!DW) { asm volatile("s_waitcnt vmcnt(0)" : "+v"(pfl) :: "memory"); \
      if (do_g2 && w == 4 && !__all(pfl[0] == tagl && pfl[1] == tagh)) poll_flags((const u64*)(ws + WS_UFLAG) + (dt2 >> 1) * 28, 28, tag, l); } } while (0)
    auto nextd = [&](int n) { if (DW) { if (do_g2) d_g2(DA, n, (2 + n) % 3); else d_wo(DA, n, ((2 + n) % 3) * SLOT); } };
    PLOOP((void)0);
#undef PLB
#undef PLA
#undef PBASE
#undef POLL_ISSUE
#undef POLL_CHECK
    char* pp = ws + WS_PART + ((size_t)(rb * 8 + w) * 8 + g) * 8192 + l * 16;
    { constexpr bool MM = true;
      PMM(1, 0, 0, 0); PMM(1, 1, 0, 0); PMM(1, 0, 0, 1); PMM(1, 1, 0, 1); PMM(1, 0, 1, 0);
      store16_sc1(pp, __builtin_bit_cast(u32x4, pack8n<0>(acc[0][0]))); store16_sc1(pp + 4096, __builtin_bit_cast(u32x4, pack8n<1>(acc[0][0]))); SB();
      PMM(1, 1, 1, 0);
      store16_sc1(pp + 1024, __builtin_bit_cast(u32x4, pack8n<0>(acc[0][1]))); store16_sc1(pp + 5120, __builtin_bit_cast(u32x4, pack8n<1>(acc[0][1]))); SB();
      PMM(1, 0, 1, 1); PMM(1, 1, 1, 1);
      store16_sc1(pp + 2048, __builtin_bit_cast(u32x4, pack8n<0>(acc[1][0]))); store16_sc1(pp + 6144, __builtin_bit_cast(u32x4, pack8n<1>(acc[1][0])));
      store16_sc1(pp + 3072, __builtin_bit_cast(u32x4, pack8n<0>(acc[1][1]))); store16_sc1(pp + 7168, __builtin_bit_cast(u32x4, pack8n<1>(acc[1][1])));
    }
    VMWAIT(0);
    if (l == 0) flag_store(ws + WS_SFLAG + ((rb * 8 + w) * 8 + g) * 8, tag);
    if (do_g2) LBAR();
  }

  if (do_g2) {
    const int wi = (w >> 1) & 1, wj = w & 1, sb2 = DW ? 1 : 0;
    f32x16 acc[2] = {f32x16{}, f32x16{}};
    const unsigned aa = h * 1024 + r * 16;
    const unsigned ba = (4 * h + q) * 128 + (4 * ((wd & 1) ^ (q >> 1)) + 2 * gq + (p >> 1)) * 16 + (p & 1) * 8;
    const unsigned gbase[3] = {65536u, 0u, 32768u};
    const unsigned gsub8 = 8192u * (1 + (wd >> 1)), gsub32 = 32768u * (1 + (wd >> 1));
    bf16x8 fa[2][4][2], fb[2][4];
    const int srow = 64 * (2 * pp2 + sb2) + 32 * wi + r;
    const size_t xo = (size_t)srow * 512 + 64 * dt2 + 32 * wj + 8 * h;
    const char* pp = ws + WS_PART + (size_t)((2 * pp2 + sb2) * 8 + dt2) * 65536 + (wi * 2 + wj) * 1024 + l * 16;
    const u64* sfl = (const u64*)(ws + WS_SFLAG) + ((2 * pp2 + sb2) * 8 + dt2) * 8;
    u32x4 pv[16];
    u32x2 pfl;
#define GLA(N, kk, j_) fa[N][kk][j_] = lds_b128(ga_ + aa + 2 * (kk) * 1024 + 512 * (j_))
#define GLB(N, kk) fb[N][kk] = lds_tr2(gb_ + ba + (16 * (kk)) * 128, gb_ + ba + (16 * (kk) + 8) * 128)
#define PLD(c) asm volatile("global_load_dwordx4 %0, %1, off sc1" : "=v"(pv[c]) : "v"(pp + ((c) >> 1) * 8192 + ((c) & 1) * 4096) : "memory")
#define G2D(t_, j_, AUX) do { if (DW) d_g2(DA, t_, j_); else { AUX; } } while (0)
    if (DW) { DMX_G(DA); d_g2(DA, 3, 3); }
    GSTEP(1, 0, 0, false, 1, G2D(4, 2, (void)0));
    GBAR(12); GSTEP(0, 1, 1, true, 1, G2D(5, 0, (void)0));
    GBAR(12); GSTEP(1, 0, 2, true, 1, G2D(6, 1, (void)0));
    GBAR(12); GSTEP(0, 1, 3, true, 1, G2D(7, 3, (void)0));
    for (int t = 4; t < 12; t += 4) {
      GBAR(12); GSTEP(1, 0, 0, true, 1, G2D(t + 4, 2, (void)0));
      GBAR(12); GSTEP(0, 1, 1, true, 1, G2D(t + 5, 0, (void)0));
      GBAR(12); GSTEP(1, 0, 2, true, 1, G2D(t + 6, 1, (void)0));
      GBAR(12); GSTEP(0, 1, 3, true, 1, G2D(t + 7, 3, (void)0));
    }
    GBAR(12); GSTEP(1, 0, 0, true, 1, G2D(16, 2, asm volatile("global_load_dwordx2 %0, %1, off sc1" : "=&v"(pfl) : "v"(sfl + (l & 7)) : "memory")));
    GBAR(12); GSTEP(0, 1, 1, true, 1, G2D(17, 0, (void)0));
    GBAR(12); GSTEP(1, 0, 2, true, 1, G2D(18, 1, (void)0));
    GBAR(12); GSTEP(0, 1, 3, true, 1, G2D(19, 3, { asm volatile("s_waitcnt vmcnt(0)" : "+v"(pfl) :: "memory");
                                                  if (!__all(pfl[0] == tagl && pfl[1] == tagh)) poll_flags(sfl, 8, tag, l); PLD(0); PLD(1); }));
    GBAR(12); GSTEP(1, 0, 0, true, 1, G2D(20, 2, { PLD(2); PLD(3); }));
    GBAR(12); GSTEP(0, 1, 1, true, 1, G2D(21, 0, { PLD(4); PLD(5); }));
    GBAR(12); GSTEP(1, 0, 2, true, 1, G2D(22, 1, { PLD(6); PLD(7); }));
    GBAR(12); GSTEP(0, 1, 3, true, 1, G2D(23, 3, { PLD(8); PLD(9); }));
    GBAR(12); GSTEP(1, 0, 0, true, 1, G2D(24, 2, { PLD(10); PLD(11); }));
    GBAR(12); GSTEP(0, 1, 1, true, 1, G2D(25, 0, { PLD(12); PLD(13); }));
    GBAR(12); GSTEP(1, 0, 2, true, 1, G2D(26, 1, { PLD(14); PLD(15); }));
    GBAR(12); GSTEP(0, 1, 3, true, 1, G2D(27, 3, (void)0));
    GBAR(12); GSTEP(1, 0, 0, true, 1, (void)0);
    u32x4 xv0, xv1; unsigned b2r;
    asm volatile("global_load_dwordx4 %0, %3, off\n\tglobal_load_dwordx4 %1, %3, off offset:32\n\tglobal_load_ushort %2, %4, off"
                 : "=&v"(xv0), "=&v"(xv1), "=&v"(b2r) : "v"(x + xo), "v"(A.b2 + srow) : "memory");
    GBAR(15); GSTEP(0, 1, 1, true, 0, (void)0);
    GBAR(9); GSTEP(1, 0, 2, true, 0, (void)0);
    GBAR(3); GSTEP(0, 1, 3, true, 0, (void)0);
    GLAST();
#undef GLA
#undef GLB
    if (!DW) {
      #pragma unroll
      for (int dh = 0; dh < 2; ++dh)
        #pragma unroll
        for (int m = 0; m < 4; ++m) {
          const f32x4 v4 = {acc[dh][4 * m], acc[dh][4 * m + 1], acc[dh][4 * m + 2], acc[dh][4 * m + 3]};
          *(LDS3 f32x4*)(XO + (wd * 2 + dh) * 4096 + m * 1024 + l * 16) = v4;
        }
    }
    LBAR();
    f32x16 yv;
    #pragma unroll
    for (int m = 0; m < 4; ++m) {
      const f32x4 v4 = *(LDS3 f32x4*)(XO + ((sb2 * 2 + wi) * 2 + wj) * 4096 + m * 1024 + l * 16);
      yv[4 * m] = v4[0]; yv[4 * m + 1] = v4[1]; yv[4 * m + 2] = v4[2]; yv[4 * m + 3] = v4[3];
    }
    asm volatile("s_waitcnt vmcnt(0)" : "+v"(xv0), "+v"(xv1), "+v"(b2r) :: "memory");
    if (!DW) asm volatile("" : "+v"(pv[0]), "+v"(pv[1]), "+v"(pv[2]), "+v"(pv[3]), "+v"(pv[4]), "+v"(pv[5]), "+v"(pv[6]), "+v"(pv[7]),
                 "+v"(pv[8]), "+v"(pv[9]), "+v"(pv[10]), "+v"(pv[11]), "+v"(pv[12]), "+v"(pv[13]), "+v"(pv[14]), "+v"(pv[15]) :: "memory");
    const float bias2 = bflo(b2r);
    #pragma unroll
    for (int e = 0; e < 16; ++e) {
      const float yy = yv[e] + bias2;
      yv[e] = yy * __builtin_amdgcn_rcpf(1.f + __builtin_amdgcn_exp2f(-1.4426950408889634f * yy));
    }
    asm volatile("" : "+v"(yv));
    float t[16];
    #pragma unroll
    for (int e = 0; e < 16; ++e) t[e] = 0.f;
    auto accum = [&](const u32x4& a, const u32x4& c) {
      #pragma unroll
      for (int e = 0; e < 4; ++e) { t[2 * e] += bflo(a[e]); t[2 * e + 1] += bfhi(a[e]); t[8 + 2 * e] += bflo(c[e]); t[8 + 2 * e + 1] += bfhi(c[e]); }
    };
    if (DW) {
      unsigned done = 0;
      const char* pb = ws + WS_PART + (size_t)((2 * pp2 + 1) * 8 + dt2) * 65536 + (wi * 2 + wj) * 1024;
      for (unsigned spins = 0; done != 0xffu && spins < (1u << 20); ++spins) {
        const u64 f = __hip_atomic_load((GAS const u64*)(sfl + (l & 7)), __ATOMIC_RELAXED, __HIP_MEMORY_SCOPE_AGENT);
        unsigned nw = (unsigned)__ballot(f == tag) & 0xffu & ~done;
        done |= nw;
        if (!nw) __builtin_amdgcn_s_sleep(1);
        while (nw) {
          const int gg = __builtin_ctz(nw); nw &= nw - 1;
          dma16o<0>(pb + gg * 8192, l * 16u, wd * 16384 + gg * 2048);
          dma16o<1024>(pb + gg * 8192 + 4096, l * 16u, wd * 16384 + gg * 2048);
        }
      }
      VMWAIT(0);
      #pragma unroll
      for (int c = 0; c < 16; ++c) pv[c] = *(LDS3 u32x4*)(wd * 16384 + c * 1024 + l * 16);
    }
    #pragma unroll
    for (int gg = 0; gg < 8; ++gg) accum(pv[2 * gg], pv[2 * gg + 1]);
    #pragma unroll
    for (int c = 0; c < 2; ++c) {
      #pragma unroll
      for (int e = 0; e < 8; ++e) t[8 * c + e] *= yv[8 * c + e];
      #pragma unroll
      for (int e = 0; e < 4; ++e) {
        auto rr = __builtin_amdgcn_permlane32_swap(__float_as_uint(t[8 * c + e]), __float_as_uint(t[8 * c + 4 + e]), false, false);
        t[8 * c + e] = __uint_as_float(rr[0]); t[8 * c + 4 + e] = __uint_as_float(rr[1]);
      }
      const u32x4 xv = c ? xv1 : xv0;
      u32x4 ov;
      #pragma unroll
      for (int e = 0; e < 4; ++e) ov[e] = pk2(t[8 * c + 2 * e] + bflo(xv[e]), t[8 * c + 2 * e + 1] + bfhi(xv[e]));
      *(u32x4*)(A.out + xo + 16 * c) = ov;
    }
  }
}

__global__ __launch_bounds__(512, 2) void fused_kernel(Args A) {
  extern __shared__ __attribute__((aligned(16))) char lds_raw[];
  const int l = threadIdx.x & 63;
  const int w = __builtin_amdgcn_readfirstlane(threadIdx.x >> 6);
  if (w < 4) body<true>(A, w, l); else body<false>(A, w, l);
}

extern "C" void kernel_launch(void* const* d_in, const int* in_sizes, int n_in, void* d_out, int out_size, void* d_ws, size_t ws_size, hipStream_t stream) {
  (void)hipFuncSetAttribute((const void*)fused_kernel, hipFuncAttributeMaxDynamicSharedMemorySize, LDS_BYTES);
  Args A;
  A.x = (const __bf16*)d_in[0]; A.kc = (const __bf16*)d_in[1]; A.vc = (const __bf16*)d_in[2]; A.wq = (const __bf16*)d_in[3]; A.wo = (const __bf16*)d_in[4];
  A.w1 = (const __bf16*)d_in[6]; A.b1 = (const __bf16*)d_in[7]; A.w2 = (const __bf16*)d_in[8]; A.b2 = (const __bf16*)d_in[9];
  A.out = (__bf16*)d_out; A.ws = (char*)d_ws;
  fused_kernel<<<256, 512, LDS_BYTES, stream>>>(A);
}
```

```cpp
#include <hip/hip_runtime.h>
#include <stdint.h>
#pragma clang diagnostic ignored "-Wint-to-pointer-cast"

typedef __bf16 bf16x8 __attribute__((ext_vector_type(8)));
typedef __bf16 bf16x4 __attribute__((ext_vector_type(4)));
typedef __bf16 bf16x2 __attribute__((ext_vector_type(2)));
typedef float f32x16 __attribute__((ext_vector_type(16)));
typedef float f32x4 __attribute__((ext_vector_type(4)));
typedef float f32x2 __attribute__((ext_vector_type(2)));
typedef unsigned u32x4 __attribute__((ext_vector_type(4)));
typedef unsigned u32x2 __attribute__((ext_vector_type(2)));
typedef unsigned long long u64;
#define LDS3 __attribute__((address_space(3)))
#define GAS __attribute__((address_space(1)))
#define DI __device__ __forceinline__
#define SB() __builtin_amdgcn_sched_barrier(0)
#define PRIO(n) __builtin_amdgcn_s_setprio(n)

constexpr int SLOT = 32768;
constexpr int XO = 98304;
constexpr int LDS_BYTES = 163840;
constexpr size_t WS_EPOCH = 0;
constexpr size_t WS_HFLAG = 4096;
constexpr size_t WS_UFLAG = 8192;
constexpr size_t WS_SFLAG = 16384;
constexpr size_t WS_H     = 1u << 20;
constexpr size_t WS_U     = 4u << 20;
constexpr size_t WS_PART  = 8u << 20;

constexpr float THR = 16.0f;
constexpr float QSCALE = 0.08838834764831845f * 1.4426950408889634f;

DI bf16x8 lds_b128(unsigned a) { return *(LDS3 bf16x8*)a; }
DI bf16x8 lds_tr2(unsigned a0, unsigned a1) {
  bf16x4 lo = __builtin_amdgcn_ds_read_tr16_b64_v4bf16((LDS3 bf16x4*)a0);
  bf16x4 hi = __builtin_amdgcn_ds_read_tr16_b64_v4bf16((LDS3 bf16x4*)a1);
  return __builtin_shufflevector(lo, hi, 0, 1, 2, 3, 4, 5, 6, 7);
}
DI f32x16 mfma(bf16x8 a, bf16x8 b, f32x16 c) { return __builtin_amdgcn_mfma_f32_32x32x16_bf16(a, b, c, 0, 0, 0); }
DI f32x4 mfma16(bf16x8 a, bf16x8 b, f32x4 c) { return __builtin_amdgcn_mfma_f32_16x16x32_bf16(a, b, c, 0, 0, 0); }
DI unsigned pk2(float lo, float hi) { f32x2 v = {lo, hi}; bf16x2 b = __builtin_convertvector(v, bf16x2); return __builtin_bit_cast(unsigned, b); }
template <int S> DI bf16x8 pack8(const f32x16& x, float sc) {
  u32x4 p;
  p[0] = pk2(x[8 * S + 0] * sc, x[8 * S + 1] * sc); p[1] = pk2(x[8 * S + 2] * sc, x[8 * S + 3] * sc);
  p[2] = pk2(x[8 * S + 4] * sc, x[8 * S + 5] * sc); p[3] = pk2(x[8 * S + 6] * sc, x[8 * S + 7] * sc);
  return __builtin_bit_cast(bf16x8, p);
}
template <int S> DI bf16x8 pack8n(const f32x16& x) {
  u32x4 p;
  p[0] = pk2(x[8 * S + 0], x[8 * S + 1]); p[1] = pk2(x[8 * S + 2], x[8 * S + 3]);
  p[2] = pk2(x[8 * S + 4], x[8 * S + 5]); p[3] = pk2(x[8 * S + 6], x[8 * S + 7]);
  return __builtin_bit_cast(bf16x8, p);
}
DI u32x4 pack44(const f32x4& a, const f32x4& c);
DI float bflo(unsigned u) { return __uint_as_float(u << 16); }
DI float bfhi(unsigned u) { return __uint_as_float(u & 0xffff0000u); }
template <int C> DI float dadd(float v) { return v + __builtin_bit_cast(float, __builtin_amdgcn_update_dpp(0, __builtin_bit_cast(int, v), C, 0xf, 0xf, true)); }
DI float dot_one(unsigned u, float c) { return __builtin_amdgcn_fdot2_f32_bf16(__builtin_bit_cast(bf16x2, u), __builtin_bit_cast(bf16x2, 0x3f803f80u), c, false); }
DI float dot_sq(unsigned u, float c) { return __builtin_amdgcn_fdot2_f32_bf16(__builtin_bit_cast(bf16x2, u), __builtin_bit_cast(bf16x2, u), c, false); }
DI float half_sum(float v) {
  auto b = __builtin_amdgcn_permlane32_swap(__float_as_uint(v), __float_as_uint(v), false, false);
  return __uint_as_float(b[0]) + __uint_as_float(b[1]);
}
DI float half_max(float v) {
  auto b = __builtin_amdgcn_permlane32_swap(__float_as_uint(v), __float_as_uint(v), false, false);
  return fmaxf(fmaxf(-3.0e38f, __uint_as_float(b[0])), __uint_as_float(b[1]));
}
DI u32x4 pack44(const f32x4& a, const f32x4& c) { u32x4 p; p[0] = pk2(a[0], a[1]); p[1] = pk2(a[2], a[3]); p[2] = pk2(c[0], c[1]); p[3] = pk2(c[2], c[3]); return p; }
DI void store16_sc1(void* p, u32x4 v) {
  asm volatile("global_store_dwordx4 %0, %1, off sc1\n\ts_nop 1" :: "v"(p), "v"(v) : "memory");
}
#define VMWAIT(N) asm volatile("s_waitcnt vmcnt(" #N ")" ::: "memory")
#define LBAR() do { asm volatile("" ::: "memory"); __builtin_amdgcn_s_waitcnt(0xC07F); __builtin_amdgcn_s_barrier(); asm volatile("" ::: "memory"); } while (0)
#define STEPBAR() do { if (DW) VMWAIT(8); LBAR(); } while (0)

DI void poll_flags(const u64* f, int nflags, u64 tag, int lane) {
  for (unsigned spins = 0; spins < (1u << 20); ++spins) {
    bool ok = true;
    for (int i = lane; i < nflags; i += 64)
      ok &= (__hip_atomic_load((GAS const u64*)(f + i), __ATOMIC_RELAXED, __HIP_MEMORY_SCOPE_AGENT) == tag);
    if (__all(ok)) break;
    __builtin_amdgcn_s_sleep(2);
  }
  asm volatile("" ::: "memory");
}
DI void flag_store(char* p, u64 tag) { __hip_atomic_store((GAS u64*)p, tag, __ATOMIC_RELAXED, __HIP_MEMORY_SCOPE_AGENT); }


struct Args {
  const __bf16* x; const __bf16* kc; const __bf16* vc; const __bf16* wq; const __bf16* wo; const __bf16* w1;
  const __bf16* b1; const __bf16* w2; const __bf16* b2; __bf16* out; char* ws;
};
struct Desc { const char* p[4]; unsigned lds[4]; unsigned v[4]; };

template <int OFF> DI void dma16p(const void* sbase, unsigned voff, unsigned lds) {
  asm volatile("s_add_u32 m0, %2, %3\n\ts_nop 0\n\tglobal_load_lds_dwordx4 %0, %1" :: "v"(voff), "s"(sbase), "s"(__builtin_amdgcn_readfirstlane(lds)), "i"(OFF) : "memory", "scc");
}
template <int OFF> DI void dma16o(const void* sbase, unsigned voff, unsigned lds) {
  asm volatile("s_add_u32 m0, %2, %3\n\ts_nop 0\n\tglobal_load_lds_dwordx4 %0, %1 sc1" :: "v"(voff), "s"(sbase), "s"(__builtin_amdgcn_readfirstlane(lds)), "i"(OFF) : "memory", "scc");
}
template <bool DW>
DI void body(const Args& A, const int w, const int l) {
  const __bf16* __restrict__ x = A.x; char* __restrict__ ws = A.ws;
  const int wd = w & 3;
  const int b = blockIdx.x, xcd = b & 7, idx = b >> 3;
  const int pidx = ((idx >> 2) << 1) | (idx & 1);
  const int g = 2 * (xcd & 3) + (idx & 1), rb = 16 * (xcd >> 2) + (idx >> 1);
  const int r = l & 31, h = l >> 5, gq = (l >> 4) & 1, q = (l >> 2) & 3, p = l & 3;
  const unsigned l4 = l >> 4;

  const int xrow8 = 8 * w + (l >> 3);
  const int hrow = 64 * rb + 8 * g + 2 * wd + h;
  u32x4 xr[8], hr[2];
  u32x2 ep2;
  {
    const char* xb = (const char*)x + (size_t)(64 * rb + xrow8) * 1024 + (l & 7) * 16;
    asm volatile("global_load_dwordx4 %0, %9, off\n\tglobal_load_dwordx4 %1, %9, off offset:128\n\tglobal_load_dwordx4 %2, %9, off offset:256\n\t"
                 "global_load_dwordx4 %3, %9, off offset:384\n\tglobal_load_dwordx4 %4, %9, off offset:512\n\tglobal_load_dwordx4 %5, %9, off offset:640\n\t"
                 "global_load_dwordx4 %6, %9, off offset:768\n\tglobal_load_dwordx4 %7, %9, off offset:896\n\tglobal_load_dwordx2 %8, %10, off sc1"
                 : "=&v"(xr[0]), "=&v"(xr[1]), "=&v"(xr[2]), "=&v"(xr[3]), "=&v"(xr[4]), "=&v"(xr[5]), "=&v"(xr[6]), "=&v"(xr[7]), "=&v"(ep2)
                 : "v"(xb), "v"(ws + WS_EPOCH) : "memory");
    if (!DW) asm volatile("global_load_dwordx4 %0, %2, off\n\tglobal_load_dwordx4 %1, %2, off offset:512"
                 : "=&v"(hr[0]), "=&v"(hr[1]) : "v"((const char*)x + (size_t)hrow * 1024 + r * 16) : "memory");
  }
  const int rq = rb & 15, rt = pidx >> 1, ro = pidx & 14;
  const bool do_g2 = (rb & 1) == 0, do_g1 = !do_g2 && pidx < 14;
  const int ft1 = 7 * (xcd >> 1) + (pidx >> 1), dp1 = 2 * (xcd & 1) + (pidx & 1);
  const int pp2 = 4 * (xcd >> 1) + (pidx >> 2), dt2 = 4 * (xcd & 1) + (pidx & 3);
  const int r1 = (16 * (pidx & 1) + 4 * (pidx >> 1) + (pidx >> 2)) & 31;
  const int r2 = (7 * (pidx & 3) + 7 * (pidx >> 2) / 4 + 2 * (pidx >> 2)) % 28;
  const unsigned wsw = (unsigned)((l ^ (wd << 2)) << 4);
  const unsigned gsw = (unsigned)(((l & 7) ^ (((l >> 4) & 3) << 1)) << 4);
  const unsigned grow = 8 * wd + (l >> 3);
  Desc DA;
  auto d_lds = [&](Desc& D, unsigned slot) { D.lds[0] = slot + wd * 1024; D.lds[1] = D.lds[0] + 8192; D.lds[2] = D.lds[0] + 16384; D.lds[3] = D.lds[0] + 24576; };
  auto d_ldsg = [&](Desc& D, int j) { const unsigned b0 = j < 3 ? j * 32768u : 24576u, st = j < 3 ? 8192u : 32768u;
                                      D.lds[0] = D.lds[1] = b0 + wd * 1024; D.lds[2] = D.lds[0] + st; D.lds[3] = D.lds[0] + 2 * st; };
  auto d_wq = [&](Desc& D, int s, unsigned slot) {
    D.p[0] = (const char*)A.wq + (size_t)(32 * ((s + rq) & 15)) * 8192 + g * 1024;
    D.p[1] = D.p[0] + 8 * 8192; D.p[2] = D.p[0] + 16 * 8192; D.p[3] = D.p[0] + 24 * 8192;
    D.v[0] = D.v[2] = wd * 8192u + wsw; D.v[1] = D.v[3] = D.v[0] + 4 * 8192; d_lds(D, slot);
  };
  auto d_wo = [&](Desc& D, int s, unsigned slot) {
    D.p[0] = (const char*)A.wo + (size_t)(512 * g + 32 * ((s + ro) & 15)) * 1024;
    D.p[1] = D.p[0] + 8192; D.p[2] = D.p[0] + 16384; D.p[3] = D.p[0] + 24576;
    D.v[0] = D.v[2] = wd * 1024u + wsw; D.v[1] = D.v[3] = (D.v[0] ^ 32u) + 4096; d_lds(D, slot);
  };
  auto d_kv = [&](Desc& D, int u, unsigned slot) {
    const size_t o = ((size_t)g * 2048 + 1536) * 256;
    D.p[0] = (const char*)A.kc + o + (size_t)((u + 1 + rt) & 7) * 16384; D.p[1] = D.p[0] + 8192;
    D.p[2] = (const char*)A.vc + o + (size_t)((u + rt) & 7) * 16384 + 8192;
    D.p[3] = (const char*)A.vc + o + (size_t)((u + 1 + rt) & 7) * 16384;
    D.v[0] = (4 * wd + l4) * 256 + (((l & 15) ^ (4 * wd + l4)) << 4); D.v[1] = D.v[0] + 4096;
    D.v[2] = (4 * wd + l4) * 256 + (((l & 15) ^ (l4 << 2)) << 4); D.v[3] = D.v[2] + 4096; d_lds(D, slot);
  };
  auto d_g1 = [&](Desc& D, int t, int j) {
    const int tt = (t + r1) & 31;
    D.p[0] = D.p[1] = (const char*)A.w1 + (size_t)(64 * tt) * 3584 + ft1 * 128;
    D.p[2] = ws + WS_H + (size_t)dp1 * 524288 + (size_t)(64 * tt) * 256; D.p[3] = D.p[2] + 128;
    D.v[0] = grow * 3584u + gsw; D.v[1] = D.v[0] + 32 * 3584; D.v[2] = grow * 256u + gsw; D.v[3] = D.v[2] + 32 * 256; d_ldsg(D, j);
  };
  auto d_g2 = [&](Desc& D, int t, int j) {
    int tt = t + r2; if (tt >= 28) tt -= 28;
    D.p[0] = D.p[1] = ws + WS_U + (size_t)dt2 * (224 * 1024) + (size_t)tt * 8192;
    D.p[2] = (const char*)A.w2 + (size_t)(64 * tt) * 4096 + pp2 * 256; D.p[3] = D.p[2] + 128;
    D.v[0] = wd * 1024u + l * 16u; D.v[1] = D.v[0] + 4096; D.v[2] = grow * 4096u + gsw; D.v[3] = D.v[2] + 32 * 4096; d_ldsg(D, j);
  };
#define DMX(D, i) do { if (DW) dma16o<((i) & 1) * 4096>(D.p[(i) >> 1], D.v[((i) >> 2) * 2 + ((i) & 1)], D.lds[(i) >> 1]); } while (0)
#define DMA(i) do { if (DW) dma16p<((i) & 1) * 4096>(DA.p[(i) >> 1], DA.v[((i) >> 2) * 2 + ((i) & 1)], DA.lds[(i) >> 1]); } while (0)
#define DMX_ALL(D) do { DMX(D, 0); DMX(D, 1); DMX(D, 2); DMX(D, 3); DMX(D, 4); DMX(D, 5); DMX(D, 6); DMX(D, 7); } while (0)
#define DMX_G(D) do { DMX(D, 0); DMX(D, 1); DMX(D, 4); DMX(D, 5); DMX(D, 6); DMX(D, 7); } while (0)
  auto nextslot = [](unsigned s) { return s >= 2 * SLOT ? 0u : s + SLOT; };
  auto next5 = [](unsigned s) { return (s == 2 * SLOT || s == 4 * SLOT) ? 0u : s + SLOT; };

  if (DW) { d_wq(DA, 0, 0); DMX_ALL(DA); d_wq(DA, 1, SLOT); }
#define XOPS "+v"(xr[0]), "+v"(xr[1]), "+v"(xr[2]), "+v"(xr[3]), "+v"(xr[4]), "+v"(xr[5]), "+v"(xr[6]), "+v"(xr[7]), "+v"(ep2)
  if (DW) asm volatile("s_waitcnt vmcnt(8) ; REL" : XOPS :: "memory");
  else asm volatile("s_waitcnt vmcnt(0)" : XOPS, "+v"(hr[0]), "+v"(hr[1]) :: "memory");
  const u64 tag = (((u64)(unsigned)__builtin_amdgcn_readfirstlane(ep2[1]) << 32) | (u64)(unsigned)__builtin_amdgcn_readfirstlane(ep2[0])) + 0x9E3779B97F4A7C15ull;
  const unsigned tagl = (unsigned)tag, tagh = (unsigned)(tag >> 32);

  {
    float sm = 0.f, sq = 0.f, sm2 = 0.f, sq2 = 0.f;
    #pragma unroll
    for (int i = 0; i < 8; ++i)
      #pragma unroll
      for (int j = 0; j < 4; j += 2) { sm = dot_one(xr[i][j], sm); sq = dot_sq(xr[i][j], sq); sm2 = dot_one(xr[i][j + 1], sm2); sq2 = dot_sq(xr[i][j + 1], sq2); }
    sm += sm2; sq += sq2;
    sm = dadd<0xB1>(sm); sq = dadd<0xB1>(sq); sm = dadd<0x4E>(sm); sq = dadd<0x4E>(sq); sm = dadd<0x141>(sm); sq = dadd<0x141>(sq);
    const float m = sm * (1.f / 512.f);
    const float var = fmaxf(sq * (1.f / 512.f) - m * m, 0.f);
    const float rs = __builtin_amdgcn_rsqf(var + 1e-5f);
    const float mb = -m * rs;
    const unsigned xw = XO + (l & 7) * 1024 + ((xrow8 ^ (l & 7)) << 4);
#define LNROW(i) do { u32x4 o; _Pragma("unroll") for (int j = 0; j < 4; ++j) o[j] = pk2(bflo(xr[i][j]) * rs + mb, bfhi(xr[i][j]) * rs + mb); \
      *(LDS3 u32x4*)(xw + (i) * 8192) = o; DMA(i); } while (0)
    LNROW(0); LNROW(1); LNROW(2); LNROW(3); LNROW(4); LNROW(5); LNROW(6); LNROW(7);
  }
  auto hpub = [&]() { if (!DW) {
    float sm = 0.f, sq = 0.f;
    #pragma unroll
    for (int i = 0; i < 2; ++i)
      #pragma unroll
      for (int j = 0; j < 4; ++j) { sm = dot_one(hr[i][j], sm); sq = dot_sq(hr[i][j], sq); }
    sm = dadd<0xB1>(sm); sq = dadd<0xB1>(sq); sm = dadd<0x4E>(sm); sq = dadd<0x4E>(sq);
    sm = dadd<0x141>(sm); sq = dadd<0x141>(sq); sm = dadd<0x140>(sm); sq = dadd<0x140>(sq);
    { auto a = __builtin_amdgcn_permlane16_swap(__float_as_uint(sm), __float_as_uint(sm), false, false); sm = __uint_as_float(a[0]) + __uint_as_float(a[1]);
      auto c = __builtin_amdgcn_permlane16_swap(__float_as_uint(sq), __float_as_uint(sq), false, false); sq = __uint_as_float(c[0]) + __uint_as_float(c[1]); }
    const float m = sm * (1.f / 512.f);
    const float var = fmaxf(sq * (1.f / 512.f) - m * m, 0.f);
    const float rs = __builtin_amdgcn_rsqf(var + 1e-5f);
    const float rs2 = rs * __builtin_amdgcn_rsqf(var * rs * rs + 1e-5f), mb2 = -m * rs2;
    char* hp = ws + WS_H + (size_t)(r >> 4) * 524288 + (size_t)hrow * 256 + (r & 15) * 16;
    #pragma unroll
    for (int i = 0; i < 2; ++i) {
      u32x4 o;
      #pragma unroll
      for (int j = 0; j < 4; ++j) o[j] = pk2(bflo(hr[i][j]) * rs2 + mb2, bfhi(hr[i][j]) * rs2 + mb2);
      store16_sc1(hp + i * 1048576, o);
    }
  } };
  unsigned cur = 0;
  unsigned fre = 2 * SLOT + wd * 1024;

#define PMM(P, ks_, i_, j_) do { if (MM) acc[i_][j_] = mfma(fa[P][ks_][j_], fb[P][ks_][i_], acc[i_][j_]); SB(); } while (0)
#define PDM(FAST, i) do { if (FAST) { if (DW) dma16p<(i) * 4096>(pb_, pv8[i], fre); } else DMX(DA, i); SB(); } while (0)
#define PSTEP32(P, N, s_, MM_, FAST, MID) do { constexpr bool MM = MM_; STEPBAR(); PRIO(1); \
      PMM(P, 0, 1, 0); fb[N][0][1] = PLB(1, s_, 0); const char* pb_ = PBASE((s_) + 2); PX(P, 0); SB(); PDM(FAST, 0); \
      PMM(P, 0, 0, 0); fa[N][0][0] = PLA(0, 0); PX(P, 1); SB(); PDM(FAST, 1); \
      PMM(P, 0, 1, 1); fb[N][0][0] = PLB(0, s_, 0); fa[N][0][1] = PLA(1, 0); SB(); PDM(FAST, 2); \
      PMM(P, 0, 0, 1); fb[N][1][1] = PLB(1, s_, 1); PX(P, 2); SB(); PDM(FAST, 3); PRIO(0); \
      PMM(P, 1, 1, 0); fa[N][1][0] = PLA(0, 1); fb[N][1][0] = PLB(0, s_, 1); PX(P, 3); SB(); PDM(FAST, 4); \
      PMM(P, 1, 1, 1); fa[N][1][1] = PLA(1, 1); PX(P, 4); SB(); PDM(FAST, 5); \
      PMM(P, 1, 0, 0); PX(P, 5); PDM(FAST, 6); PDM(FAST, 7); MID; fre = cur + wd * 1024; cur = nextslot(cur); SB(); \
      PMM(P, 1, 0, 1); } while (0)
#define OMM(P, a_, b_) do { if (MM) acc[a_][b_] = mfma16(fa[P][a_], fb[P][b_], acc[a_][b_]); SB(); } while (0)
#define PSTEP16(P, N, s_, MM_, FAST, MID) do { constexpr bool MM = MM_; STEPBAR(); PRIO(1); \
      OMM(P, 0, 0); OMM(P, 0, 1); fb[N][0] = PLB(0, s_); const char* pb_ = PBASE((s_) + 2); SB(); PDM(FAST, 0); \
      OMM(P, 0, 2); OMM(P, 0, 3); fa[N][0] = PLA(0); SB(); PDM(FAST, 1); \
      OMM(P, 1, 0); fb[N][1] = PLB(1, s_); SB(); OMM(P, 1, 1); fa[N][1] = PLA(1); SB(); PDM(FAST, 2); \
      OMM(P, 1, 2); OMM(P, 1, 3); fb[N][2] = PLB(2, s_); SB(); PDM(FAST, 3); PRIO(0); \
      OMM(P, 2, 0); fb[N][3] = PLB(3, s_); SB(); OMM(P, 2, 1); fa[N][2] = PLA(2); SB(); PDM(FAST, 4); \
      OMM(P, 2, 2); OMM(P, 2, 3); fa[N][3] = PLA(3); SB(); PDM(FAST, 5); \
      OMM(P, 3, 0); PDM(FAST, 6); OMM(P, 3, 1); PDM(FAST, 7); MID; fre = cur + wd * 1024; cur = nextslot(cur); SB(); \
      OMM(P, 3, 2); OMM(P, 3, 3); } while (0)
#define PLOOP(MID0) do { PSTEP(1, 0, 0, false, true, MID0); \
    for (int s = 1; s < 11; s += 2) { PSTEP(0, 1, s, true, true, auxmid(s)); PSTEP(1, 0, s + 1, true, true, (void)0); } \
    PSTEP(0, 1, 11, true, true, POLL_ISSUE); PSTEP(1, 0, 12, true, true, (void)0); \
    PSTEP(0, 1, 13, true, true, { POLL_CHECK; nextd(0); }); PSTEP(1, 0, 14, true, false, nextd(1)); PSTEP(0, 1, 15, true, false, nextd(2)); } while (0)

#define GMM(P, k_, a_, b_) do { if (MM) acc[a_][b_] = mfma16(fa[P][k_][a_], fb[P][k_][b_], acc[a_][b_]); SB(); } while (0)
#define GM2(P, k_, a_) do { GMM(P, k_, a_, 0); GMM(P, k_, a_, 1); } while (0)
#define GSTEP(P, N, J, MM_, DD, MID) do { constexpr bool MM = MM_; \
    if (DW) { if ((DD) == 1) DMX_G(DA); if ((DD) == 2) DMX_ALL(DA); MID; } else { \
    asm volatile("" : "+v"(acc[0][0]), "+v"(acc[0][1]), "+v"(acc[1][0]), "+v"(acc[1][1]), "+v"(acc[2][0]), "+v"(acc[2][1]), "+v"(acc[3][0]), "+v"(acc[3][1])); \
    const unsigned ga_ = (J) < 3 ? gbase[(J) % 3] : 24576u, gb_ = (J) < 3 ? gbase[(J) % 3] + gsub8 : 24576u + gsub32; \
    GM2(P, 0, 0); GLB(N, 0, 0); GLA(N, 0, 0); SB(); GM2(P, 0, 1); GLB(N, 0, 1); GLA(N, 0, 1); SB(); \
    GM2(P, 0, 2); GLA(N, 0, 2); GLA(N, 0, 3); SB(); GM2(P, 0, 3); GLB(N, 1, 0); GLA(N, 1, 0); SB(); \
    GM2(P, 1, 0); GLB(N, 1, 1); GLA(N, 1, 1); SB(); GM2(P, 1, 1); GLA(N, 1, 2); GLA(N, 1, 3); SB(); \
    GM2(P, 1, 2); MID; SB(); GM2(P, 1, 3); } } while (0)
#define GLAST() do { if (!DW) { constexpr bool MM = true; GM2(1, 0, 0); GM2(1, 0, 1); GM2(1, 0, 2); GM2(1, 0, 3); GM2(1, 1, 0); GM2(1, 1, 1); GM2(1, 1, 2); GM2(1, 1, 3); } } while (0)
  const unsigned gtr = (4 * l4 + q) * 128 + (p >> 1) * 16 + (p & 1) * 8, gtf = 2 * (l4 & 1) + (q >> 1);
#define GBAR(N) do { if (DW) VMWAIT(N); LBAR(); } while (0)

  const int hh = w >> 1, rh = w & 1;
  bf16x8 qf[8];
  {
    f32x16 acc[2][2];
    #pragma unroll
    for (int i = 0; i < 2; ++i)
      #pragma unroll
      for (int j = 0; j < 2; ++j) acc[i][j] = f32x16{};
    unsigned aq[2], xrow[2];
    #pragma unroll
    for (int j = 0; j < 2; ++j) {
      aq[j] = (8 * h + q) * 1024 + (16 * hh + 4 * ((2 * rh + j) ^ q) + 2 * gq + (p & 1)) * 16 + (p >> 1) * 8;
      xrow[j] = (unsigned)(((32 * (rh ^ j) + r) ^ h) << 4) + h * 1024 + XO;
    }
    bf16x8 fa[2][2][2], fb[2][2][2];
    u32x4 pf0, pf1;
#define PLB(i_, s_, ks_) lds_b128((xrow[i_] ^ (unsigned)((4 * (((s_) + rq) & 1) + 2 * (ks_)) << 4)) + (4 * (((s_) + rq) & 15) + 2 * (ks_)) * 1024)
#define PX(P, n_) do { } while (0)
#define PLA(j_, ks_) lds_tr2(cur + aq[j_] + (16 * (ks_)) * 1024, cur + aq[j_] + (16 * (ks_) + 4) * 1024)
#define PBASE(s_) ((const char*)A.wq + (size_t)(((s_) + rq) & 15) * (32 * 8192) + g * 1024)
    unsigned pv8[8];
    #pragma unroll
    for (int i = 0; i < 8; ++i) pv8[i] = (4 * i + wd) * 8192u + wsw;
    auto auxmid = [&](int s) {
      if (!DW) { if (s == 3) VMWAIT(0); if (s == 5 && w == 4 && l == 0) flag_store(ws + WS_HFLAG + (rb * 8 + g) * 8, tag); }
    };
#define POLL_ISSUE do { if (!DW) asm volatile("global_load_dwordx4 %0, %2, off sc1\n\tglobal_load_dwordx4 %1, %2, off offset:16 sc1" \
                          : "=&v"(pf0), "=&v"(pf1) : "v"(ws + WS_HFLAG + l * 32) : "memory"); } while (0)
#define POLL_CHECK do { if (!DW) { \
      asm volatile("s_waitcnt vmcnt(0)" : "+v"(pf0), "+v"(pf1) :: "memory"); \
      const bool ok = pf0[0] == tagl && pf0[1] == tagh && pf0[2] == tagl && pf0[3] == tagh && pf1[0] == tagl && pf1[1] == tagh && pf1[2] == tagl && pf1[3] == tagh; \
      if (do_g1 && w == 4) { if (!__all(ok)) poll_flags((const u64*)(ws + WS_HFLAG), 256, tag, l); \
        if (b == 16 && l == 0) flag_store(ws + WS_EPOCH, tag); } } } while (0)
    auto nextd = [&](int n) { if (DW) { if (do_g1) d_g1(DA, n, (1 + n) % 3); else d_kv(DA, n - 1, ((1 + n) % 3) * SLOT); } };
#define PSTEP PSTEP32
    PLOOP(hpub());
#undef PSTEP
#undef PLB
#undef PLA
#undef PBASE
#undef POLL_ISSUE
#undef POLL_CHECK
    { constexpr bool MM = true;
      PMM(1, 0, 1, 0); PMM(1, 0, 1, 1); PMM(1, 1, 1, 0); PMM(1, 1, 1, 1);
      LBAR();
      PMM(1, 0, 0, 0); PMM(1, 0, 0, 1);
      const unsigned xa = XO + w * 4096 + l * 16;
      *(LDS3 bf16x8*)(xa) = pack8<0>(acc[1][0], QSCALE); *(LDS3 bf16x8*)(xa + 1024) = pack8<1>(acc[1][0], QSCALE); SB();
      PMM(1, 1, 0, 0);
      *(LDS3 bf16x8*)(xa + 2048) = pack8<0>(acc[1][1], QSCALE); *(LDS3 bf16x8*)(xa + 3072) = pack8<1>(acc[1][1], QSCALE); SB();
      PMM(1, 1, 0, 1);
      STEPBAR();
      const unsigned xb2 = XO + (w ^ 1) * 4096 + l * 16;
      qf[4] = lds_b128(xb2); qf[5] = lds_b128(xb2 + 1024); qf[6] = lds_b128(xb2 + 2048); qf[7] = lds_b128(xb2 + 3072);
      qf[0] = pack8<0>(acc[0][0], QSCALE); qf[1] = pack8<1>(acc[0][0], QSCALE); qf[2] = pack8<0>(acc[0][1], QSCALE); qf[3] = pack8<1>(acc[0][1], QSCALE);
    }
#undef PX
    asm volatile("" : "+v"(qf[0]), "+v"(qf[1]), "+v"(qf[2]), "+v"(qf[3]), "+v"(qf[4]), "+v"(qf[5]), "+v"(qf[6]), "+v"(qf[7]));
  }
  unsigned fill = 0;

  if (do_g1) {
    f32x4 acc[4][2] = {};
    unsigned aa[4], ba[2];
    #pragma unroll
    for (int a = 0; a < 4; ++a) aa[a] = gtr + 32 * (a ^ gtf);
    #pragma unroll
    for (int a = 0; a < 2; ++a) ba[a] = gtr + 32 * ((2 * (wd & 1) + a) ^ gtf);
    const unsigned gbase[3] = {32768u, 65536u, 0u};
    const unsigned gsub8 = 8192u * (1 + (wd >> 1)), gsub32 = 32768u * (1 + (wd >> 1));
    bf16x8 fa[2][2][4], fb[2][2][2];
#define GLA(N, kk, j_) fa[N][kk][j_] = lds_tr2(ga_ + aa[j_] + (kk) * 4096, ga_ + aa[j_] + (kk) * 4096 + 2048)
#define GLB(N, kk, j_) fb[N][kk][j_] = lds_tr2(gb_ + ba[j_] + (kk) * 4096, gb_ + ba[j_] + (kk) * 4096 + 2048)
#define G1D(t_, j_) do { if (DW) d_g1(DA, t_, j_); } while (0)
    if (DW) { DMX_G(DA); d_g1(DA, 3, 3); }
    GSTEP(1, 0, 0, false, 1, G1D(4, 1));
    GBAR(12); GSTEP(0, 1, 1, true, 1, G1D(5, 2));
    GBAR(12); GSTEP(1, 0, 2, true, 1, G1D(6, 0));
    GBAR(12); GSTEP(0, 1, 3, true, 1, G1D(7, 3));
    for (int t = 4; t < 28; t += 4) {
      GBAR(12); GSTEP(1, 0, 0, true, 1, G1D(t + 4, 1));
      GBAR(12); GSTEP(0, 1, 1, true, 1, G1D(t + 5, 2));
      GBAR(12); GSTEP(1, 0, 2, true, 1, G1D(t + 6, 0));
      GBAR(12); GSTEP(0, 1, 3, true, 1, G1D(t + 7, 3));
    }
    GBAR(12); GSTEP(1, 0, 0, true, 1, if (DW) d_kv(DA, -1, XO));
    GBAR(12); GSTEP(0, 1, 1, true, 2, if (DW) d_kv(DA, 0, XO + SLOT));
    GBAR(14); GSTEP(1, 0, 2, true, 2, if (DW) d_kv(DA, 1, 0));
    constexpr int f2 = DW ? 1 : 0;
    u32x2 bb[2];
    asm volatile("global_load_dwordx2 %0, %2, off\n\tglobal_load_dwordx2 %1, %2, off offset:32"
                 : "=&v"(bb[0]), "=&v"(bb[1]) : "v"(A.b1 + 64 * ft1 + 32 * f2 + 4 * l4) : "memory");
    GBAR(18); GSTEP(0, 1, 3, true, 0, (void)0);
    if (!DW) {
      constexpr bool MM = true;
      GM2(1, 0, 2); GM2(1, 0, 3); GM2(1, 1, 2); GM2(1, 1, 3); GM2(1, 0, 0); GM2(1, 0, 1);
      #pragma unroll
      for (int m = 0; m < 4; ++m) *(LDS3 f32x4*)(SLOT + wd * 4096 + m * 1024 + l * 16) = acc[2 + (m >> 1)][m & 1];
      SB(); GM2(1, 1, 0); GM2(1, 1, 1);
    }
    asm volatile("s_waitcnt vmcnt(0)" : "+v"(bb[0]), "+v"(bb[1]) :: "memory");
    LBAR();
#undef GLA
#undef GLB
    if (DW) {
      #pragma unroll
      for (int m = 0; m < 4; ++m) acc[m >> 1][m & 1] = *(LDS3 f32x4*)(SLOT + wd * 4096 + m * 1024 + l * 16);
    }
    #pragma unroll
    for (int m = 0; m < 4; ++m) {
      f32x4& v = acc[m >> 1][m & 1];
      v[0] += bflo(bb[m >> 1][0]); v[1] += bfhi(bb[m >> 1][0]); v[2] += bflo(bb[m >> 1][1]); v[3] += bfhi(bb[m >> 1][1]);
      #pragma unroll
      for (int e = 0; e < 4; ++e) {
        const float u = v[e];
        const float z = u * (1.f + 0.044715f * u * u) * (-2.f * 0.7978845608028654f * 1.4426950408889634f);
        v[e] = u * __builtin_amdgcn_rcpf(1.f + __builtin_amdgcn_exp2f(z));
      }
    }
    char* up = ws + WS_U + (size_t)(2 * dp1 + (wd >> 1)) * (224 * 1024) + (size_t)(ft1 * 8 + f2 * 4 + l4) * 1024 + (32 * (wd & 1) + (l & 15)) * 16;
    store16_sc1(up, pack44(acc[0][0], acc[1][0]));
    store16_sc1(up + 256, pack44(acc[0][1], acc[1][1]));
    cur = XO;
  }

  {
    f32x16 o[4];
    f32x16 negm, sA, sB;
    float lsum = 0.f;
    unsigned ka[8];
    #pragma unroll
    for (int kk = 0; kk < 8; ++kk) ka[kk] = (r * 256 + ((h ^ (r & 15)) << 4)) ^ (unsigned)((kk ^ (4 * rh)) << 5);
    unsigned va[4];
    #pragma unroll
    for (int d = 0; d < 4; ++d) va[d] = 16384 + (4 * h + q) * 256 + (4 * (d ^ q) + 2 * gq + (p >> 1)) * 16 + (p & 1) * 8;
    bf16x8 kf[8], vf[8];
    u32x4 pw0 = {}, pw1 = {};
    float mx;
    unsigned kc_[8], vc_[4];
#define SETADDR() do { _Pragma("unroll") for (int i_ = 0; i_ < 8; ++i_) { kc_[i_] = cur + ka[i_]; } _Pragma("unroll") for (int i_ = 0; i_ < 4; ++i_) { vc_[i_] = cur + va[i_]; } } while (0)
#define LDK(i, KB) kf[i] = lds_b128(kc_[i] + (KB) * 8192)
#define LDV(i, VP) vf[i] = lds_tr2(vc_[(i) & 3] + ((VP) * 32 + 16 * ((i) >> 2)) * 256, vc_[(i) & 3] + ((VP) * 32 + 16 * ((i) >> 2) + 8) * 256)
#define EXP(Y, e) do { Y[e] = __builtin_amdgcn_exp2f(Y[e]); } while (0)
#define ADD(Y, e) do { lsum += Y[e]; } while (0)
#define CVT(P, Y, c) do { P[(c) & 3] = pk2(Y[2 * (c)], Y[2 * (c) + 1]); } while (0)
#define MX(X, e) do { mx = fmaxf(fmaxf(mx, X[2 * (e)]), X[2 * (e) + 1]); } while (0)
#define QK(X, i) do { X = mfma(kf[i], qf[i], (i) == 0 ? negm : X); } while (0)
#define PV(i) do { o[(i) & 3] = mfma(vf[i], __builtin_bit_cast(bf16x8, (i) < 4 ? pw0 : pw1), o[(i) & 3]); } while (0)
#define DMAC(C, i) do { if (C) DMA(i); } while (0)
#define STAGE_A(X, Y, KB, VP, DC) do { \
      QK(X, 0); LDK(6, KB); EXP(Y, 0); EXP(Y, 1); DMAC(DC, 3); SB(); \
      QK(X, 1); LDK(7, KB); EXP(Y, 2); CVT(pw0, Y, 0); DMAC(DC, 4); SB(); \
      QK(X, 2); LDV(0, VP); EXP(Y, 3); EXP(Y, 4); DMAC(DC, 5); SB(); \
      QK(X, 3); LDV(1, VP); EXP(Y, 5); ADD(Y, 0); CVT(pw0, Y, 1); DMAC(DC, 6); SB(); \
      QK(X, 4); LDV(2, VP); EXP(Y, 6); EXP(Y, 7); CVT(pw0, Y, 2); DMAC(DC, 7); SB(); \
      QK(X, 5); LDV(3, VP); EXP(Y, 8); ADD(Y, 1); ADD(Y, 2); CVT(pw0, Y, 3); SB(); \
      QK(X, 6); LDV(4, VP); EXP(Y, 9); EXP(Y, 10); ADD(Y, 3); SB(); \
      QK(X, 7); LDV(5, VP); EXP(Y, 11); ADD(Y, 4); CVT(pw1, Y, 4); SB(); \
    } while (0)
#define RESCALE(X) do { \
      if (__any(mx > THR)) { \
        const float delta = fmaxf(mx, 0.f); \
        const float alpha = __builtin_amdgcn_exp2f(-delta); \
        _Pragma("unroll") for (int e = 0; e < 16; ++e) { X[e] -= delta; negm[e] -= delta; } \
        _Pragma("unroll") for (int d = 0; d < 4; ++d) _Pragma("unroll") for (int e = 0; e < 16; ++e) o[d][e] *= alpha; \
        lsum *= alpha; \
      } } while (0)
#define STAGE_B(X, Y, VP, KBN) do { \
      PV(0); LDV(6, VP); EXP(Y, 12); EXP(Y, 13); CVT(pw1, Y, 5); SB(); \
      PV(1); LDV(7, VP); EXP(Y, 14); EXP(Y, 15); ADD(Y, 5); SB(); \
      PV(2); LDK(0, KBN); CVT(pw1, Y, 6); CVT(pw1, Y, 7); ADD(Y, 6); mx = -3.0e38f; MX(X, 0); SB(); \
      PV(3); LDK(1, KBN); MX(X, 1); MX(X, 2); ADD(Y, 7); ADD(Y, 8); SB(); \
      PV(4); LDK(2, KBN); MX(X, 3); MX(X, 4); ADD(Y, 9); ADD(Y, 10); SB(); \
      PV(5); LDK(3, KBN); MX(X, 5); MX(X, 6); ADD(Y, 11); ADD(Y, 12); SB(); \
      PV(6); LDK(4, KBN); MX(X, 7); ADD(Y, 13); ADD(Y, 14); SB(); \
      PV(7); LDK(5, KBN); mx = half_max(mx); ADD(Y, 15); SB(); \
      RESCALE(X); SB(); \
    } while (0)
#define STAGE_B_BAR(X, Y, VP, WAITS, SETD, POST) do { \
      PV(0); LDV(6, VP); EXP(Y, 12); EXP(Y, 13); CVT(pw1, Y, 5); SB(); \
      PV(1); LDV(7, VP); EXP(Y, 14); EXP(Y, 15); ADD(Y, 5); SB(); \
      PV(2); CVT(pw1, Y, 6); CVT(pw1, Y, 7); ADD(Y, 6); mx = -3.0e38f; MX(X, 0); SB(); \
      cur = next5(cur); \
      PV(3); SETADDR(); SETD; SB(); \
      PV(4); SB(); \
      WAITS; LBAR(); PRIO(3); \
      PV(5); SB(); LDK(0, 0); LDK(1, 0); LDK(2, 0); LDK(3, 0); MX(X, 1); MX(X, 2); MX(X, 3); MX(X, 4); ADD(Y, 7); ADD(Y, 8); ADD(Y, 9); POST; DMA(0); SB(); \
      PV(6); LDK(4, 0); MX(X, 5); MX(X, 6); MX(X, 7); ADD(Y, 10); ADD(Y, 11); ADD(Y, 12); DMA(1); SB(); \
      PV(7); LDK(5, 0); mx = half_max(mx); ADD(Y, 13); ADD(Y, 14); ADD(Y, 15); DMA(2); SB(); \
      RESCALE(X); SB(); \
    } while (0)


    SETADDR();
    #pragma unroll
    for (int i = 0; i < 8; ++i) LDK(i, 0);
    negm = f32x16{};
    #pragma unroll
    for (int d = 0; d < 4; ++d) o[d] = f32x16{};
    SB();
    QK(sA, 0); LDK(0, 1); DMA(0); SB(); QK(sA, 1); LDK(1, 1); DMA(1); SB(); QK(sA, 2); LDK(2, 1); DMA(2); SB(); QK(sA, 3); LDK(3, 1); SB();
    QK(sA, 4); LDK(4, 1); SB(); QK(sA, 5); LDK(5, 1); SB(); QK(sA, 6); SB(); QK(sA, 7); SB();
    mx = -3.0e38f;
    #pragma unroll
    for (int e = 0; e < 8; ++e) MX(sA, e);
    mx = half_max(mx);
    #pragma unroll
    for (int e = 0; e < 16; ++e) { sA[e] -= mx; negm[e] = -mx; }
    SB();
    STAGE_A(sB, sA, 1, 1, true);
    fill = SLOT;
    STAGE_B_BAR(sB, sA, 1, VMWAIT(0), if (DW) d_kv(DA, 2, fill),
                if (!DW && do_g1 && w == 4 && l == 0) flag_store(ws + WS_UFLAG + (dp1 * 28 + ft1) * 8, tag));
    for (int u = 0; u < 7; ++u) {
      STAGE_A(sA, sB, 0, 0, true);
      PRIO(2);
      STAGE_B(sA, sB, 0, 1);
      PRIO(1);
      STAGE_A(sB, sA, 1, 1, false);
      PRIO(0);
      fill = nextslot(fill);
      STAGE_B_BAR(sB, sA, 1, if (DW) VMWAIT(8), if (DW) { if (u + 3 < 8) d_kv(DA, u + 3, fill); else d_wo(DA, u - 5, fill); }, (void)0);
    }
    PRIO(0);
    #pragma unroll
    for (int i = 0; i < 8; ++i) LDV(i, 0);
    DMA(3); DMA(4);
    #pragma unroll
    for (int e = 0; e < 16; ++e) { EXP(sB, e); ADD(sB, e); }
    #pragma unroll
    for (int c = 0; c < 4; ++c) { CVT(pw0, sB, c); CVT(pw1, sB, 4 + c); }
    SB();
    fre = cur + wd * 1024; cur = nextslot(cur);
    const float inv = __builtin_amdgcn_rcpf(half_sum(lsum));
    const unsigned ob = XO + (hh * 16 + h) * 1024 + (32 * rh + r) * 16;
#define OWR(d) do { *(LDS3 bf16x8*)(ob + (4 * (d)) * 1024) = pack8<0>(o[d], inv); *(LDS3 bf16x8*)(ob + (4 * (d) + 2) * 1024) = pack8<1>(o[d], inv); } while (0)
    PV(0); PV(4); DMA(5); SB(); PV(1); PV(5); DMA(6); SB(); PV(2); OWR(0); SB(); PV(6); DMA(7); SB(); PV(3); OWR(1); SB(); PV(7); SB(); OWR(2); OWR(3);
  }

  {
    f32x4 acc[4][4] = {};
    unsigned oa[4];
    #pragma unroll
    for (int j = 0; j < 4; ++j)
      oa[j] = (16 * (l4 >> 1) + 4 * (l4 & 1) + q) * 1024 + (16 * (w >> 1) + 4 * ((2 * (w & 1) + (j >> 1)) ^ q) + 2 * ((j & 1) ^ (l4 & 1)) + (p >> 1)) * 16 + (p & 1) * 8;
    const unsigned ob = XO + l4 * 1024 + (l & 15) * 16;
    bf16x8 fa[2][4], fb[2][4];
    u32x2 pfl;
#define PLB(i_, s_) lds_b128(ob + (((s_) + ro) & 15) * 4096 + 256 * (i_))
#define PLA(j_) lds_tr2(cur + oa[j_], cur + oa[j_] + 8192)
#define PBASE(s_) ((const char*)A.wo + (size_t)(16 * g + (((s_) + ro) & 15)) * 32768)
    unsigned pv8[8];
    #pragma unroll
    for (int i = 0; i < 8; ++i) pv8[i] = (4 * i + wd) * 1024u + (wsw ^ ((i & 1) << 5));
    auto auxmid = [&](int) {};
#define PX(P, n_) do { } while (0)
#define POLL_ISSUE do { if (!DW) asm volatile("global_load_dwordx2 %0, %1, off sc1" : "=&v"(pfl) : "v"(ws + WS_UFLAG + ((dt2 >> 1) * 28 + (l < 28 ? l : 0)) * 8) : "memory"); } while (0)
#define POLL_CHECK do { if (!DW) { asm volatile("s_waitcnt vmcnt(0)" : "+v"(pfl) :: "memory"); \
      if (do_g2 && w == 4 && !__all(pfl[0] == tagl && pfl[1] == tagh)) poll_flags((const u64*)(ws + WS_UFLAG) + (dt2 >> 1) * 28, 28, tag, l); } } while (0)
    auto nextd = [&](int n) { if (DW) { if (do_g2) d_g2(DA, n, (2 + n) % 3); else d_wo(DA, n, ((2 + n) % 3) * SLOT); } };
#define PSTEP PSTEP16
    PLOOP((void)0);
#undef PSTEP
#undef PLB
#undef PLA
#undef PBASE
#undef POLL_ISSUE
#undef POLL_CHECK
    char* pp = ws + WS_PART + ((size_t)(rb * 8 + w) * 8 + g) * 8192 + l * 16;
    { constexpr bool MM = true;
      OMM(1, 0, 0); OMM(1, 1, 0); OMM(1, 2, 0); OMM(1, 3, 0); OMM(1, 0, 1); OMM(1, 1, 1); OMM(1, 2, 1); OMM(1, 3, 1);
      store16_sc1(pp, pack44(acc[0][0], acc[1][0])); store16_sc1(pp + 1024, pack44(acc[2][0], acc[3][0])); SB();
      OMM(1, 0, 2); OMM(1, 1, 2); OMM(1, 2, 2); OMM(1, 3, 2);
      store16_sc1(pp + 2048, pack44(acc[0][1], acc[1][1])); store16_sc1(pp + 3072, pack44(acc[2][1], acc[3][1])); SB();
      OMM(1, 0, 3); OMM(1, 1, 3); OMM(1, 2, 3); OMM(1, 3, 3);
      store16_sc1(pp + 4096, pack44(acc[0][2], acc[1][2])); store16_sc1(pp + 5120, pack44(acc[2][2], acc[3][2]));
      store16_sc1(pp + 6144, pack44(acc[0][3], acc[1][3])); store16_sc1(pp + 7168, pack44(acc[2][3], acc[3][3]));
    }
    VMWAIT(0);
    if (l == 0) flag_store(ws + WS_SFLAG + ((rb * 8 + w) * 8 + g) * 8, tag);
    if (do_g2) LBAR();
  }

  if (do_g2) {
    const int wi = (w >> 1) & 1, wj = w & 1, sb2 = DW ? 1 : 0;
    f32x4 acc[4][2] = {};
    const unsigned aa = l4 * 1024 + (l & 15) * 16;
    unsigned ba[2];
    #pragma unroll
    for (int a = 0; a < 2; ++a) ba[a] = gtr + 32 * ((2 * (wd & 1) + a) ^ gtf);
    const unsigned gbase[3] = {65536u, 0u, 32768u};
    const unsigned gsub8 = 8192u * (1 + (wd >> 1)), gsub32 = 32768u * (1 + (wd >> 1));
    bf16x8 fa[2][2][4], fb[2][2][2];
    const int srow = 64 * (2 * pp2 + sb2) + 32 * wi + (l & 15);
    const size_t xo = (size_t)srow * 512 + 64 * dt2 + 32 * wj + 16 * (l4 & 1) + 8 * (l4 >> 1);
    const char* pp = ws + WS_PART + (size_t)((2 * pp2 + sb2) * 8 + dt2) * 65536 + (wi * 4 + wj) * 1024 + l * 16;
    const u64* sfl = (const u64*)(ws + WS_SFLAG) + ((2 * pp2 + sb2) * 8 + dt2) * 8;
    u32x4 pv[16];
    u32x2 pfl;
#define GLA(N, kk, j_) fa[N][kk][j_] = lds_b128(ga_ + aa + (kk) * 4096 + 256 * (j_))
#define GLB(N, kk, j_) fb[N][kk][j_] = lds_tr2(gb_ + ba[j_] + (kk) * 4096, gb_ + ba[j_] + (kk) * 4096 + 2048)
#define PLD(c) asm volatile("global_load_dwordx4 %0, %1, off sc1" : "=v"(pv[c]) : "v"(pp + ((c) >> 1) * 8192 + ((c) & 1) * 2048) : "memory")
#define G2D(t_, j_, AUX) do { if (DW) d_g2(DA, t_, j_); else { AUX; } } while (0)
    if (DW) { DMX_G(DA); d_g2(DA, 3, 3); }
    GSTEP(1, 0, 0, false, 1, G2D(4, 2, (void)0));
    GBAR(12); GSTEP(0, 1, 1, true, 1, G2D(5, 0, (void)0));
    GBAR(12); GSTEP(1, 0, 2, true, 1, G2D(6, 1, (void)0));
    GBAR(12); GSTEP(0, 1, 3, true, 1, G2D(7, 3, (void)0));
    for (int t = 4; t < 12; t += 4) {
      GBAR(12); GSTEP(1, 0, 0, true, 1, G2D(t + 4, 2, (void)0));
      GBAR(12); GSTEP(0, 1, 1, true, 1, G2D(t + 5, 0, (void)0));
      GBAR(12); GSTEP(1, 0, 2, true, 1, G2D(t + 6, 1, (void)0));
      GBAR(12); GSTEP(0, 1, 3, true, 1, G2D(t + 7, 3, (void)0));
    }
    GBAR(12); GSTEP(1, 0, 0, true, 1, G2D(16, 2, asm volatile("global_load_dwordx2 %0, %1, off sc1" : "=&v"(pfl) : "v"(sfl + (l & 7)) : "memory")));
    GBAR(12); GSTEP(0, 1, 1, true, 1, G2D(17, 0, (void)0));
    GBAR(12); GSTEP(1, 0, 2, true, 1, G2D(18, 1, (void)0));
    GBAR(12); GSTEP(0, 1, 3, true, 1, G2D(19, 3, { asm volatile("s_waitcnt vmcnt(0)" : "+v"(pfl) :: "memory");
                                                  if (!__all(pfl[0] == tagl && pfl[1] == tagh)) poll_flags(sfl, 8, tag, l); PLD(0); PLD(1); }));
    GBAR(12); GSTEP(1, 0, 0, true, 1, G2D(20, 2, { PLD(2); PLD(3); }));
    GBAR(12); GSTEP(0, 1, 1, true, 1, G2D(21, 0, { PLD(4); PLD(5); }));
    GBAR(12); GSTEP(1, 0, 2, true, 1, G2D(22, 1, { PLD(6); PLD(7); }));
    GBAR(12); GSTEP(0, 1, 3, true, 1, G2D(23, 3, { PLD(8); PLD(9); }));
    GBAR(12); GSTEP(1, 0, 0, true, 1, G2D(24, 2, { PLD(10); PLD(11); }));
    GBAR(12); GSTEP(0, 1, 1, true, 1, G2D(25, 0, { PLD(12); PLD(13); }));
    GBAR(12); GSTEP(1, 0, 2, true, 1, G2D(26, 1, { PLD(14); PLD(15); }));
    GBAR(12); GSTEP(0, 1, 3, true, 1, G2D(27, 3, (void)0));
    GBAR(12); GSTEP(1, 0, 0, true, 1, (void)0);
    u32x4 xv0, xv1; unsigned b2r, b2s;
    asm volatile("global_load_dwordx4 %0, %4, off\n\tglobal_load_dwordx4 %1, %5, off\n\tglobal_load_ushort %2, %6, off\n\tglobal_load_ushort %3, %6, off offset:32"
                 : "=&v"(xv0), "=&v"(xv1), "=&v"(b2r), "=&v"(b2s) : "v"(x + xo), "v"(x + xo + 16 * 512), "v"(A.b2 + srow) : "memory");
    GBAR(16); GSTEP(0, 1, 1, true, 0, (void)0);
    GBAR(10); GSTEP(1, 0, 2, true, 0, (void)0);
    GBAR(4); GSTEP(0, 1, 3, true, 0, (void)0);
    GLAST();
#undef GLA
#undef GLB
    if (!DW) {
      #pragma unroll
      for (int m = 0; m < 8; ++m) *(LDS3 f32x4*)(XO + wd * 8192 + m * 1024 + l * 16) = acc[m >> 1][m & 1];
    }
    LBAR();
    f32x16 yv;
    #pragma unroll
    for (int m = 0; m < 4; ++m) {
      const f32x4 v4 = *(LDS3 f32x4*)(XO + (sb2 * 2 + wi) * 8192 + ((2 * wj + (m & 1)) * 2 + (m >> 1)) * 1024 + l * 16);
      yv[4 * m] = v4[0]; yv[4 * m + 1] = v4[1]; yv[4 * m + 2] = v4[2]; yv[4 * m + 3] = v4[3];
    }
    asm volatile("s_waitcnt vmcnt(0)" : "+v"(xv0), "+v"(xv1), "+v"(b2r), "+v"(b2s) :: "memory");
    if (!DW) asm volatile("" : "+v"(pv[0]), "+v"(pv[1]), "+v"(pv[2]), "+v"(pv[3]), "+v"(pv[4]), "+v"(pv[5]), "+v"(pv[6]), "+v"(pv[7]),
                 "+v"(pv[8]), "+v"(pv[9]), "+v"(pv[10]), "+v"(pv[11]), "+v"(pv[12]), "+v"(pv[13]), "+v"(pv[14]), "+v"(pv[15]) :: "memory");
    #pragma unroll
    for (int e = 0; e < 16; ++e) {
      const float yy = yv[e] + bflo(e < 8 ? b2r : b2s);
      yv[e] = yy * __builtin_amdgcn_rcpf(1.f + __builtin_amdgcn_exp2f(-1.4426950408889634f * yy));
    }
    asm volatile("" : "+v"(yv));
    float t[16];
    #pragma unroll
    for (int e = 0; e < 16; ++e) t[e] = 0.f;
    auto accum = [&](const u32x4& a, const u32x4& c) {
      #pragma unroll
      for (int e = 0; e < 4; ++e) { t[2 * e] += bflo(a[e]); t[2 * e + 1] += bfhi(a[e]); t[8 + 2 * e] += bflo(c[e]); t[8 + 2 * e + 1] += bfhi(c[e]); }
    };
    if (DW) {
      unsigned done = 0;
      const char* pb = ws + WS_PART + (size_t)((2 * pp2 + 1) * 8 + dt2) * 65536 + (wi * 4 + wj) * 1024;
      for (unsigned spins = 0; done != 0xffu && spins < (1u << 20); ++spins) {
        const u64 f = __hip_atomic_load((GAS const u64*)(sfl + (l & 7)), __ATOMIC_RELAXED, __HIP_MEMORY_SCOPE_AGENT);
        unsigned nw = (unsigned)__ballot(f == tag) & 0xffu & ~done;
        done |= nw;
        if (!nw) __builtin_amdgcn_s_sleep(1);
        while (nw) {
          const int gg = __builtin_ctz(nw); nw &= nw - 1;
          dma16o<0>(pb + gg * 8192, l * 16u, wd * 16384 + gg * 2048);
          dma16o<1024>(pb + gg * 8192 + 2048, l * 16u, wd * 16384 + gg * 2048);
        }
      }
      VMWAIT(0);
      #pragma unroll
      for (int c = 0; c < 16; ++c) pv[c] = *(LDS3 u32x4*)(wd * 16384 + c * 1024 + l * 16);
    }
    #pragma unroll
    for (int gg = 0; gg < 8; ++gg) accum(pv[2 * gg], pv[2 * gg + 1]);
    #pragma unroll
    for (int c = 0; c < 2; ++c) {
      #pragma unroll
      for (int e = 0; e < 8; ++e) t[8 * c + e] *= yv[8 * c + e];
      #pragma unroll
      for (int e = 0; e < 4; ++e) {
        auto rr = __builtin_amdgcn_permlane16_swap(__float_as_uint(t[8 * c + e]), __float_as_uint(t[8 * c + 4 + e]), false, false);
        t[8 * c + e] = __uint_as_float(rr[0]); t[8 * c + 4 + e] = __uint_as_float(rr[1]);
      }
      const u32x4 xv = c ? xv1 : xv0;
      u32x4 ov;
      #pragma unroll
      for (int e = 0; e < 4; ++e) ov[e] = pk2(t[8 * c + 2 * e] + bflo(xv[e]), t[8 * c + 2 * e + 1] + bfhi(xv[e]));
      *(u32x4*)(A.out + xo + 16 * 512 * c) = ov;
    }
  }
}

__global__ __launch_bounds__(512, 2) void fused_kernel(Args A) {
  extern __shared__ __attribute__((aligned(16))) char lds_raw[];
  const int l = threadIdx.x & 63;
  const int w = __builtin_amdgcn_readfirstlane(threadIdx.x >> 6);
  if (w < 4) body<true>(A, w, l); else body<false>(A, w, l);
}

extern "C" void kernel_launch(void* const* d_in, const int* in_sizes, int n_in, void* d_out, int out_size, void* d_ws, size_t ws_size, hipStream_t stream) {
  (void)hipFuncSetAttribute((const void*)fused_kernel, hipFuncAttributeMaxDynamicSharedMemorySize, LDS_BYTES);
  Args A;
  A.x = (const __bf16*)d_in[0]; A.kc = (const __bf16*)d_in[1]; A.vc = (const __bf16*)d_in[2]; A.wq = (const __bf16*)d_in[3]; A.wo = (const __bf16*)d_in[4];
  A.w1 = (const __bf16*)d_in[6]; A.b1 = (const __bf16*)d_in[7]; A.w2 = (const __bf16*)d_in[8]; A.b2 = (const __bf16*)d_in[9];
  A.out = (__bf16*)d_out; A.ws = (char*)d_ws;
  fused_kernel<<<256, 512, LDS_BYTES, stream>>>(A);
}
```

```cpp
#include <hip/hip_runtime.h>
#include <stdint.h>
#pragma clang diagnostic ignored "-Wint-to-pointer-cast"

typedef __bf16 bf16x8 __attribute__((ext_vector_type(8)));
typedef __bf16 bf16x4 __attribute__((ext_vector_type(4)));
typedef __bf16 bf16x2 __attribute__((ext_vector_type(2)));
typedef float f32x16 __attribute__((ext_vector_type(16)));
typedef float f32x4 __attribute__((ext_vector_type(4)));
typedef float f32x2 __attribute__((ext_vector_type(2)));
typedef unsigned u32x4 __attribute__((ext_vector_type(4)));
typedef unsigned u32x2 __attribute__((ext_vector_type(2)));
typedef unsigned long long u64;
#define LDS3 __attribute__((address_space(3)))
#define GAS __attribute__((address_space(1)))
#define DI __device__ __forceinline__
#define SB() __builtin_amdgcn_sched_barrier(0)
#define PRIO(n) __builtin_amdgcn_s_setprio(n)

constexpr int SLOT = 32768;
constexpr int XO = 98304;
constexpr int LDS_BYTES = 163840;
constexpr size_t WS_EPOCH = 0;
constexpr size_t WS_HFLAG = 4096;
constexpr size_t WS_UFLAG = 8192;
constexpr size_t WS_SFLAG = 16384;
constexpr size_t WS_H     = 1u << 20;
constexpr size_t WS_U     = 4u << 20;
constexpr size_t WS_PART  = 8u << 20;

constexpr float THR = 16.0f;
constexpr float QSCALE = 0.08838834764831845f * 1.4426950408889634f;

DI bf16x8 lds_b128(unsigned a) { return *(LDS3 bf16x8*)a; }
DI bf16x8 lds_tr2(unsigned a0, unsigned a1) {
  bf16x4 lo = __builtin_amdgcn_ds_read_tr16_b64_v4bf16((LDS3 bf16x4*)a0);
  bf16x4 hi = __builtin_amdgcn_ds_read_tr16_b64_v4bf16((LDS3 bf16x4*)a1);
  return __builtin_shufflevector(lo, hi, 0, 1, 2, 3, 4, 5, 6, 7);
}
DI f32x16 mfma(bf16x8 a, bf16x8 b, f32x16 c) { return __builtin_amdgcn_mfma_f32_32x32x16_bf16(a, b, c, 0, 0, 0); }
DI f32x4 mfma16(bf16x8 a, bf16x8 b, f32x4 c) { return __builtin_amdgcn_mfma_f32_16x16x32_bf16(a, b, c, 0, 0, 0); }
DI unsigned pk2(float lo, float hi) { f32x2 v = {lo, hi}; bf16x2 b = __builtin_convertvector(v, bf16x2); return __builtin_bit_cast(unsigned, b); }
template <int S> DI bf16x8 pack8(const f32x16& x, float sc) {
  u32x4 p;
  p[0] = pk2(x[8 * S + 0] * sc, x[8 * S + 1] * sc); p[1] = pk2(x[8 * S + 2] * sc, x[8 * S + 3] * sc);
  p[2] = pk2(x[8 * S + 4] * sc, x[8 * S + 5] * sc); p[3] = pk2(x[8 * S + 6] * sc, x[8 * S + 7] * sc);
  return __builtin_bit_cast(bf16x8, p);
}
template <int S> DI bf16x8 pack8n(const f32x16& x) {
  u32x4 p;
  p[0] = pk2(x[8 * S + 0], x[8 * S + 1]); p[1] = pk2(x[8 * S + 2], x[8 * S + 3]);
  p[2] = pk2(x[8 * S + 4], x[8 * S + 5]); p[3] = pk2(x[8 * S + 6], x[8 * S + 7]);
  return __builtin_bit_cast(bf16x8, p);
}
DI u32x4 pack44(const f32x4& a, const f32x4& c);
DI float bflo(unsigned u) { return __uint_as_float(u << 16); }
DI float bfhi(unsigned u) { return __uint_as_float(u & 0xffff0000u); }
template <int C> DI float dadd(float v) { return v + __builtin_bit_cast(float, __builtin_amdgcn_update_dpp(0, __builtin_bit_cast(int, v), C, 0xf, 0xf, true)); }
DI float dot_one(unsigned u, float c) { return __builtin_amdgcn_fdot2_f32_bf16(__builtin_bit_cast(bf16x2, u), __builtin_bit_cast(bf16x2, 0x3f803f80u), c, false); }
DI float dot_sq(unsigned u, float c) { return __builtin_amdgcn_fdot2_f32_bf16(__builtin_bit_cast(bf16x2, u), __builtin_bit_cast(bf16x2, u), c, false); }
DI float half_sum(float v) {
  auto b = __builtin_amdgcn_permlane32_swap(__float_as_uint(v), __float_as_uint(v), false, false);
  return __uint_as_float(b[0]) + __uint_as_float(b[1]);
}
DI float half_max(float v) {
  auto b = __builtin_amdgcn_permlane32_swap(__float_as_uint(v), __float_as_uint(v), false, false);
  return fmaxf(fmaxf(-3.0e38f, __uint_as_float(b[0])), __uint_as_float(b[1]));
}
DI u32x4 pack44(const f32x4& a, const f32x4& c) { u32x4 p; p[0] = pk2(a[0], a[1]); p[1] = pk2(a[2], a[3]); p[2] = pk2(c[0], c[1]); p[3] = pk2(c[2], c[3]); return p; }
DI void store16_sc1(void* p, u32x4 v) {
  asm volatile("global_store_dwordx4 %0, %1, off sc1\n\ts_nop 1" :: "v"(p), "v"(v) : "memory");
}
#define VMWAIT(N) asm volatile("s_waitcnt vmcnt(" #N ")" ::: "memory")
#define LBAR() do { asm volatile("" ::: "memory"); __builtin_amdgcn_s_waitcnt(0xC07F); __builtin_amdgcn_s_barrier(); asm volatile("" ::: "memory"); } while (0)
#define STEPBAR() do { if (DW) VMWAIT(8); LBAR(); } while (0)

DI void poll_flags(const u64* f, int nflags, u64 tag, int lane) {
  for (unsigned spins = 0; spins < (1u << 20); ++spins) {
    bool ok = true;
    for (int i = lane; i < nflags; i += 64)
      ok &= (__hip_atomic_load((GAS const u64*)(f + i), __ATOMIC_RELAXED, __HIP_MEMORY_SCOPE_AGENT) == tag);
    if (__all(ok)) break;
    __builtin_amdgcn_s_sleep(2);
  }
  asm volatile("" ::: "memory");
}
DI void flag_store(char* p, u64 tag) { __hip_atomic_store((GAS u64*)p, tag, __ATOMIC_RELAXED, __HIP_MEMORY_SCOPE_AGENT); }


struct Args {
  const __bf16* x; const __bf16* kc; const __bf16* vc; const __bf16* wq; const __bf16* wo; const __bf16* w1;
  const __bf16* b1; const __bf16* w2; const __bf16* b2; __bf16* out; char* ws;
};
struct Desc { const char* p[4]; unsigned lds[4]; unsigned v[4]; };

template <int OFF> DI void dma16p(const void* sbase, unsigned voff, unsigned lds) {
  asm volatile("s_add_u32 m0, %2, %3\n\ts_nop 0\n\tglobal_load_lds_dwordx4 %0, %1" :: "v"(voff), "s"(sbase), "s"(__builtin_amdgcn_readfirstlane(lds)), "i"(OFF) : "memory", "scc");
}
template <int OFF> DI void dma16o(const void* sbase, unsigned voff, unsigned lds) {
  asm volatile("s_add_u32 m0, %2, %3\n\ts_nop 0\n\tglobal_load_lds_dwordx4 %0, %1 sc1" :: "v"(voff), "s"(sbase), "s"(__builtin_amdgcn_readfirstlane(lds)), "i"(OFF) : "memory", "scc");
}
template <bool DW>
DI void body(const Args& A, const int w, const int l) {
  const __bf16* __restrict__ x = A.x; char* __restrict__ ws = A.ws;
  const int wd = w & 3;
  const int b = blockIdx.x, xcd = b & 7, idx = b >> 3;
  const int pidx = ((idx >> 2) << 1) | (idx & 1);
  const int g = 2 * (xcd & 3) + (idx & 1), rb = 16 * (xcd >> 2) + (idx >> 1);
  const int r = l & 31, h = l >> 5, gq = (l >> 4) & 1, q = (l >> 2) & 3, p = l & 3;
  const unsigned l4 = l >> 4;

  const int xrow8 = 8 * w + (l >> 3);
  const int hrow = 64 * rb + 8 * g + 2 * wd + h;
  u32x4 xr[8], hr[2];
  u32x2 ep2;
  {
    const char* xb = (const char*)x + (size_t)(64 * rb + xrow8) * 1024 + (l & 7) * 16;
    asm volatile("global_load_dwordx4 %0, %9, off\n\tglobal_load_dwordx4 %1, %9, off offset:128\n\tglobal_load_dwordx4 %2, %9, off offset:256\n\t"
                 "global_load_dwordx4 %3, %9, off offset:384\n\tglobal_load_dwordx4 %4, %9, off offset:512\n\tglobal_load_dwordx4 %5, %9, off offset:640\n\t"
                 "global_load_dwordx4 %6, %9, off offset:768\n\tglobal_load_dwordx4 %7, %9, off offset:896\n\tglobal_load_dwordx2 %8, %10, off sc1"
                 : "=&v"(xr[0]), "=&v"(xr[1]), "=&v"(xr[2]), "=&v"(xr[3]), "=&v"(xr[4]), "=&v"(xr[5]), "=&v"(xr[6]), "=&v"(xr[7]), "=&v"(ep2)
                 : "v"(xb), "v"(ws + WS_EPOCH) : "memory");
    if (!DW) asm volatile("global_load_dwordx4 %0, %2, off\n\tglobal_load_dwordx4 %1, %2, off offset:512"
                 : "=&v"(hr[0]), "=&v"(hr[1]) : "v"((const char*)x + (size_t)hrow * 1024 + r * 16) : "memory");
  }
  const int rq = rb & 15, rt = pidx >> 1, ro = pidx & 14;
  const bool do_g2 = (rb & 1) == 0, do_g1 = !do_g2 && pidx < 14;
  const int ft1 = 7 * (xcd >> 1) + (pidx >> 1), dp1 = 2 * (xcd & 1) + (pidx & 1);
  const int pp2 = 4 * (xcd >> 1) + (pidx >> 2), dt2 = 4 * (xcd & 1) + (pidx & 3);
  const int r1 = (16 * (pidx & 1) + 4 * (pidx >> 1) + (pidx >> 2)) & 31;
  const int r2 = (7 * (pidx & 3) + 7 * (pidx >> 2) / 4 + 2 * (pidx >> 2)) % 28;
  const unsigned wsw = (unsigned)((l ^ (wd << 2)) << 4);
  const unsigned gsw = (unsigned)(((l & 7) ^ (((l >> 4) & 3) << 1)) << 4);
  const unsigned grow = 8 * wd + (l >> 3);
  Desc DA;
  auto d_lds = [&](Desc& D, unsigned slot) { D.lds[0] = slot + wd * 1024; D.lds[1] = D.lds[0] + 8192; D.lds[2] = D.lds[0] + 16384; D.lds[3] = D.lds[0] + 24576; };
  auto d_ldsg = [&](Desc& D, int j) { const unsigned b0 = j < 3 ? j * 32768u : 24576u, st = j < 3 ? 8192u : 32768u;
                                      D.lds[0] = D.lds[1] = b0 + wd * 1024; D.lds[2] = D.lds[0] + st; D.lds[3] = D.lds[0] + 2 * st; };
  auto d_wo = [&](Desc& D, int s, unsigned slot) {
    D.p[0] = (const char*)A.wo + (size_t)(512 * g + 32 * ((s + ro) & 15)) * 1024;
    D.p[1] = D.p[0] + 8192; D.p[2] = D.p[0] + 16384; D.p[3] = D.p[0] + 24576;
    D.v[0] = D.v[2] = wd * 1024u + wsw; D.v[1] = D.v[3] = (D.v[0] ^ 32u) + 4096; d_lds(D, slot);
  };
  auto d_kv = [&](Desc& D, int u, unsigned slot) {
    const size_t o = ((size_t)g * 2048 + 1536) * 256;
    D.p[0] = (const char*)A.kc + o + (size_t)((u + 1 + rt) & 7) * 16384; D.p[1] = D.p[0] + 8192;
    D.p[2] = (const char*)A.vc + o + (size_t)((u + rt) & 7) * 16384 + 8192;
    D.p[3] = (const char*)A.vc + o + (size_t)((u + 1 + rt) & 7) * 16384;
    D.v[0] = (4 * wd + l4) * 256 + (((l & 15) ^ (4 * wd + l4)) << 4); D.v[1] = D.v[0] + 4096;
    D.v[2] = (4 * wd + l4) * 256 + (((l & 15) ^ (l4 << 2)) << 4); D.v[3] = D.v[2] + 4096; d_lds(D, slot);
  };
  auto d_g1 = [&](Desc& D, int t, int j) {
    const int tt = (t + r1) & 31;
    D.p[0] = D.p[1] = (const char*)A.w1 + (size_t)(64 * tt) * 3584 + ft1 * 128;
    D.p[2] = ws + WS_H + (size_t)dp1 * 524288 + (size_t)(64 * tt) * 256; D.p[3] = D.p[2] + 128;
    D.v[0] = grow * 3584u + gsw; D.v[1] = D.v[0] + 32 * 3584; D.v[2] = grow * 256u + gsw; D.v[3] = D.v[2] + 32 * 256; d_ldsg(D, j);
  };
  auto d_g2 = [&](Desc& D, int t, int j) {
    int tt = t + r2; if (tt >= 28) tt -= 28;
    D.p[0] = D.p[1] = ws + WS_U + (size_t)dt2 * (224 * 1024) + (size_t)tt * 8192;
    D.p[2] = (const char*)A.w2 + (size_t)(64 * tt) * 4096 + pp2 * 256; D.p[3] = D.p[2] + 128;
    D.v[0] = wd * 1024u + l * 16u; D.v[1] = D.v[0] + 4096; D.v[2] = grow * 4096u + gsw; D.v[3] = D.v[2] + 32 * 4096; d_ldsg(D, j);
  };
#define DMX(D, i) do { if (DW) dma16o<((i) & 1) * 4096>(D.p[(i) >> 1], D.v[((i) >> 2) * 2 + ((i) & 1)], D.lds[(i) >> 1]); } while (0)
#define DMA(i) do { if (DW) dma16p<((i) & 1) * 4096>(DA.p[(i) >> 1], DA.v[((i) >> 2) * 2 + ((i) & 1)], DA.lds[(i) >> 1]); } while (0)
#define DMX_ALL(D) do { DMX(D, 0); DMX(D, 1); DMX(D, 2); DMX(D, 3); DMX(D, 4); DMX(D, 5); DMX(D, 6); DMX(D, 7); } while (0)
#define DMX_G(D) do { DMX(D, 0); DMX(D, 1); DMX(D, 4); DMX(D, 5); DMX(D, 6); DMX(D, 7); } while (0)
  auto nextslot = [](unsigned s) { return s >= 2 * SLOT ? 0u : s + SLOT; };
  auto next5 = [](unsigned s) { return (s == 2 * SLOT || s == 4 * SLOT) ? 0u : s + SLOT; };

  unsigned qv8[8];
  #pragma unroll
  for (int i = 0; i < 8; ++i) qv8[i] = (4 * i + wd) * 8192u + (wsw ^ (((i >> 1) & 1) << 5));
#define QBASE(s_) ((const char*)A.wq + (size_t)(((s_) + rq) & 15) * (32 * 8192) + g * 1024)
#define QDM(s_, i) do { if (DW) dma16p<(i) * 4096>(QBASE(s_), qv8[i], (s_) * SLOT + wd * 1024); } while (0)

#define XOPS "+v"(xr[0]), "+v"(xr[1]), "+v"(xr[2]), "+v"(xr[3]), "+v"(xr[4]), "+v"(xr[5]), "+v"(xr[6]), "+v"(xr[7]), "+v"(ep2)
  if (DW) asm volatile("s_waitcnt vmcnt(0)" : XOPS :: "memory");
  else asm volatile("s_waitcnt vmcnt(0)" : XOPS, "+v"(hr[0]), "+v"(hr[1]) :: "memory");
  QDM(0, 0); QDM(0, 1); QDM(0, 2); QDM(0, 3); QDM(0, 4); QDM(0, 5); QDM(0, 6); QDM(0, 7);
  const u64 tag = (((u64)(unsigned)__builtin_amdgcn_readfirstlane(ep2[1]) << 32) | (u64)(unsigned)__builtin_amdgcn_readfirstlane(ep2[0])) + 0x9E3779B97F4A7C15ull;
  const unsigned tagl = (unsigned)tag, tagh = (unsigned)(tag >> 32);

  if (!DW) __builtin_amdgcn_s_setprio(1);
  {
    float sm = 0.f, sq = 0.f, sm2 = 0.f, sq2 = 0.f;
    #pragma unroll
    for (int i = 0; i < 8; ++i)
      #pragma unroll
      for (int j = 0; j < 4; j += 2) { sm = dot_one(xr[i][j], sm); sq = dot_sq(xr[i][j], sq); sm2 = dot_one(xr[i][j + 1], sm2); sq2 = dot_sq(xr[i][j + 1], sq2); }
    sm += sm2; sq += sq2;
    sm = dadd<0xB1>(sm); sq = dadd<0xB1>(sq); sm = dadd<0x4E>(sm); sq = dadd<0x4E>(sq); sm = dadd<0x141>(sm); sq = dadd<0x141>(sq);
    const float m = sm * (1.f / 512.f);
    const float var = fmaxf(sq * (1.f / 512.f) - m * m, 0.f);
    const float rs = __builtin_amdgcn_rsqf(var + 1e-5f);
    const float mb = -m * rs;
    const unsigned xw = XO + (l & 7) * 1024 + ((xrow8 ^ (l & 7)) << 4);
#define LNROW(i) do { u32x4 o; _Pragma("unroll") for (int j = 0; j < 4; ++j) o[j] = pk2(bflo(xr[i][j]) * rs + mb, bfhi(xr[i][j]) * rs + mb); \
      *(LDS3 u32x4*)(xw + (i) * 8192) = o; QDM(1, i); } while (0)
    LNROW(0); LNROW(1); LNROW(2); LNROW(3); LNROW(4); LNROW(5); LNROW(6); LNROW(7);
  }
  auto hpub = [&]() { if (!DW) {
    float sm = 0.f, sq = 0.f;
    #pragma unroll
    for (int i = 0; i < 2; ++i)
      #pragma unroll
      for (int j = 0; j < 4; ++j) { sm = dot_one(hr[i][j], sm); sq = dot_sq(hr[i][j], sq); }
    sm = dadd<0xB1>(sm); sq = dadd<0xB1>(sq); sm = dadd<0x4E>(sm); sq = dadd<0x4E>(sq);
    sm = dadd<0x141>(sm); sq = dadd<0x141>(sq); sm = dadd<0x140>(sm); sq = dadd<0x140>(sq);
    { auto a = __builtin_amdgcn_permlane16_swap(__float_as_uint(sm), __float_as_uint(sm), false, false); sm = __uint_as_float(a[0]) + __uint_as_float(a[1]);
      auto c = __builtin_amdgcn_permlane16_swap(__float_as_uint(sq), __float_as_uint(sq), false, false); sq = __uint_as_float(c[0]) + __uint_as_float(c[1]); }
    const float m = sm * (1.f / 512.f);
    const float var = fmaxf(sq * (1.f / 512.f) - m * m, 0.f);
    const float rs = __builtin_amdgcn_rsqf(var + 1e-5f);
    const float rs2 = rs * __builtin_amdgcn_rsqf(var * rs * rs + 1e-5f), mb2 = -m * rs2;
    char* hp = ws + WS_H + (size_t)(r >> 4) * 524288 + (size_t)hrow * 256 + (r & 15) * 16;
    #pragma unroll
    for (int i = 0; i < 2; ++i) {
      u32x4 o;
      #pragma unroll
      for (int j = 0; j < 4; ++j) o[j] = pk2(bflo(hr[i][j]) * rs2 + mb2, bfhi(hr[i][j]) * rs2 + mb2);
      store16_sc1(hp + i * 1048576, o);
    }
  } };
  __builtin_amdgcn_s_setprio(0);
  unsigned cur = 0;
  unsigned fre = 2 * SLOT + wd * 1024;

#define PMM(P, ks_, i_, j_) do { if (MM) acc[i_][j_] = mfma(fa[P][ks_][j_], fb[P][ks_][i_], acc[i_][j_]); SB(); } while (0)
#define PDM(FAST, i) do { if (FAST) { if (DW) dma16p<(i) * 4096>(pb_, pv8[i], fre); } else DMX(DA, i); SB(); } while (0)
#define OMM(P, a_, b_) do { if (MM) acc[a_][b_] = mfma16(fa[P][a_], fb[P][b_], acc[a_][b_]); SB(); } while (0)
#define PSTEP16(P, N, s_, MM_, FAST, MID) do { constexpr bool MM = MM_; STEPBAR(); \
      OMM(P, 0, 0); fb[N][0] = PLB(0, s_); const char* pb_ = PBASE((s_) + 2); SB(); OMM(P, 0, 1); PDM(FAST, 0); \
      OMM(P, 0, 2); fa[N][0] = PLA(0); SB(); OMM(P, 0, 3); PDM(FAST, 1); \
      OMM(P, 1, 0); fb[N][1] = PLB(1, s_); SB(); OMM(P, 1, 1); PDM(FAST, 2); \
      OMM(P, 1, 2); fa[N][1] = PLA(1); SB(); OMM(P, 1, 3); PDM(FAST, 3); fb[N][2] = PLB(2, s_); SB(); \
      OMM(P, 2, 0); fa[N][2] = PLA(2); SB(); OMM(P, 2, 1); PDM(FAST, 4); fb[N][3] = PLB(3, s_); SB(); \
      OMM(P, 2, 2); fa[N][3] = PLA(3); SB(); OMM(P, 2, 3); PDM(FAST, 5); \
      OMM(P, 3, 0); PDM(FAST, 6); OMM(P, 3, 1); PDM(FAST, 7); MID; fre = cur + wd * 1024; cur = nextslot(cur); SB(); \
      OMM(P, 3, 2); OMM(P, 3, 3); } while (0)
#define PLOOP(MID0) do { PSTEP(1, 0, 0, false, true, MID0); \
    for (int s = 1; s < 11; s += 2) { PSTEP(0, 1, s, true, true, auxmid(s)); PSTEP(1, 0, s + 1, true, true, (void)0); } \
    PSTEP(0, 1, 11, true, true, POLL_ISSUE); PSTEP(1, 0, 12, true, true, (void)0); \
    PSTEP(0, 1, 13, true, true, { POLL_CHECK; nextd(0); }); PSTEP(1, 0, 14, true, false, nextd(1)); PSTEP(0, 1, 15, true, false, nextd(2)); } while (0)

#define GMM(P, k_, a_, b_) do { if (MM) acc[a_][b_] = mfma16(fa[P][k_][a_], fb[P][k_][b_], acc[a_][b_]); SB(); } while (0)
#define GM2(P, k_, a_) do { GMM(P, k_, a_, 0); GMM(P, k_, a_, 1); } while (0)
#define GSTEP(P, N, J, MM_, DD, MID) do { constexpr bool MM = MM_; \
    if (DW) { if ((DD) == 1) DMX_G(DA); if ((DD) == 2) DMX_ALL(DA); MID; } else { \
    asm volatile("" : "+v"(acc[0][0]), "+v"(acc[0][1]), "+v"(acc[1][0]), "+v"(acc[1][1]), "+v"(acc[2][0]), "+v"(acc[2][1]), "+v"(acc[3][0]), "+v"(acc[3][1])); \
    const unsigned ga_ = (J) < 3 ? gbase[(J) % 3] : 24576u, gb_ = (J) < 3 ? gbase[(J) % 3] + gsub8 : 24576u + gsub32; \
    GM2(P, 0, 0); GLB(N, 0, 0); GLA(N, 0, 0); SB(); GM2(P, 0, 1); GLB(N, 0, 1); GLA(N, 0, 1); SB(); \
    GM2(P, 0, 2); GLA(N, 0, 2); GLA(N, 0, 3); SB(); GM2(P, 0, 3); GLB(N, 1, 0); GLA(N, 1, 0); SB(); \
    GM2(P, 1, 0); GLB(N, 1, 1); GLA(N, 1, 1); SB(); GM2(P, 1, 1); GLA(N, 1, 2); GLA(N, 1, 3); SB(); \
    GM2(P, 1, 2); MID; SB(); GM2(P, 1, 3); } } while (0)
#define GLAST() do { if (!DW) { constexpr bool MM = true; GM2(1, 0, 0); GM2(1, 0, 1); GM2(1, 0, 2); GM2(1, 0, 3); GM2(1, 1, 0); GM2(1, 1, 1); GM2(1, 1, 2); GM2(1, 1, 3); } } while (0)
  const unsigned gtr = (4 * l4 + q) * 128 + (p >> 1) * 16 + (p & 1) * 8, gtf = 2 * (l4 & 1) + (q >> 1);
#define GBAR(N) do { if (DW) VMWAIT(N); LBAR(); } while (0)

  const int hh = w >> 1, rh = w & 1;
  bf16x8 qf[8];
  {
    f32x4 acc[4][4] = {};
    unsigned aq[4];
    #pragma unroll
    for (int j = 0; j < 4; ++j)
      aq[j] = (8 * l4 + q) * 1024 + (16 * hh + 4 * ((2 * rh + (j >> 1)) ^ q) + 2 * ((j & 1) ^ (l4 & 1)) + (p >> 1)) * 16 + (p & 1) * 8;
    const unsigned xrow = XO + l4 * 1024 + (((l & 15) ^ l4) << 4);
    bf16x8 fa[2][4], fb[2][4];
    u32x4 pf0, pf1;
#define PLB(i_, s_) lds_b128((xrow ^ (unsigned)((((s_) + rq) & 1) << 6)) + (((s_) + rq) & 15) * 4096 + 256 * (i_))
#define PLA(j_) lds_tr2(cur + aq[j_], cur + aq[j_] + 4096)
#define PBASE(s_) QBASE(s_)
#define pv8 qv8
    auto auxmid = [&](int s) {
      if (!DW) { if (s == 3) VMWAIT(0); if (s == 5 && w == 4 && l == 0) flag_store(ws + WS_HFLAG + (rb * 8 + g) * 8, tag); }
    };
#define POLL_ISSUE do { if (!DW) asm volatile("global_load_dwordx4 %0, %2, off sc1\n\tglobal_load_dwordx4 %1, %2, off offset:16 sc1" \
                          : "=&v"(pf0), "=&v"(pf1) : "v"(ws + WS_HFLAG + l * 32) : "memory"); } while (0)
#define POLL_CHECK do { if (!DW) { \
      asm volatile("s_waitcnt vmcnt(0)" : "+v"(pf0), "+v"(pf1) :: "memory"); \
      const bool ok = pf0[0] == tagl && pf0[1] == tagh && pf0[2] == tagl && pf0[3] == tagh && pf1[0] == tagl && pf1[1] == tagh && pf1[2] == tagl && pf1[3] == tagh; \
      if (do_g1 && w == 4) { if (!__all(ok)) poll_flags((const u64*)(ws + WS_HFLAG), 256, tag, l); \
        if (b == 16 && l == 0) flag_store(ws + WS_EPOCH, tag); } } } while (0)
    auto nextd = [&](int n) { if (DW) { if (do_g1) d_g1(DA, n, (1 + n) % 3); else d_kv(DA, n - 1, ((1 + n) % 3) * SLOT); } };
#define PSTEP PSTEP16
    PLOOP(hpub());
#undef PSTEP
#undef pv8
#undef PLB
#undef PLA
#undef PBASE
#undef POLL_ISSUE
#undef POLL_CHECK
    { constexpr bool MM = true;
      const unsigned qa = XO + (hh * 16 + 8 * rh + 2 * (l4 & 1) + (l4 >> 1)) * 1024 + (l & 15) * 16;
#define QWR(pr, rbk) do { u32x4 v_ = pack44(acc[2 * (pr)][rbk] * QSCALE, acc[2 * (pr) + 1][rbk] * QSCALE); \
        auto s0_ = __builtin_amdgcn_permlane16_swap(v_[0], v_[2], false, false); auto s1_ = __builtin_amdgcn_permlane16_swap(v_[1], v_[3], false, false); \
        const u32x4 o_ = {s0_[0], s1_[0], s0_[1], s1_[1]}; *(LDS3 u32x4*)(qa + (pr) * 4096 + (rbk) * 256) = o_; SB(); } while (0)
      OMM(1, 0, 0); OMM(1, 1, 0); OMM(1, 2, 0); OMM(1, 3, 0); OMM(1, 0, 1); OMM(1, 1, 1); OMM(1, 2, 1); OMM(1, 3, 1);
      LBAR();
      OMM(1, 0, 2); OMM(1, 1, 2); QWR(0, 0); OMM(1, 2, 2); OMM(1, 3, 2); QWR(1, 0);
      OMM(1, 0, 3); OMM(1, 1, 3); QWR(0, 1); OMM(1, 2, 3); OMM(1, 3, 3); QWR(1, 1);
      QWR(0, 2); QWR(1, 2); QWR(0, 3); QWR(1, 3);
      STEPBAR();
      const unsigned qr = XO + (hh * 16 + h) * 1024 + (32 * rh + r) * 16;
      #pragma unroll
      for (int kk = 0; kk < 8; ++kk) qf[kk] = lds_b128(qr + kk * 2048);
    }
    asm volatile("" : "+v"(qf[0]), "+v"(qf[1]), "+v"(qf[2]), "+v"(qf[3]), "+v"(qf[4]), "+v"(qf[5]), "+v"(qf[6]), "+v"(qf[7]));
  }
  unsigned fill = 0;

  if (do_g1) {
    f32x4 acc[4][2] = {};
    unsigned aa[4], ba[2];
    #pragma unroll
    for (int a = 0; a < 4; ++a) aa[a] = gtr + 32 * (a ^ gtf);
    #pragma unroll
    for (int a = 0; a < 2; ++a) ba[a] = gtr + 32 * ((2 * (wd & 1) + a) ^ gtf);
    const unsigned gbase[3] = {32768u, 65536u, 0u};
    const unsigned gsub8 = 8192u * (1 + (wd >> 1)), gsub32 = 32768u * (1 + (wd >> 1));
    bf16x8 fa[2][2][4], fb[2][2][2];
#define GLA(N, kk, j_) fa[N][kk][j_] = lds_tr2(ga_ + aa[j_] + (kk) * 4096, ga_ + aa[j_] + (kk) * 4096 + 2048)
#define GLB(N, kk, j_) fb[N][kk][j_] = lds_tr2(gb_ + ba[j_] + (kk) * 4096, gb_ + ba[j_] + (kk) * 4096 + 2048)
#define G1D(t_, j_) do { if (DW) d_g1(DA, t_, j_); } while (0)
    if (DW) { DMX_G(DA); d_g1(DA, 3, 3); }
    GSTEP(1, 0, 0, false, 1, G1D(4, 1));
    GBAR(12); GSTEP(0, 1, 1, true, 1, G1D(5, 2));
    GBAR(12); GSTEP(1, 0, 2, true, 1, G1D(6, 0));
    GBAR(12); GSTEP(0, 1, 3, true, 1, G1D(7, 3));
    for (int t = 4; t < 28; t += 4) {
      GBAR(12); GSTEP(1, 0, 0, true, 1, G1D(t + 4, 1));
      GBAR(12); GSTEP(0, 1, 1, true, 1, G1D(t + 5, 2));
      GBAR(12); GSTEP(1, 0, 2, true, 1, G1D(t + 6, 0));
      GBAR(12); GSTEP(0, 1, 3, true, 1, G1D(t + 7, 3));
    }
    GBAR(12); GSTEP(1, 0, 0, true, 1, if (DW) d_kv(DA, -1, XO));
    GBAR(12); GSTEP(0, 1, 1, true, 2, if (DW) d_kv(DA, 0, XO + SLOT));
    GBAR(14); GSTEP(1, 0, 2, true, 2, if (DW) d_kv(DA, 1, 0));
    constexpr int f2 = DW ? 1 : 0;
    u32x2 bb[2];
    asm volatile("global_load_dwordx2 %0, %2, off\n\tglobal_load_dwordx2 %1, %2, off offset:32"
                 : "=&v"(bb[0]), "=&v"(bb[1]) : "v"(A.b1 + 64 * ft1 + 32 * f2 + 4 * l4) : "memory");
    GBAR(18); GSTEP(0, 1, 3, true, 0, (void)0);
    if (!DW) {
      constexpr bool MM = true;
      GM2(1, 0, 2); GM2(1, 0, 3); GM2(1, 1, 2); GM2(1, 1, 3); GM2(1, 0, 0); GM2(1, 0, 1);
      #pragma unroll
      for (int m = 0; m < 4; ++m) *(LDS3 f32x4*)(SLOT + wd * 4096 + m * 1024 + l * 16) = acc[2 + (m >> 1)][m & 1];
      SB(); GM2(1, 1, 0); GM2(1, 1, 1);
    }
    asm volatile("s_waitcnt vmcnt(0)" : "+v"(bb[0]), "+v"(bb[1]) :: "memory");
    LBAR();
#undef GLA
#undef GLB
    if (DW) {
      #pragma unroll
      for (int m = 0; m < 4; ++m) acc[m >> 1][m & 1] = *(LDS3 f32x4*)(SLOT + wd * 4096 + m * 1024 + l * 16);
    }
    #pragma unroll
    for (int m = 0; m < 4; ++m) {
      f32x4& v = acc[m >> 1][m & 1];
      v[0] += bflo(bb[m >> 1][0]); v[1] += bfhi(bb[m >> 1][0]); v[2] += bflo(bb[m >> 1][1]); v[3] += bfhi(bb[m >> 1][1]);
      #pragma unroll
      for (int e = 0; e < 4; ++e) {
        const float u = v[e];
        const float z = u * (1.f + 0.044715f * u * u) * (-2.f * 0.7978845608028654f * 1.4426950408889634f);
        v[e] = u * __builtin_amdgcn_rcpf(1.f + __builtin_amdgcn_exp2f(z));
      }
    }
    char* up = ws + WS_U + (size_t)(2 * dp1 + (wd >> 1)) * (224 * 1024) + (size_t)(ft1 * 8 + f2 * 4 + l4) * 1024 + (32 * (wd & 1) + (l & 15)) * 16;
    store16_sc1(up, pack44(acc[0][0], acc[1][0]));
    store16_sc1(up + 256, pack44(acc[0][1], acc[1][1]));
    cur = XO;
  }

  {
    f32x16 o[4];
    f32x16 negm, sA, sB;
    float lsum = 0.f;
    unsigned ka[8];
    #pragma unroll
    for (int kk = 0; kk < 8; ++kk) ka[kk] = (r * 256 + ((h ^ (r & 15)) << 4)) ^ (unsigned)(kk << 5);
    unsigned va[4];
    #pragma unroll
    for (int d = 0; d < 4; ++d) va[d] = 16384 + (4 * h + q) * 256 + (4 * (d ^ q) + 2 * gq + (p >> 1)) * 16 + (p & 1) * 8;
    bf16x8 kf[8], vf[8];
    u32x4 pw0 = {}, pw1 = {};
    float mx;
    unsigned kc_[8], vc_[4];
#define SETADDR() do { _Pragma("unroll") for (int i_ = 0; i_ < 8; ++i_) { kc_[i_] = cur + ka[i_]; } _Pragma("unroll") for (int i_ = 0; i_ < 4; ++i_) { vc_[i_] = cur + va[i_]; } } while (0)
#define LDK(i, KB) kf[i] = lds_b128(kc_[i] + (KB) * 8192)
#define LDV(i, VP) vf[i] = lds_tr2(vc_[(i) & 3] + ((VP) * 32 + 16 * ((i) >> 2)) * 256, vc_[(i) & 3] + ((VP) * 32 + 16 * ((i) >> 2) + 8) * 256)
#define EXP(Y, e) do { Y[e] = __builtin_amdgcn_exp2f(Y[e]); } while (0)
#define ADD(Y, e) do { lsum += Y[e]; } while (0)
#define CVT(P, Y, c) do { P[(c) & 3] = pk2(Y[2 * (c)], Y[2 * (c) + 1]); } while (0)
#define MX(X, e) do { mx = fmaxf(fmaxf(mx, X[2 * (e)]), X[2 * (e) + 1]); } while (0)
#define QK(X, i) do { X = mfma(kf[i], qf[i], (i) == 0 ? negm : X); } while (0)
#define PV(i) do { o[(i) & 3] = mfma(vf[i], __builtin_bit_cast(bf16x8, (i) < 4 ? pw0 : pw1), o[(i) & 3]); } while (0)
#define DMAC(C, i) do { if (C) DMA(i); } while (0)
#define STAGE_A(X, Y, KB, VP, DC) do { \
      QK(X, 0); LDK(6, KB); EXP(Y, 0); EXP(Y, 1); DMAC(DC, 3); SB(); \
      QK(X, 1); LDK(7, KB); EXP(Y, 2); CVT(pw0, Y, 0); DMAC(DC, 4); SB(); \
      QK(X, 2); LDV(0, VP); EXP(Y, 3); EXP(Y, 4); DMAC(DC, 5); SB(); \
      QK(X, 3); LDV(1, VP); EXP(Y, 5); ADD(Y, 0); CVT(pw0, Y, 1); DMAC(DC, 6); SB(); \
      QK(X, 4); LDV(2, VP); EXP(Y, 6); EXP(Y, 7); CVT(pw0, Y, 2); DMAC(DC, 7); SB(); \
      QK(X, 5); LDV(3, VP); EXP(Y, 8); ADD(Y, 1); ADD(Y, 2); CVT(pw0, Y, 3); SB(); \
      QK(X, 6); LDV(4, VP); EXP(Y, 9); EXP(Y, 10); ADD(Y, 3); SB(); \
      QK(X, 7); LDV(5, VP); EXP(Y, 11); ADD(Y, 4); CVT(pw1, Y, 4); SB(); \
    } while (0)
#define RESCALE(X) do { \
      if (__any(mx > THR)) { \
        const float delta = fmaxf(mx, 0.f); \
        const float alpha = __builtin_amdgcn_exp2f(-delta); \
        _Pragma("unroll") for (int e = 0; e < 16; ++e) { X[e] -= delta; negm[e] -= delta; } \
        _Pragma("unroll") for (int d = 0; d < 4; ++d) _Pragma("unroll") for (int e = 0; e < 16; ++e) o[d][e] *= alpha; \
        lsum *= alpha; \
      } } while (0)
#define STAGE_B(X, Y, VP, KBN) do { \
      PV(0); LDV(6, VP); EXP(Y, 12); EXP(Y, 13); CVT(pw1, Y, 5); SB(); \
      PV(1); LDV(7, VP); EXP(Y, 14); EXP(Y, 15); ADD(Y, 5); SB(); \
      PV(2); LDK(0, KBN); CVT(pw1, Y, 6); CVT(pw1, Y, 7); ADD(Y, 6); mx = -3.0e38f; MX(X, 0); SB(); \
      PV(3); LDK(1, KBN); MX(X, 1); MX(X, 2); ADD(Y, 7); ADD(Y, 8); SB(); \
      PV(4); LDK(2, KBN); MX(X, 3); MX(X, 4); ADD(Y, 9); ADD(Y, 10); SB(); \
      PV(5); LDK(3, KBN); MX(X, 5); MX(X, 6); ADD(Y, 11); ADD(Y, 12); SB(); \
      PV(6); LDK(4, KBN); MX(X, 7); ADD(Y, 13); ADD(Y, 14); SB(); \
      PV(7); LDK(5, KBN); mx = half_max(mx); ADD(Y, 15); SB(); \
      RESCALE(X); SB(); \
    } while (0)
#define STAGE_B_BAR(X, Y, VP, WAITS, SETD, POST) do { \
      PV(0); LDV(6, VP); EXP(Y, 12); EXP(Y, 13); CVT(pw1, Y, 5); SB(); \
      PV(1); LDV(7, VP); EXP(Y, 14); EXP(Y, 15); ADD(Y, 5); SB(); \
      PV(2); CVT(pw1, Y, 6); CVT(pw1, Y, 7); ADD(Y, 6); mx = -3.0e38f; MX(X, 0); SB(); \
      cur = next5(cur); \
      PV(3); SETADDR(); SETD; SB(); \
      PV(4); SB(); \
      WAITS; LBAR(); PRIO(3); \
      PV(5); SB(); LDK(0, 0); LDK(1, 0); LDK(2, 0); LDK(3, 0); MX(X, 1); MX(X, 2); MX(X, 3); MX(X, 4); ADD(Y, 7); ADD(Y, 8); ADD(Y, 9); POST; DMA(0); SB(); \
      PV(6); LDK(4, 0); MX(X, 5); MX(X, 6); MX(X, 7); ADD(Y, 10); ADD(Y, 11); ADD(Y, 12); DMA(1); SB(); \
      PV(7); LDK(5, 0); mx = half_max(mx); ADD(Y, 13); ADD(Y, 14); ADD(Y, 15); DMA(2); SB(); \
      RESCALE(X); SB(); \
    } while (0)


    SETADDR();
    #pragma unroll
    for (int i = 0; i < 8; ++i) LDK(i, 0);
    negm = f32x16{};
    #pragma unroll
    for (int d = 0; d < 4; ++d) o[d] = f32x16{};
    SB();
    QK(sA, 0); LDK(0, 1); DMA(0); SB(); QK(sA, 1); LDK(1, 1); DMA(1); SB(); QK(sA, 2); LDK(2, 1); DMA(2); SB(); QK(sA, 3); LDK(3, 1); SB();
    QK(sA, 4); LDK(4, 1); SB(); QK(sA, 5); LDK(5, 1); SB(); QK(sA, 6); SB(); QK(sA, 7); SB();
    mx = -3.0e38f;
    #pragma unroll
    for (int e = 0; e < 8; ++e) MX(sA, e);
    mx = half_max(mx);
    #pragma unroll
    for (int e = 0; e < 16; ++e) { sA[e] -= mx; negm[e] = -mx; }
    SB();
    STAGE_A(sB, sA, 1, 1, true);
    fill = SLOT;
    STAGE_B_BAR(sB, sA, 1, VMWAIT(0), if (DW) d_kv(DA, 2, fill),
                if (!DW && do_g1 && w == 4 && l == 0) flag_store(ws + WS_UFLAG + (dp1 * 28 + ft1) * 8, tag));
    for (int u = 0; u < 7; ++u) {
      STAGE_A(sA, sB, 0, 0, true);
      PRIO(2);
      STAGE_B(sA, sB, 0, 1);
      PRIO(1);
      STAGE_A(sB, sA, 1, 1, false);
      PRIO(0);
      fill = nextslot(fill);
      STAGE_B_BAR(sB, sA, 1, if (DW) VMWAIT(8), if (DW) { if (u + 3 < 8) d_kv(DA, u + 3, fill); else d_wo(DA, u - 5, fill); }, (void)0);
    }
    PRIO(0);
    #pragma unroll
    for (int i = 0; i < 8; ++i) LDV(i, 0);
    DMA(3); DMA(4);
    #pragma unroll
    for (int e = 0; e < 16; ++e) { EXP(sB, e); ADD(sB, e); }
    #pragma unroll
    for (int c = 0; c < 4; ++c) { CVT(pw0, sB, c); CVT(pw1, sB, 4 + c); }
    SB();
    fre = cur + wd * 1024; cur = nextslot(cur);
    const float inv = __builtin_amdgcn_rcpf(half_sum(lsum));
    const unsigned ob = XO + (hh * 16 + h) * 1024 + (32 * rh + r) * 16;
#define OWR(d) do { *(LDS3 bf16x8*)(ob + (4 * (d)) * 1024) = pack8<0>(o[d], inv); *(LDS3 bf16x8*)(ob + (4 * (d) + 2) * 1024) = pack8<1>(o[d], inv); } while (0)
    PV(0); PV(4); DMA(5); SB(); PV(1); PV(5); DMA(6); SB(); PV(2); OWR(0); SB(); PV(6); DMA(7); SB(); PV(3); OWR(1); SB(); PV(7); SB(); OWR(2); OWR(3);
  }

  {
    f32x4 acc[4][4] = {};
    unsigned oa[4];
    #pragma unroll
    for (int j = 0; j < 4; ++j)
      oa[j] = (16 * (l4 >> 1) + 4 * (l4 & 1) + q) * 1024 + (16 * (w >> 1) + 4 * ((2 * (w & 1) + (j >> 1)) ^ q) + 2 * ((j & 1) ^ (l4 & 1)) + (p >> 1)) * 16 + (p & 1) * 8;
    const unsigned ob = XO + l4 * 1024 + (l & 15) * 16;
    bf16x8 fa[2][4], fb[2][4];
    u32x2 pfl;
#define PLB(i_, s_) lds_b128(ob + (((s_) + ro) & 15) * 4096 + 256 * (i_))
#define PLA(j_) lds_tr2(cur + oa[j_], cur + oa[j_] + 8192)
#define PBASE(s_) ((const char*)A.wo + (size_t)(16 * g + (((s_) + ro) & 15)) * 32768)
    unsigned pv8[8];
    #pragma unroll
    for (int i = 0; i < 8; ++i) pv8[i] = (4 * i + wd) * 1024u + (wsw ^ ((i & 1) << 5));
    auto auxmid = [&](int) {};
#define PX(P, n_) do { } while (0)
#define POLL_ISSUE do { if (!DW) asm volatile("global_load_dwordx2 %0, %1, off sc1" : "=&v"(pfl) : "v"(ws + WS_UFLAG + ((dt2 >> 1) * 28 + (l < 28 ? l : 0)) * 8) : "memory"); } while (0)
#define POLL_CHECK do { if (!DW) { asm volatile("s_waitcnt vmcnt(0)" : "+v"(pfl) :: "memory"); \
      if (do_g2 && w == 4 && !__all(pfl[0] == tagl && pfl[1] == tagh)) poll_flags((const u64*)(ws + WS_UFLAG) + (dt2 >> 1) * 28, 28, tag, l); } } while (0)
    auto nextd = [&](int n) { if (DW) { if (do_g2) d_g2(DA, n, (2 + n) % 3); else d_wo(DA, n, ((2 + n) % 3) * SLOT); } };
#define PSTEP PSTEP16
    PLOOP((void)0);
#undef PSTEP
#undef PLB
#undef PLA
#undef PBASE
#undef POLL_ISSUE
#undef POLL_CHECK
    char* pp = ws + WS_PART + ((size_t)(rb * 8 + w) * 8 + g) * 8192 + l * 16;
    { constexpr bool MM = true;
      OMM(1, 0, 0); OMM(1, 1, 0); OMM(1, 2, 0); OMM(1, 3, 0); OMM(1, 0, 1); OMM(1, 1, 1); OMM(1, 2, 1); OMM(1, 3, 1);
      store16_sc1(pp, pack44(acc[0][0], acc[1][0])); store16_sc1(pp + 1024, pack44(acc[2][0], acc[3][0])); SB();
      OMM(1, 0, 2); OMM(1, 1, 2); OMM(1, 2, 2); OMM(1, 3, 2);
      store16_sc1(pp + 2048, pack44(acc[0][1], acc[1][1])); store16_sc1(pp + 3072, pack44(acc[2][1], acc[3][1])); SB();
      OMM(1, 0, 3); OMM(1, 1, 3); OMM(1, 2, 3); OMM(1, 3, 3);
      store16_sc1(pp + 4096, pack44(acc[0][2], acc[1][2])); store16_sc1(pp + 5120, pack44(acc[2][2], acc[3][2]));
      store16_sc1(pp + 6144, pack44(acc[0][3], acc[1][3])); store16_sc1(pp + 7168, pack44(acc[2][3], acc[3][3]));
    }
    VMWAIT(0);
    if (l == 0) flag_store(ws + WS_SFLAG + ((rb * 8 + w) * 8 + g) * 8, tag);
    if (do_g2) LBAR();
  }

  if (do_g2) {
    const int wi = (w >> 1) & 1, wj = w & 1, sb2 = DW ? 1 : 0;
    f32x4 acc[4][2] = {};
    const unsigned aa = l4 * 1024 + (l & 15) * 16;
    unsigned ba[2];
    #pragma unroll
    for (int a = 0; a < 2; ++a) ba[a] = gtr + 32 * ((2 * (wd & 1) + a) ^ gtf);
    const unsigned gbase[3] = {65536u, 0u, 32768u};
    const unsigned gsub8 = 8192u * (1 + (wd >> 1)), gsub32 = 32768u * (1 + (wd >> 1));
    bf16x8 fa[2][2][4], fb[2][2][2];
    const int srow = 64 * (2 * pp2 + sb2) + 32 * wi + (l & 15);
    const size_t xo = (size_t)srow * 512 + 64 * dt2 + 32 * wj + 16 * (l4 & 1) + 8 * (l4 >> 1);
    const char* pp = ws + WS_PART + (size_t)((2 * pp2 + sb2) * 8 + dt2) * 65536 + (wi * 4 + wj) * 1024 + l * 16;
    const u64* sfl = (const u64*)(ws + WS_SFLAG) + ((2 * pp2 + sb2) * 8 + dt2) * 8;
    u32x4 pv[16];
    u32x2 pfl;
#define GLA(N, kk, j_) fa[N][kk][j_] = lds_b128(ga_ + aa + (kk) * 4096 + 256 * (j_))
#define GLB(N, kk, j_) fb[N][kk][j_] = lds_tr2(gb_ + ba[j_] + (kk) * 4096, gb_ + ba[j_] + (kk) * 4096 + 2048)
#define PLD(c) asm volatile("global_load_dwordx4 %0, %1, off sc1" : "=v"(pv[c]) : "v"(pp + ((c) >> 1) * 8192 + ((c) & 1) * 2048) : "memory")
#define G2D(t_, j_, AUX) do { if (DW) d_g2(DA, t_, j_); else { AUX; } } while (0)
    if (DW) { DMX_G(DA); d_g2(DA, 3, 3); }
    GSTEP(1, 0, 0, false, 1, G2D(4, 2, (void)0));
    GBAR(12); GSTEP(0, 1, 1, true, 1, G2D(5, 0, (void)0));
    GBAR(12); GSTEP(1, 0, 2, true, 1, G2D(6, 1, (void)0));
    GBAR(12); GSTEP(0, 1, 3, true, 1, G2D(7, 3, (void)0));
    for (int t = 4; t < 12; t += 4) {
      GBAR(12); GSTEP(1, 0, 0, true, 1, G2D(t + 4, 2, (void)0));
      GBAR(12); GSTEP(0, 1, 1, true, 1, G2D(t + 5, 0, (void)0));
      GBAR(12); GSTEP(1, 0, 2, true, 1, G2D(t + 6, 1, (void)0));
      GBAR(12); GSTEP(0, 1, 3, true, 1, G2D(t + 7, 3, (void)0));
    }
    GBAR(12); GSTEP(1, 0, 0, true, 1, G2D(16, 2, asm volatile("global_load_dwordx2 %0, %1, off sc1" : "=&v"(pfl) : "v"(sfl + (l & 7)) : "memory")));
    GBAR(12); GSTEP(0, 1, 1, true, 1, G2D(17, 0, (void)0));
    GBAR(12); GSTEP(1, 0, 2, true, 1, G2D(18, 1, (void)0));
    GBAR(12); GSTEP(0, 1, 3, true, 1, G2D(19, 3, { asm volatile("s_waitcnt vmcnt(0)" : "+v"(pfl) :: "memory");
                                                  if (!__all(pfl[0] == tagl && pfl[1] == tagh)) poll_flags(sfl, 8, tag, l); PLD(0); PLD(1); }));
    GBAR(12); GSTEP(1, 0, 0, true, 1, G2D(20, 2, { PLD(2); PLD(3); }));
    GBAR(12); GSTEP(0, 1, 1, true, 1, G2D(21, 0, { PLD(4); PLD(5); }));
    GBAR(12); GSTEP(1, 0, 2, true, 1, G2D(22, 1, { PLD(6); PLD(7); }));
    GBAR(12); GSTEP(0, 1, 3, true, 1, G2D(23, 3, { PLD(8); PLD(9); }));
    GBAR(12); GSTEP(1, 0, 0, true, 1, G2D(24, 2, { PLD(10); PLD(11); }));
    GBAR(12); GSTEP(0, 1, 1, true, 1, G2D(25, 0, { PLD(12); PLD(13); }));
    GBAR(12); GSTEP(1, 0, 2, true, 1, G2D(26, 1, { PLD(14); PLD(15); }));
    GBAR(12); GSTEP(0, 1, 3, true, 1, G2D(27, 3, (void)0));
    GBAR(12); GSTEP(1, 0, 0, true, 1, (void)0);
    u32x4 xv0, xv1; unsigned b2r, b2s;
    asm volatile("global_load_dwordx4 %0, %4, off\n\tglobal_load_dwordx4 %1, %5, off\n\tglobal_load_ushort %2, %6, off\n\tglobal_load_ushort %3, %6, off offset:32"
                 : "=&v"(xv0), "=&v"(xv1), "=&v"(b2r), "=&v"(b2s) : "v"(x + xo), "v"(x + xo + 16 * 512), "v"(A.b2 + srow) : "memory");
    GBAR(16); GSTEP(0, 1, 1, true, 0, (void)0);
    GBAR(10); GSTEP(1, 0, 2, true, 0, (void)0);
    GBAR(4); GSTEP(0, 1, 3, true, 0, (void)0);
    GLAST();
#undef GLA
#undef GLB
    if (!DW) {
      #pragma unroll
      for (int m = 0; m < 8; ++m) *(LDS3 f32x4*)(XO + wd * 8192 + m * 1024 + l * 16) = acc[m >> 1][m & 1];
    }
    LBAR();
    f32x16 yv;
    #pragma unroll
    for (int m = 0; m < 4; ++m) {
      const f32x4 v4 = *(LDS3 f32x4*)(XO + (sb2 * 2 + wi) * 8192 + ((2 * wj + (m & 1)) * 2 + (m >> 1)) * 1024 + l * 16);
      yv[4 * m] = v4[0]; yv[4 * m + 1] = v4[1]; yv[4 * m + 2] = v4[2]; yv[4 * m + 3] = v4[3];
    }
    asm volatile("s_waitcnt vmcnt(0)" : "+v"(xv0), "+v"(xv1), "+v"(b2r), "+v"(b2s) :: "memory");
    if (!DW) asm volatile("" : "+v"(pv[0]), "+v"(pv[1]), "+v"(pv[2]), "+v"(pv[3]), "+v"(pv[4]), "+v"(pv[5]), "+v"(pv[6]), "+v"(pv[7]),
                 "+v"(pv[8]), "+v"(pv[9]), "+v"(pv[10]), "+v"(pv[11]), "+v"(pv[12]), "+v"(pv[13]), "+v"(pv[14]), "+v"(pv[15]) :: "memory");
    #pragma unroll
    for (int e = 0; e < 16; ++e) {
      const float yy = yv[e] + bflo(e < 8 ? b2r : b2s);
      yv[e] = yy * __builtin_amdgcn_rcpf(1.f + __builtin_amdgcn_exp2f(-1.4426950408889634f * yy));
    }
    asm volatile("" : "+v"(yv));
    float t[16];
    #pragma unroll
    for (int e = 0; e < 16; ++e) t[e] = 0.f;
    auto accum = [&](const u32x4& a, const u32x4& c) {
      #pragma unroll
      for (int e = 0; e < 4; ++e) { t[2 * e] += bflo(a[e]); t[2 * e + 1] += bfhi(a[e]); t[8 + 2 * e] += bflo(c[e]); t[8 + 2 * e + 1] += bfhi(c[e]); }
    };
    if (DW) {
      unsigned done = 0;
      const char* pb = ws + WS_PART + (size_t)((2 * pp2 + 1) * 8 + dt2) * 65536 + (wi * 4 + wj) * 1024;
      for (unsigned spins = 0; done != 0xffu && spins < (1u << 20); ++spins) {
        const u64 f = __hip_atomic_load((GAS const u64*)(sfl + (l & 7)), __ATOMIC_RELAXED, __HIP_MEMORY_SCOPE_AGENT);
        unsigned nw = (unsigned)__ballot(f == tag) & 0xffu & ~done;
        done |= nw;
        if (!nw) __builtin_amdgcn_s_sleep(1);
        while (nw) {
          const int gg = __builtin_ctz(nw); nw &= nw - 1;
          dma16o<0>(pb + gg * 8192, l * 16u, wd * 16384 + gg * 2048);
          dma16o<1024>(pb + gg * 8192 + 2048, l * 16u, wd * 16384 + gg * 2048);
        }
      }
      VMWAIT(0);
      #pragma unroll
      for (int c = 0; c < 16; ++c) pv[c] = *(LDS3 u32x4*)(wd * 16384 + c * 1024 + l * 16);
    }
    #pragma unroll
    for (int gg = 0; gg < 8; ++gg) accum(pv[2 * gg], pv[2 * gg + 1]);
    #pragma unroll
    for (int c = 0; c < 2; ++c) {
      #pragma unroll
      for (int e = 0; e < 8; ++e) t[8 * c + e] *= yv[8 * c + e];
      #pragma unroll
      for (int e = 0; e < 4; ++e) {
        auto rr = __builtin_amdgcn_permlane16_swap(__float_as_uint(t[8 * c + e]), __float_as_uint(t[8 * c + 4 + e]), false, false);
        t[8 * c + e] = __uint_as_float(rr[0]); t[8 * c + 4 + e] = __uint_as_float(rr[1]);
      }
      const u32x4 xv = c ? xv1 : xv0;
      u32x4 ov;
      #pragma unroll
      for (int e = 0; e < 4; ++e) ov[e] = pk2(t[8 * c + 2 * e] + bflo(xv[e]), t[8 * c + 2 * e + 1] + bfhi(xv[e]));
      *(u32x4*)(A.out + xo + 16 * 512 * c) = ov;
    }
  }
}

__global__ __launch_bounds__(512, 2) void fused_kernel(Args A) {
  extern __shared__ __attribute__((aligned(16))) char lds_raw[];
  const int l = threadIdx.x & 63;
  const int w = __builtin_amdgcn_readfirstlane(threadIdx.x >> 6);
  if (w < 4) body<true>(A, w, l); else body<false>(A, w, l);
}

extern "C" void kernel_launch(void* const* d_in, const int* in_sizes, int n_in, void* d_out, int out_size, void* d_ws, size_t ws_size, hipStream_t stream) {
  (void)hipFuncSetAttribute((const void*)fused_kernel, hipFuncAttributeMaxDynamicSharedMemorySize, LDS_BYTES);
  Args A;
  A.x = (const __bf16*)d_in[0]; A.kc = (const __bf16*)d_in[1]; A.vc = (const __bf16*)d_in[2]; A.wq = (const __bf16*)d_in[3]; A.wo = (const __bf16*)d_in[4];
  A.w1 = (const __bf16*)d_in[6]; A.b1 = (const __bf16*)d_in[7]; A.w2 = (const __bf16*)d_in[8]; A.b2 = (const __bf16*)d_in[9];
  A.out = (__bf16*)d_out; A.ws = (char*)d_ws;
  fused_kernel<<<256, 512, LDS_BYTES, stream>>>(A);
}
```

```cpp
#include <hip/hip_runtime.h>
#include <stdint.h>
#pragma clang diagnostic ignored "-Wint-to-pointer-cast"

typedef __bf16 bf16x8 __attribute__((ext_vector_type(8)));
typedef __bf16 bf16x4 __attribute__((ext_vector_type(4)));
typedef __bf16 bf16x2 __attribute__((ext_vector_type(2)));
typedef float f32x16 __attribute__((ext_vector_type(16)));
typedef float f32x4 __attribute__((ext_vector_type(4)));
typedef float f32x2 __attribute__((ext_vector_type(2)));
typedef unsigned u32x4 __attribute__((ext_vector_type(4)));
typedef unsigned u32x2 __attribute__((ext_vector_type(2)));
typedef unsigned long long u64;
#define LDS3 __attribute__((address_space(3)))
#define GAS __attribute__((address_space(1)))
#define DI __device__ __forceinline__
#define SB() __builtin_amdgcn_sched_barrier(0)
#define PRIO(n) __builtin_amdgcn_s_setprio(n)

constexpr int SLOT = 32768;
constexpr int XO = 98304;
constexpr int LDS_BYTES = 163840;
constexpr size_t WS_EPOCH = 0;
constexpr size_t WS_HFLAG = 4096;
constexpr size_t WS_UFLAG = 8192;
constexpr size_t WS_SFLAG = 16384;
constexpr size_t WS_H     = 1u << 20;
constexpr size_t WS_U     = 4u << 20;
constexpr size_t WS_PART  = 8u << 20;

constexpr float THR = 16.0f;
constexpr float QSCALE = 0.08838834764831845f * 1.4426950408889634f;

DI bf16x8 lds_b128(unsigned a) { return *(LDS3 bf16x8*)a; }
DI bf16x8 lds_tr2(unsigned a0, unsigned a1) {
  bf16x4 lo = __builtin_amdgcn_ds_read_tr16_b64_v4bf16((LDS3 bf16x4*)a0);
  bf16x4 hi = __builtin_amdgcn_ds_read_tr16_b64_v4bf16((LDS3 bf16x4*)a1);
  return __builtin_shufflevector(lo, hi, 0, 1, 2, 3, 4, 5, 6, 7);
}
DI f32x16 mfma(bf16x8 a, bf16x8 b, f32x16 c) { return __builtin_amdgcn_mfma_f32_32x32x16_bf16(a, b, c, 0, 0, 0); }
DI f32x4 mfma16(bf16x8 a, bf16x8 b, f32x4 c) { return __builtin_amdgcn_mfma_f32_16x16x32_bf16(a, b, c, 0, 0, 0); }
DI unsigned pk2(float lo, float hi) { f32x2 v = {lo, hi}; bf16x2 b = __builtin_convertvector(v, bf16x2); return __builtin_bit_cast(unsigned, b); }
template <int S> DI bf16x8 pack8(const f32x16& x, float sc) {
  u32x4 p;
  p[0] = pk2(x[8 * S + 0] * sc, x[8 * S + 1] * sc); p[1] = pk2(x[8 * S + 2] * sc, x[8 * S + 3] * sc);
  p[2] = pk2(x[8 * S + 4] * sc, x[8 * S + 5] * sc); p[3] = pk2(x[8 * S + 6] * sc, x[8 * S + 7] * sc);
  return __builtin_bit_cast(bf16x8, p);
}
DI float bflo(unsigned u) { return __uint_as_float(u << 16); }
DI float bfhi(unsigned u) { return __uint_as_float(u & 0xffff0000u); }
template <int C> DI float dadd(float v) { return v + __builtin_bit_cast(float, __builtin_amdgcn_update_dpp(0, __builtin_bit_cast(int, v), C, 0xf, 0xf, true)); }
DI float dot_one(unsigned u, float c) { return __builtin_amdgcn_fdot2_f32_bf16(__builtin_bit_cast(bf16x2, u), __builtin_bit_cast(bf16x2, 0x3f803f80u), c, false); }
DI float dot_sq(unsigned u, float c) { return __builtin_amdgcn_fdot2_f32_bf16(__builtin_bit_cast(bf16x2, u), __builtin_bit_cast(bf16x2, u), c, false); }
DI float half_sum(float v) {
  auto b = __builtin_amdgcn_permlane32_swap(__float_as_uint(v), __float_as_uint(v), false, false);
  return __uint_as_float(b[0]) + __uint_as_float(b[1]);
}
DI float half_max(float v) {
  auto b = __builtin_amdgcn_permlane32_swap(__float_as_uint(v), __float_as_uint(v), false, false);
  return fmaxf(fmaxf(-3.0e38f, __uint_as_float(b[0])), __uint_as_float(b[1]));
}
DI u32x4 pack44(const f32x4& a, const f32x4& c) { u32x4 p; p[0] = pk2(a[0], a[1]); p[1] = pk2(a[2], a[3]); p[2] = pk2(c[0], c[1]); p[3] = pk2(c[2], c[3]); return p; }
DI void store16_sc1(void* p, u32x4 v) {
  asm volatile("global_store_dwordx4 %0, %1, off sc1\n\ts_nop 1" :: "v"(p), "v"(v) : "memory");
}
#define VMWAIT(N) asm volatile("s_waitcnt vmcnt(" #N ")" ::: "memory")
#define LBAR() do { asm volatile("" ::: "memory"); __builtin_amdgcn_s_waitcnt(0xC07F); __builtin_amdgcn_s_barrier(); asm volatile("" ::: "memory"); } while (0)
#define STEPBAR() do { if (DW) VMWAIT(8); LBAR(); } while (0)

DI void poll_flags(const u64* f, int nflags, u64 tag, int lane) {
  for (unsigned spins = 0; spins < (1u << 20); ++spins) {
    bool ok = true;
    for (int i = lane; i < nflags; i += 64)
      ok &= (__hip_atomic_load((GAS const u64*)(f + i), __ATOMIC_RELAXED, __HIP_MEMORY_SCOPE_AGENT) == tag);
    if (__all(ok)) break;
    __builtin_amdgcn_s_sleep(2);
  }
  asm volatile("" ::: "memory");
}
DI void flag_store(char* p, u64 tag) { __hip_atomic_store((GAS u64*)p, tag, __ATOMIC_RELAXED, __HIP_MEMORY_SCOPE_AGENT); }

struct Args {
  const __bf16* x; const __bf16* kc; const __bf16* vc; const __bf16* wq; const __bf16* wo; const __bf16* w1;
  const __bf16* b1; const __bf16* w2; const __bf16* b2; __bf16* out; char* ws;
};
struct Desc { const char* p[4]; unsigned lds[4]; unsigned v[4]; };

template <int OFF> DI void dma16p(const void* sbase, unsigned voff, unsigned lds) {
  asm volatile("s_add_u32 m0, %2, %3\n\ts_nop 0\n\tglobal_load_lds_dwordx4 %0, %1" :: "v"(voff), "s"(sbase), "s"(__builtin_amdgcn_readfirstlane(lds)), "i"(OFF) : "memory", "scc");
}
template <int OFF> DI void dma16o(const void* sbase, unsigned voff, unsigned lds) {
  asm volatile("s_add_u32 m0, %2, %3\n\ts_nop 0\n\tglobal_load_lds_dwordx4 %0, %1 sc1" :: "v"(voff), "s"(sbase), "s"(__builtin_amdgcn_readfirstlane(lds)), "i"(OFF) : "memory", "scc");
}
template <bool DW>
DI void body(const Args& A, const int w, const int l) {
  const __bf16* __restrict__ x = A.x; char* __restrict__ ws = A.ws;
  const int wd = w & 3;
  const int b = blockIdx.x, xcd = b & 7, idx = b >> 3;
  const int pidx = ((idx >> 2) << 1) | (idx & 1);
  const int g = 2 * (xcd & 3) + (idx & 1), rb = 16 * (xcd >> 2) + (idx >> 1);
  const int r = l & 31, h = l >> 5, gq = (l >> 4) & 1, q = (l >> 2) & 3, p = l & 3;
  const unsigned l4 = l >> 4;

  const int xrow8 = 8 * w + (l >> 3);
  const int hrow = 64 * rb + 8 * g + 2 * wd + h;
  u32x4 xr[8], hr[2];
  u32x2 ep2;
  {
    const char* xb = (const char*)x + (size_t)(64 * rb + xrow8) * 1024 + (l & 7) * 16;
    asm volatile("global_load_dwordx4 %0, %9, off\n\tglobal_load_dwordx4 %1, %9, off offset:128\n\tglobal_load_dwordx4 %2, %9, off offset:256\n\t"
                 "global_load_dwordx4 %3, %9, off offset:384\n\tglobal_load_dwordx4 %4, %9, off offset:512\n\tglobal_load_dwordx4 %5, %9, off offset:640\n\t"
                 "global_load_dwordx4 %6, %9, off offset:768\n\tglobal_load_dwordx4 %7, %9, off offset:896\n\tglobal_load_dwordx2 %8, %10, off sc1"
                 : "=&v"(xr[0]), "=&v"(xr[1]), "=&v"(xr[2]), "=&v"(xr[3]), "=&v"(xr[4]), "=&v"(xr[5]), "=&v"(xr[6]), "=&v"(xr[7]), "=&v"(ep2)
                 : "v"(xb), "v"(ws + WS_EPOCH) : "memory");
    if (!DW) asm volatile("global_load_dwordx4 %0, %2, off\n\tglobal_load_dwordx4 %1, %2, off offset:512"
                 : "=&v"(hr[0]), "=&v"(hr[1]) : "v"((const char*)x + (size_t)hrow * 1024 + r * 16) : "memory");
  }
  const int rq = rb & 15, rt = pidx >> 1, ro = pidx & 14;
  const bool do_g2 = (rb & 1) == 0, do_g1 = !do_g2 && pidx < 14;
  const int ft1 = 7 * (xcd >> 1) + (pidx >> 1), dp1 = 2 * (xcd & 1) + (pidx & 1);
  const int pp2 = 4 * (xcd >> 1) + (pidx >> 2), dt2 = 4 * (xcd & 1) + (pidx & 3);
  const int r1 = (16 * (pidx & 1) + 4 * (pidx >> 1) + (pidx >> 2)) & 31;
  const int r2 = (7 * (pidx & 3) + 7 * (pidx >> 2) / 4 + 2 * (pidx >> 2)) % 28;
  const unsigned wsw = (unsigned)((l ^ (wd << 2)) << 4);
  const unsigned gsw = (unsigned)(((l & 7) ^ (((l >> 4) & 3) << 1)) << 4);
  const unsigned grow = 8 * wd + (l >> 3);
  Desc DA;
  auto d_lds = [&](Desc& D, unsigned slot) { D.lds[0] = slot + wd * 1024; D.lds[1] = D.lds[0] + 8192; D.lds[2] = D.lds[0] + 16384; D.lds[3] = D.lds[0] + 24576; };
  auto d_ldsg = [&](Desc& D, int j) { const unsigned b0 = j < 3 ? j * 32768u : 24576u, st = j < 3 ? 8192u : 32768u;
                                      D.lds[0] = D.lds[1] = b0 + wd * 1024; D.lds[2] = D.lds[0] + st; D.lds[3] = D.lds[0] + 2 * st; };
  auto d_wo = [&](Desc& D, int s, unsigned slot) {
    D.p[0] = (const char*)A.wo + (size_t)(512 * g + 32 * ((s + ro) & 15)) * 1024;
    D.p[1] = D.p[0] + 8192; D.p[2] = D.p[0] + 16384; D.p[3] = D.p[0] + 24576;
    D.v[0] = D.v[2] = wd * 1024u + wsw; D.v[1] = D.v[3] = (D.v[0] ^ 32u) + 4096; d_lds(D, slot);
  };
  auto d_kv = [&](Desc& D, int u, unsigned slot) {
    const size_t o = ((size_t)g * 2048 + 1536) * 256;
    D.p[0] = (const char*)A.kc + o + (size_t)((u + 1 + rt) & 7) * 16384; D.p[1] = D.p[0] + 8192;
    D.p[2] = (const char*)A.vc + o + (size_t)((u + rt) & 7) * 16384 + 8192;
    D.p[3] = (const char*)A.vc + o + (size_t)((u + 1 + rt) & 7) * 16384;
    D.v[0] = (4 * wd + l4) * 256 + (((l & 15) ^ (4 * wd + l4)) << 4); D.v[1] = D.v[0] + 4096;
    D.v[2] = (4 * wd + l4) * 256 + (((l & 15) ^ (l4 << 2)) << 4); D.v[3] = D.v[2] + 4096; d_lds(D, slot);
  };
  auto d_g1 = [&](Desc& D, int t, int j) {
    const int tt = (t + r1) & 31;
    D.p[0] = D.p[1] = (const char*)A.w1 + (size_t)(64 * tt) * 3584 + ft1 * 128;
    D.p[2] = ws + WS_H + (size_t)dp1 * 524288 + (size_t)(64 * tt) * 256; D.p[3] = D.p[2] + 128;
    D.v[0] = grow * 3584u + gsw; D.v[1] = D.v[0] + 32 * 3584; D.v[2] = grow * 256u + gsw; D.v[3] = D.v[2] + 32 * 256; d_ldsg(D, j);
  };
  auto d_g2 = [&](Desc& D, int t, int j) {
    int tt = t + r2; if (tt >= 28) tt -= 28;
    D.p[0] = D.p[1] = ws + WS_U + (size_t)dt2 * (224 * 1024) + (size_t)tt * 8192;
    D.p[2] = (const char*)A.w2 + (size_t)(64 * tt) * 4096 + pp2 * 256; D.p[3] = D.p[2] + 128;
    D.v[0] = wd * 1024u + l * 16u; D.v[1] = D.v[0] + 4096; D.v[2] = grow * 4096u + gsw; D.v[3] = D.v[2] + 32 * 4096; d_ldsg(D, j);
  };
#define DMX(D, i) do { if (DW) dma16o<((i) & 1) * 4096>(D.p[(i) >> 1], D.v[((i) >> 2) * 2 + ((i) & 1)], D.lds[(i) >> 1]); } while (0)
#define DMA(i) do { if (DW) dma16p<((i) & 1) * 4096>(DA.p[(i) >> 1], DA.v[((i) >> 2) * 2 + ((i) & 1)], DA.lds[(i) >> 1]); } while (0)
#define DMX_ALL(D) do { DMX(D, 0); DMX(D, 1); DMX(D, 2); DMX(D, 3); DMX(D, 4); DMX(D, 5); DMX(D, 6); DMX(D, 7); } while (0)
#define DMX_G(D) do { DMX(D, 0); DMX(D, 1); DMX(D, 4); DMX(D, 5); DMX(D, 6); DMX(D, 7); } while (0)
  auto nextslot = [](unsigned s) { return s >= 2 * SLOT ? 0u : s + SLOT; };
  auto next5 = [](unsigned s) { return (s == 2 * SLOT || s == 4 * SLOT) ? 0u : s + SLOT; };

  unsigned qv8[8];
  #pragma unroll
  for (int i = 0; i < 8; ++i) qv8[i] = (4 * i + wd) * 8192u + (wsw ^ (((i >> 1) & 1) << 5));
#define QBASE(s_) ((const char*)A.wq + (size_t)(((s_) + rq) & 15) * (32 * 8192) + g * 1024)
#define QDM(s_, i) do { if (DW) dma16p<(i) * 4096>(QBASE(s_), qv8[i], (s_) * SLOT + wd * 1024); } while (0)

#define XOPS "+v"(xr[0]), "+v"(xr[1]), "+v"(xr[2]), "+v"(xr[3]), "+v"(xr[4]), "+v"(xr[5]), "+v"(xr[6]), "+v"(xr[7]), "+v"(ep2)
  if (DW) asm volatile("s_waitcnt vmcnt(0)" : XOPS :: "memory");
  else asm volatile("s_waitcnt vmcnt(0)" : XOPS, "+v"(hr[0]), "+v"(hr[1]) :: "memory");
  QDM(0, 0); QDM(0, 1); QDM(0, 2); QDM(0, 3); QDM(0, 4); QDM(0, 5); QDM(0, 6); QDM(0, 7);
  const u64 tag = (((u64)(unsigned)__builtin_amdgcn_readfirstlane(ep2[1]) << 32) | (u64)(unsigned)__builtin_amdgcn_readfirstlane(ep2[0])) + 0x9E3779B97F4A7C15ull;
  const unsigned tagl = (unsigned)tag, tagh = (unsigned)(tag >> 32);

  if (!DW) __builtin_amdgcn_s_setprio(1);
  {
    float sm = 0.f, sq = 0.f, sm2 = 0.f, sq2 = 0.f;
    #pragma unroll
    for (int i = 0; i < 8; ++i)
      #pragma unroll
      for (int j = 0; j < 4; j += 2) { sm = dot_one(xr[i][j], sm); sq = dot_sq(xr[i][j], sq); sm2 = dot_one(xr[i][j + 1], sm2); sq2 = dot_sq(xr[i][j + 1], sq2); }
    sm += sm2; sq += sq2;
    sm = dadd<0xB1>(sm); sq = dadd<0xB1>(sq); sm = dadd<0x4E>(sm); sq = dadd<0x4E>(sq); sm = dadd<0x141>(sm); sq = dadd<0x141>(sq);
    const float m = sm * (1.f / 512.f);
    const float var = fmaxf(sq * (1.f / 512.f) - m * m, 0.f);
    const float rs = __builtin_amdgcn_rsqf(var + 1e-5f);
    const float mb = -m * rs;
    const unsigned xw = XO + (l & 7) * 1024 + ((xrow8 ^ (l & 7)) << 4);
#define LNROW(i) do { u32x4 o; _Pragma("unroll") for (int j = 0; j < 4; ++j) o[j] = pk2(bflo(xr[i][j]) * rs + mb, bfhi(xr[i][j]) * rs + mb); \
      *(LDS3 u32x4*)(xw + (i) * 8192) = o; QDM(1, i); } while (0)
    LNROW(0); LNROW(1); LNROW(2); LNROW(3); LNROW(4); LNROW(5); LNROW(6); LNROW(7);
  }
  auto hpub = [&]() { if (!DW) {
    float sm = 0.f, sq = 0.f;
    #pragma unroll
    for (int i = 0; i < 2; ++i)
      #pragma unroll
      for (int j = 0; j < 4; ++j) { sm = dot_one(hr[i][j], sm); sq = dot_sq(hr[i][j], sq); }
    sm = dadd<0xB1>(sm); sq = dadd<0xB1>(sq); sm = dadd<0x4E>(sm); sq = dadd<0x4E>(sq);
    sm = dadd<0x141>(sm); sq = dadd<0x141>(sq); sm = dadd<0x140>(sm); sq = dadd<0x140>(sq);
    { auto a = __builtin_amdgcn_permlane16_swap(__float_as_uint(sm), __float_as_uint(sm), false, false); sm = __uint_as_float(a[0]) + __uint_as_float(a[1]);
      auto c = __builtin_amdgcn_permlane16_swap(__float_as_uint(sq), __float_as_uint(sq), false, false); sq = __uint_as_float(c[0]) + __uint_as_float(c[1]); }
    const float m = sm * (1.f / 512.f);
    const float var = fmaxf(sq * (1.f / 512.f) - m * m, 0.f);
    const float rs = __builtin_amdgcn_rsqf(var + 1e-5f);
    const float rs2 = rs * __builtin_amdgcn_rsqf(var * rs * rs + 1e-5f), mb2 = -m * rs2;
    char* hp = ws + WS_H + (size_t)(r >> 4) * 524288 + (size_t)hrow * 256 + (r & 15) * 16;
    #pragma unroll
    for (int i = 0; i < 2; ++i) {
      u32x4 o;
      #pragma unroll
      for (int j = 0; j < 4; ++j) o[j] = pk2(bflo(hr[i][j]) * rs2 + mb2, bfhi(hr[i][j]) * rs2 + mb2);
      store16_sc1(hp + i * 1048576, o);
    }
  } };
  __builtin_amdgcn_s_setprio(0);
  unsigned cur = 0;
  unsigned fre = 2 * SLOT + wd * 1024;

#define PDM(FAST, i) do { if (FAST) { if (DW) dma16p<(i) * 4096>(pb_, pv8[i], fre); } else DMX(DA, i); SB(); } while (0)
#define OMM(P, a_, b_) do { if (MM) acc[a_][b_] = mfma16(fa[P][a_], fb[P][b_], acc[a_][b_]); SB(); } while (0)
#define PSTEP(P, N, s_, MM_, FAST, MID) do { constexpr bool MM = MM_; STEPBAR(); \
      OMM(P, 0, 0); fb[N][0] = PLB(0, s_); const char* pb_ = PBASE((s_) + 2); SB(); OMM(P, 0, 1); PDM(FAST, 0); \
      OMM(P, 0, 2); fa[N][0] = PLA(0); SB(); OMM(P, 0, 3); PDM(FAST, 1); \
      OMM(P, 1, 0); fb[N][1] = PLB(1, s_); SB(); OMM(P, 1, 1); PDM(FAST, 2); \
      OMM(P, 1, 2); fa[N][1] = PLA(1); SB(); OMM(P, 1, 3); PDM(FAST, 3); fb[N][2] = PLB(2, s_); SB(); \
      OMM(P, 2, 0); fa[N][2] = PLA(2); SB(); OMM(P, 2, 1); PDM(FAST, 4); fb[N][3] = PLB(3, s_); SB(); \
      OMM(P, 2, 2); fa[N][3] = PLA(3); SB(); OMM(P, 2, 3); PDM(FAST, 5); \
      OMM(P, 3, 0); PDM(FAST, 6); OMM(P, 3, 1); PDM(FAST, 7); MID; fre = cur + wd * 1024; cur = nextslot(cur); SB(); \
      OMM(P, 3, 2); OMM(P, 3, 3); } while (0)
#define PLOOP(MID0) do { PSTEP(1, 0, 0, false, true, MID0); \
    for (int s = 1; s < 11; s += 2) { PSTEP(0, 1, s, true, true, auxmid(s)); PSTEP(1, 0, s + 1, true, true, (void)0); } \
    PSTEP(0, 1, 11, true, true, POLL_ISSUE); PSTEP(1, 0, 12, true, true, (void)0); \
    PSTEP(0, 1, 13, true, true, { POLL_CHECK; nextd(0); }); PSTEP(1, 0, 14, true, false, nextd(1)); PSTEP(0, 1, 15, true, false, nextd(2)); } while (0)

#define GMM(P, k_, a_, b_) do { if (MM) acc[a_][b_] = mfma16(fa[P][k_][a_], fb[P][k_][b_], acc[a_][b_]); SB(); } while (0)
#define GM2(P, k_, a_) do { GMM(P, k_, a_, 0); GMM(P, k_, a_, 1); } while (0)
#define GSTEP(P, N, J, MM_, DD, MID) do { constexpr bool MM = MM_; \
    if (DW) { if ((DD) == 1) DMX_G(DA); if ((DD) == 2) DMX_ALL(DA); MID; } else { \
    asm volatile("" : "+v"(acc[0][0]), "+v"(acc[0][1]), "+v"(acc[1][0]), "+v"(acc[1][1]), "+v"(acc[2][0]), "+v"(acc[2][1]), "+v"(acc[3][0]), "+v"(acc[3][1])); \
    const unsigned ga_ = (J) < 3 ? gbase[(J) % 3] : 24576u, gb_ = (J) < 3 ? gbase[(J) % 3] + gsub8 : 24576u + gsub32; \
    GM2(P, 0, 0); GLB(N, 0, 0); GLA(N, 0, 0); SB(); GM2(P, 0, 1); GLB(N, 0, 1); GLA(N, 0, 1); SB(); \
    GM2(P, 0, 2); GLA(N, 0, 2); GLA(N, 0, 3); SB(); GM2(P, 0, 3); GLB(N, 1, 0); GLA(N, 1, 0); SB(); \
    GM2(P, 1, 0); GLB(N, 1, 1); GLA(N, 1, 1); SB(); GM2(P, 1, 1); GLA(N, 1, 2); GLA(N, 1, 3); SB(); \
    GM2(P, 1, 2); MID; SB(); GM2(P, 1, 3); } } while (0)
#define GLAST() do { if (!DW) { constexpr bool MM = true; GM2(1, 0, 0); GM2(1, 0, 1); GM2(1, 0, 2); GM2(1, 0, 3); GM2(1, 1, 0); GM2(1, 1, 1); GM2(1, 1, 2); GM2(1, 1, 3); } } while (0)
  const unsigned gtr = (4 * l4 + q) * 128 + (p >> 1) * 16 + (p & 1) * 8, gtf = 2 * (l4 & 1) + (q >> 1);
#define GBAR(N) do { if (DW) VMWAIT(N); LBAR(); } while (0)

  const int hh = w >> 1, rh = w & 1;
  bf16x8 qf[8];
  {
    f32x4 acc[4][4] = {};
    unsigned aq[4];
    #pragma unroll
    for (int j = 0; j < 4; ++j)
      aq[j] = (8 * l4 + q) * 1024 + (16 * hh + 4 * ((2 * rh + (j >> 1)) ^ q) + 2 * ((j & 1) ^ (l4 & 1)) + (p >> 1)) * 16 + (p & 1) * 8;
    const unsigned xrow = XO + l4 * 1024 + (((l & 15) ^ l4) << 4);
    bf16x8 fa[2][4], fb[2][4];
    u32x4 pf0, pf1;
#define PLB(i_, s_) lds_b128((xrow ^ (unsigned)((((s_) + rq) & 1) << 6)) + (((s_) + rq) & 15) * 4096 + 256 * (i_))
#define PLA(j_) lds_tr2(cur + aq[j_], cur + aq[j_] + 4096)
#define PBASE(s_) QBASE(s_)
#define pv8 qv8
    auto auxmid = [&](int s) {
      if (!DW) { if (s == 3) VMWAIT(0); if (s == 5 && w == 4 && l == 0) flag_store(ws + WS_HFLAG + (rb * 8 + g) * 8, tag); }
    };
#define POLL_ISSUE do { if (!DW) asm volatile("global_load_dwordx4 %0, %2, off sc1\n\tglobal_load_dwordx4 %1, %2, off offset:16 sc1" \
                          : "=&v"(pf0), "=&v"(pf1) : "v"(ws + WS_HFLAG + l * 32) : "memory"); } while (0)
#define POLL_CHECK do { if (!DW) { \
      asm volatile("s_waitcnt vmcnt(0)" : "+v"(pf0), "+v"(pf1) :: "memory"); \
      const bool ok = pf0[0] == tagl && pf0[1] == tagh && pf0[2] == tagl && pf0[3] == tagh && pf1[0] == tagl && pf1[1] == tagh && pf1[2] == tagl && pf1[3] == tagh; \
      if (do_g1 && w == 4) { if (!__all(ok)) poll_flags((const u64*)(ws + WS_HFLAG), 256, tag, l); \
        if (b == 16 && l == 0) flag_store(ws + WS_EPOCH, tag); } } } while (0)
    auto nextd = [&](int n) { if (DW) { if (do_g1) d_g1(DA, n, (1 + n) % 3); else d_kv(DA, n - 1, ((1 + n) % 3) * SLOT); } };
    PLOOP(hpub());
#undef pv8
#undef PLB
#undef PLA
#undef PBASE
#undef POLL_ISSUE
#undef POLL_CHECK
    { constexpr bool MM = true;
      const unsigned qa = XO + (hh * 16 + 8 * rh + 2 * (l4 & 1) + (l4 >> 1)) * 1024 + (l & 15) * 16;
#define QWR(pr, rbk) do { u32x4 v_ = pack44(acc[2 * (pr)][rbk] * QSCALE, acc[2 * (pr) + 1][rbk] * QSCALE); \
        auto s0_ = __builtin_amdgcn_permlane16_swap(v_[0], v_[2], false, false); auto s1_ = __builtin_amdgcn_permlane16_swap(v_[1], v_[3], false, false); \
        const u32x4 o_ = {s0_[0], s1_[0], s0_[1], s1_[1]}; *(LDS3 u32x4*)(qa + (pr) * 4096 + (rbk) * 256) = o_; SB(); } while (0)
      OMM(1, 0, 0); OMM(1, 1, 0); OMM(1, 2, 0); OMM(1, 3, 0); OMM(1, 0, 1); OMM(1, 1, 1); OMM(1, 2, 1); OMM(1, 3, 1);
      LBAR();
      OMM(1, 0, 2); OMM(1, 1, 2); QWR(0, 0); OMM(1, 2, 2); OMM(1, 3, 2); QWR(1, 0);
      OMM(1, 0, 3); OMM(1, 1, 3); QWR(0, 1); OMM(1, 2, 3); OMM(1, 3, 3); QWR(1, 1);
      QWR(0, 2); QWR(1, 2); QWR(0, 3); QWR(1, 3);
      STEPBAR();
      const unsigned qr = XO + (hh * 16 + h) * 1024 + (32 * rh + r) * 16;
      #pragma unroll
      for (int kk = 0; kk < 8; ++kk) qf[kk] = lds_b128(qr + kk * 2048);
    }
    asm volatile("" : "+v"(qf[0]), "+v"(qf[1]), "+v"(qf[2]), "+v"(qf[3]), "+v"(qf[4]), "+v"(qf[5]), "+v"(qf[6]), "+v"(qf[7]));
  }
  unsigned fill = 0;

  if (do_g1) {
    f32x4 acc[4][2] = {};
    unsigned aa[4], ba[2];
    #pragma unroll
    for (int a = 0; a < 4; ++a) aa[a] = gtr + 32 * (a ^ gtf);
    #pragma unroll
    for (int a = 0; a < 2; ++a) ba[a] = gtr + 32 * ((2 * (wd & 1) + a) ^ gtf);
    const unsigned gbase[3] = {32768u, 65536u, 0u};
    const unsigned gsub8 = 8192u * (1 + (wd >> 1)), gsub32 = 32768u * (1 + (wd >> 1));
    bf16x8 fa[2][2][4], fb[2][2][2];
#define GLA(N, kk, j_) fa[N][kk][j_] = lds_tr2(ga_ + aa[j_] + (kk) * 4096, ga_ + aa[j_] + (kk) * 4096 + 2048)
#define GLB(N, kk, j_) fb[N][kk][j_] = lds_tr2(gb_ + ba[j_] + (kk) * 4096, gb_ + ba[j_] + (kk) * 4096 + 2048)
#define G1D(t_, j_) do { if (DW) d_g1(DA, t_, j_); } while (0)
    if (DW) { DMX_G(DA); d_g1(DA, 3, 3); }
    GSTEP(1, 0, 0, false, 1, G1D(4, 1));
    GBAR(12); GSTEP(0, 1, 1, true, 1, G1D(5, 2));
    GBAR(12); GSTEP(1, 0, 2, true, 1, G1D(6, 0));
    GBAR(12); GSTEP(0, 1, 3, true, 1, G1D(7, 3));
    for (int t = 4; t < 28; t += 4) {
      GBAR(12); GSTEP(1, 0, 0, true, 1, G1D(t + 4, 1));
      GBAR(12); GSTEP(0, 1, 1, true, 1, G1D(t + 5, 2));
      GBAR(12); GSTEP(1, 0, 2, true, 1, G1D(t + 6, 0));
      GBAR(12); GSTEP(0, 1, 3, true, 1, G1D(t + 7, 3));
    }
    GBAR(12); GSTEP(1, 0, 0, true, 1, if (DW) d_kv(DA, -1, XO));
    GBAR(12); GSTEP(0, 1, 1, true, 2, if (DW) d_kv(DA, 0, XO + SLOT));
    GBAR(14); GSTEP(1, 0, 2, true, 2, if (DW) d_kv(DA, 1, 0));
    constexpr int f2 = DW ? 1 : 0;
    u32x2 bb[2];
    asm volatile("global_load_dwordx2 %0, %2, off\n\tglobal_load_dwordx2 %1, %2, off offset:32"
                 : "=&v"(bb[0]), "=&v"(bb[1]) : "v"(A.b1 + 64 * ft1 + 32 * f2 + 4 * l4) : "memory");
    GBAR(18); GSTEP(0, 1, 3, true, 0, (void)0);
    if (!DW) {
      constexpr bool MM = true;
      GM2(1, 0, 2); GM2(1, 0, 3); GM2(1, 1, 2); GM2(1, 1, 3); GM2(1, 0, 0); GM2(1, 0, 1);
      #pragma unroll
      for (int m = 0; m < 4; ++m) *(LDS3 f32x4*)(SLOT + wd * 4096 + m * 1024 + l * 16) = acc[2 + (m >> 1)][m & 1];
      SB(); GM2(1, 1, 0); GM2(1, 1, 1);
    }
    asm volatile("s_waitcnt vmcnt(0)" : "+v"(bb[0]), "+v"(bb[1]) :: "memory");
    LBAR();
#undef GLA
#undef GLB
    if (DW) {
      #pragma unroll
      for (int m = 0; m < 4; ++m) acc[m >> 1][m & 1] = *(LDS3 f32x4*)(SLOT + wd * 4096 + m * 1024 + l * 16);
    }
    #pragma unroll
    for (int m = 0; m < 4; ++m) {
      f32x4& v = acc[m >> 1][m & 1];
      v[0] += bflo(bb[m >> 1][0]); v[1] += bfhi(bb[m >> 1][0]); v[2] += bflo(bb[m >> 1][1]); v[3] += bfhi(bb[m >> 1][1]);
      #pragma unroll
      for (int e = 0; e < 4; ++e) {
        const float u = v[e];
        const float z = u * (1.f + 0.044715f * u * u) * (-2.f * 0.7978845608028654f * 1.4426950408889634f);
        v[e] = u * __builtin_amdgcn_rcpf(1.f + __builtin_amdgcn_exp2f(z));
      }
    }
    char* up = ws + WS_U + (size_t)(2 * dp1 + (wd >> 1)) * (224 * 1024) + (size_t)(ft1 * 8 + f2 * 4 + l4) * 1024 + (32 * (wd & 1) + (l & 15)) * 16;
    store16_sc1(up, pack44(acc[0][0], acc[1][0]));
    store16_sc1(up + 256, pack44(acc[0][1], acc[1][1]));
    cur = XO;
  }

  {
    f32x16 o[4];
    f32x16 negm, sA, sB;
    float lsum = 0.f;
    unsigned ka[8];
    #pragma unroll
    for (int kk = 0; kk < 8; ++kk) ka[kk] = (r * 256 + ((h ^ (r & 15)) << 4)) ^ (unsigned)(kk << 5);
    unsigned va[4];
    #pragma unroll
    for (int d = 0; d < 4; ++d) va[d] = 16384 + (4 * h + q) * 256 + (4 * (d ^ q) + 2 * gq + (p >> 1)) * 16 + (p & 1) * 8;
    bf16x8 kf[8], vf[8];
    u32x4 pw0 = {}, pw1 = {};
    float mx;
    unsigned kc_[8], vc_[4];
#define SETADDR() do { _Pragma("unroll") for (int i_ = 0; i_ < 8; ++i_) { kc_[i_] = cur + ka[i_]; } _Pragma("unroll") for (int i_ = 0; i_ < 4; ++i_) { vc_[i_] = cur + va[i_]; } } while (0)
#define LDK(i, KB) kf[i] = lds_b128(kc_[i] + (KB) * 8192)
#define LDV(i, VP) vf[i] = lds_tr2(vc_[(i) & 3] + ((VP) * 32 + 16 * ((i) >> 2)) * 256, vc_[(i) & 3] + ((VP) * 32 + 16 * ((i) >> 2) + 8) * 256)
#define EXP(Y, e) do { Y[e] = __builtin_amdgcn_exp2f(Y[e]); } while (0)
#define ADD(Y, e) do { lsum += Y[e]; } while (0)
#define CVT(P, Y, c) do { P[(c) & 3] = pk2(Y[2 * (c)], Y[2 * (c) + 1]); } while (0)
#define MX(X, e) do { mx = fmaxf(fmaxf(mx, X[2 * (e)]), X[2 * (e) + 1]); } while (0)
#define QK(X, i) do { X = mfma(kf[i], qf[i], (i) == 0 ? negm : X); } while (0)
#define PV(i) do { o[(i) & 3] = mfma(vf[i], __builtin_bit_cast(bf16x8, (i) < 4 ? pw0 : pw1), o[(i) & 3]); } while (0)
#define DMAC(C, i) do { if (C) DMA(i); } while (0)
#define STAGE_A(X, Y, KB, VP, DC) do { \
      QK(X, 0); LDK(6, KB); EXP(Y, 0); EXP(Y, 1); DMAC(DC, 3); SB(); \
      QK(X, 1); LDK(7, KB); EXP(Y, 2); CVT(pw0, Y, 0); DMAC(DC, 4); SB(); \
      QK(X, 2); LDV(0, VP); EXP(Y, 3); EXP(Y, 4); DMAC(DC, 5); SB(); \
      QK(X, 3); LDV(1, VP); EXP(Y, 5); ADD(Y, 0); CVT(pw0, Y, 1); DMAC(DC, 6); SB(); \
      QK(X, 4); LDV(2, VP); EXP(Y, 6); EXP(Y, 7); CVT(pw0, Y, 2); DMAC(DC, 7); SB(); \
      QK(X, 5); LDV(3, VP); EXP(Y, 8); ADD(Y, 1); ADD(Y, 2); CVT(pw0, Y, 3); SB(); \
      QK(X, 6); LDV(4, VP); EXP(Y, 9); EXP(Y, 10); ADD(Y, 3); SB(); \
      QK(X, 7); LDV(5, VP); EXP(Y, 11); ADD(Y, 4); CVT(pw1, Y, 4); SB(); \
    } while (0)
#define RESCALE(X) do { \
      if (__any(mx > THR)) { \
        const float delta = fmaxf(mx, 0.f); \
        const float alpha = __builtin_amdgcn_exp2f(-delta); \
        _Pragma("unroll") for (int e = 0; e < 16; ++e) { X[e] -= delta; negm[e] -= delta; } \
        _Pragma("unroll") for (int d = 0; d < 4; ++d) _Pragma("unroll") for (int e = 0; e < 16; ++e) o[d][e] *= alpha; \
        lsum *= alpha; \
      } } while (0)
#define STAGE_B(X, Y, VP, KBN) do { \
      PV(0); LDV(6, VP); EXP(Y, 12); EXP(Y, 13); CVT(pw1, Y, 5); SB(); \
      PV(1); LDV(7, VP); EXP(Y, 14); EXP(Y, 15); ADD(Y, 5); SB(); \
      PV(2); LDK(0, KBN); CVT(pw1, Y, 6); CVT(pw1, Y, 7); ADD(Y, 6); mx = -3.0e38f; MX(X, 0); SB(); \
      PV(3); LDK(1, KBN); MX(X, 1); MX(X, 2); ADD(Y, 7); ADD(Y, 8); SB(); \
      PV(4); LDK(2, KBN); MX(X, 3); MX(X, 4); ADD(Y, 9); ADD(Y, 10); SB(); \
      PV(5); LDK(3, KBN); MX(X, 5); MX(X, 6); ADD(Y, 11); ADD(Y, 12); SB(); \
      PV(6); LDK(4, KBN); MX(X, 7); ADD(Y, 13); ADD(Y, 14); SB(); \
      PV(7); LDK(5, KBN); mx = half_max(mx); ADD(Y, 15); SB(); \
      RESCALE(X); SB(); \
    } while (0)
#define STAGE_B_BAR(X, Y, VP, WAITS, SETD, POST) do { \
      PV(0); LDV(6, VP); EXP(Y, 12); EXP(Y, 13); CVT(pw1, Y, 5); SB(); \
      PV(1); LDV(7, VP); EXP(Y, 14); EXP(Y, 15); ADD(Y, 5); SB(); \
      PV(2); CVT(pw1, Y, 6); CVT(pw1, Y, 7); ADD(Y, 6); mx = -3.0e38f; MX(X, 0); SB(); \
      cur = next5(cur); \
      PV(3); SETADDR(); SETD; SB(); \
      PV(4); SB(); \
      WAITS; LBAR(); PRIO(3); \
      PV(5); SB(); LDK(0, 0); LDK(1, 0); LDK(2, 0); LDK(3, 0); MX(X, 1); MX(X, 2); MX(X, 3); MX(X, 4); ADD(Y, 7); ADD(Y, 8); ADD(Y, 9); POST; DMA(0); SB(); \
      PV(6); LDK(4, 0); MX(X, 5); MX(X, 6); MX(X, 7); ADD(Y, 10); ADD(Y, 11); ADD(Y, 12); DMA(1); SB(); \
      PV(7); LDK(5, 0); mx = half_max(mx); ADD(Y, 13); ADD(Y, 14); ADD(Y, 15); DMA(2); SB(); \
      RESCALE(X); SB(); \
    } while (0)

    SETADDR();
    #pragma unroll
    for (int i = 0; i < 8; ++i) LDK(i, 0);
    negm = f32x16{};
    #pragma unroll
    for (int d = 0; d < 4; ++d) o[d] = f32x16{};
    SB();
    QK(sA, 0); LDK(0, 1); DMA(0); SB(); QK(sA, 1); LDK(1, 1); DMA(1); SB(); QK(sA, 2); LDK(2, 1); DMA(2); SB(); QK(sA, 3); LDK(3, 1); SB();
    QK(sA, 4); LDK(4, 1); SB(); QK(sA, 5); LDK(5, 1); SB(); QK(sA, 6); SB(); QK(sA, 7); SB();
    mx = -3.0e38f;
    #pragma unroll
    for (int e = 0; e < 8; ++e) MX(sA, e);
    mx = half_max(mx);
    #pragma unroll
    for (int e = 0; e < 16; ++e) { sA[e] -= mx; negm[e] = -mx; }
    SB();
    STAGE_A(sB, sA, 1, 1, true);
    fill = SLOT;
    STAGE_B_BAR(sB, sA, 1, VMWAIT(0), if (DW) d_kv(DA, 2, fill),
                if (!DW && do_g1 && w == 4 && l == 0) flag_store(ws + WS_UFLAG + (dp1 * 28 + ft1) * 8, tag));
    for (int u = 0; u < 7; ++u) {
      STAGE_A(sA, sB, 0, 0, true);
      PRIO(2);
      STAGE_B(sA, sB, 0, 1);
      PRIO(1);
      STAGE_A(sB, sA, 1, 1, false);
      PRIO(0);
      fill = nextslot(fill);
      STAGE_B_BAR(sB, sA, 1, if (DW) VMWAIT(8), if (DW) { if (u + 3 < 8) d_kv(DA, u + 3, fill); else d_wo(DA, u - 5, fill); }, (void)0);
    }
    PRIO(0);
    #pragma unroll
    for (int i = 0; i < 8; ++i) LDV(i, 0);
    DMA(3); DMA(4);
    #pragma unroll
    for (int e = 0; e < 16; ++e) { EXP(sB, e); ADD(sB, e); }
    #pragma unroll
    for (int c = 0; c < 4; ++c) { CVT(pw0, sB, c); CVT(pw1, sB, 4 + c); }
    SB();
    fre = cur + wd * 1024; cur = nextslot(cur);
    const float inv = __builtin_amdgcn_rcpf(half_sum(lsum));
    const unsigned ob = XO + (hh * 16 + h) * 1024 + (32 * rh + r) * 16;
#define OWR(d) do { *(LDS3 bf16x8*)(ob + (4 * (d)) * 1024) = pack8<0>(o[d], inv); *(LDS3 bf16x8*)(ob + (4 * (d) + 2) * 1024) = pack8<1>(o[d], inv); } while (0)
    PV(0); PV(4); DMA(5); SB(); PV(1); PV(5); DMA(6); SB(); PV(2); OWR(0); SB(); PV(6); DMA(7); SB(); PV(3); OWR(1); SB(); PV(7); SB(); OWR(2); OWR(3);
  }

  {
    f32x4 acc[4][4] = {};
    unsigned oa[4];
    #pragma unroll
    for (int j = 0; j < 4; ++j)
      oa[j] = (16 * (l4 >> 1) + 4 * (l4 & 1) + q) * 1024 + (16 * (w >> 1) + 4 * ((2 * (w & 1) + (j >> 1)) ^ q) + 2 * ((j & 1) ^ (l4 & 1)) + (p >> 1)) * 16 + (p & 1) * 8;
    const unsigned ob = XO + l4 * 1024 + (l & 15) * 16;
    bf16x8 fa[2][4], fb[2][4];
    u32x2 pfl;
#define PLB(i_, s_) lds_b128(ob + (((s_) + ro) & 15) * 4096 + 256 * (i_))
#define PLA(j_) lds_tr2(cur + oa[j_], cur + oa[j_] + 8192)
#define PBASE(s_) ((const char*)A.wo + (size_t)(16 * g + (((s_) + ro) & 15)) * 32768)
    unsigned pv8[8];
    #pragma unroll
    for (int i = 0; i < 8; ++i) pv8[i] = (4 * i + wd) * 1024u + (wsw ^ ((i & 1) << 5));
    auto auxmid = [&](int) {};
#define POLL_ISSUE do { if (!DW) asm volatile("global_load_dwordx2 %0, %1, off sc1" : "=&v"(pfl) : "v"(ws + WS_UFLAG + ((dt2 >> 1) * 28 + (l < 28 ? l : 0)) * 8) : "memory"); } while (0)
#define POLL_CHECK do { if (!DW) { asm volatile("s_waitcnt vmcnt(0)" : "+v"(pfl) :: "memory"); \
      if (do_g2 && w == 4 && !__all(pfl[0] == tagl && pfl[1] == tagh)) poll_flags((const u64*)(ws + WS_UFLAG) + (dt2 >> 1) * 28, 28, tag, l); } } while (0)
    auto nextd = [&](int n) { if (DW) { if (do_g2) d_g2(DA, n, (2 + n) % 3); else d_wo(DA, n, ((2 + n) % 3) * SLOT); } };
    PLOOP((void)0);
#undef PLB
#undef PLA
#undef PBASE
#undef POLL_ISSUE
#undef POLL_CHECK
    char* pp = ws + WS_PART + ((size_t)(rb * 8 + w) * 8 + g) * 8192 + l * 16;
    { constexpr bool MM = true;
      OMM(1, 0, 0); OMM(1, 1, 0); OMM(1, 2, 0); OMM(1, 3, 0); OMM(1, 0, 1); OMM(1, 1, 1); OMM(1, 2, 1); OMM(1, 3, 1);
      store16_sc1(pp, pack44(acc[0][0], acc[1][0])); store16_sc1(pp + 1024, pack44(acc[2][0], acc[3][0])); SB();
      OMM(1, 0, 2); OMM(1, 1, 2); OMM(1, 2, 2); OMM(1, 3, 2);
      store16_sc1(pp + 2048, pack44(acc[0][1], acc[1][1])); store16_sc1(pp + 3072, pack44(acc[2][1], acc[3][1])); SB();
      OMM(1, 0, 3); OMM(1, 1, 3); OMM(1, 2, 3); OMM(1, 3, 3);
      store16_sc1(pp + 4096, pack44(acc[0][2], acc[1][2])); store16_sc1(pp + 5120, pack44(acc[2][2], acc[3][2]));
      store16_sc1(pp + 6144, pack44(acc[0][3], acc[1][3])); store16_sc1(pp + 7168, pack44(acc[2][3], acc[3][3]));
    }
    VMWAIT(0);
    if (l == 0) flag_store(ws + WS_SFLAG + ((rb * 8 + w) * 8 + g) * 8, tag);
    if (do_g2) LBAR();
  }

  if (do_g2) {
    const int wi = (w >> 1) & 1, wj = w & 1, sb2 = DW ? 1 : 0;
    f32x4 acc[4][2] = {};
    const unsigned aa = l4 * 1024 + (l & 15) * 16;
    unsigned ba[2];
    #pragma unroll
    for (int a = 0; a < 2; ++a) ba[a] = gtr + 32 * ((2 * (wd & 1) + a) ^ gtf);
    const unsigned gbase[3] = {65536u, 0u, 32768u};
    const unsigned gsub8 = 8192u * (1 + (wd >> 1)), gsub32 = 32768u * (1 + (wd >> 1));
    bf16x8 fa[2][2][4], fb[2][2][2];
    const int srow = 64 * (2 * pp2 + sb2) + 32 * wi + (l & 15);
    const size_t xo = (size_t)srow * 512 + 64 * dt2 + 32 * wj + 16 * (l4 & 1) + 8 * (l4 >> 1);
    const char* pp = ws + WS_PART + (size_t)((2 * pp2 + sb2) * 8 + dt2) * 65536 + (wi * 4 + wj) * 1024 + l * 16;
    const u64* sfl = (const u64*)(ws + WS_SFLAG) + ((2 * pp2 + sb2) * 8 + dt2) * 8;
    u32x4 pv[16];
    u32x2 pfl;
#define GLA(N, kk, j_) fa[N][kk][j_] = lds_b128(ga_ + aa + (kk) * 4096 + 256 * (j_))
#define GLB(N, kk, j_) fb[N][kk][j_] = lds_tr2(gb_ + ba[j_] + (kk) * 4096, gb_ + ba[j_] + (kk) * 4096 + 2048)
#define PLD(c) asm volatile("global_load_dwordx4 %0, %1, off sc1" : "=v"(pv[c]) : "v"(pp + ((c) >> 1) * 8192 + ((c) & 1) * 2048) : "memory")
#define G2D(t_, j_, AUX) do { if (DW) d_g2(DA, t_, j_); else { AUX; } } while (0)
    if (DW) { DMX_G(DA); d_g2(DA, 3, 3); }
    GSTEP(1, 0, 0, false, 1, G2D(4, 2, (void)0));
    GBAR(12); GSTEP(0, 1, 1, true, 1, G2D(5, 0, (void)0));
    GBAR(12); GSTEP(1, 0, 2, true, 1, G2D(6, 1, (void)0));
    GBAR(12); GSTEP(0, 1, 3, true, 1, G2D(7, 3, (void)0));
    for (int t = 4; t < 12; t += 4) {
      GBAR(12); GSTEP(1, 0, 0, true, 1, G2D(t + 4, 2, (void)0));
      GBAR(12); GSTEP(0, 1, 1, true, 1, G2D(t + 5, 0, (void)0));
      GBAR(12); GSTEP(1, 0, 2, true, 1, G2D(t + 6, 1, (void)0));
      GBAR(12); GSTEP(0, 1, 3, true, 1, G2D(t + 7, 3, (void)0));
    }
    GBAR(12); GSTEP(1, 0, 0, true, 1, G2D(16, 2, asm volatile("global_load_dwordx2 %0, %1, off sc1" : "=&v"(pfl) : "v"(sfl + (l & 7)) : "memory")));
    GBAR(12); GSTEP(0, 1, 1, true, 1, G2D(17, 0, (void)0));
    GBAR(12); GSTEP(1, 0, 2, true, 1, G2D(18, 1, (void)0));
    GBAR(12); GSTEP(0, 1, 3, true, 1, G2D(19, 3, { asm volatile("s_waitcnt vmcnt(0)" : "+v"(pfl) :: "memory");
                                                  if (!__all(pfl[0] == tagl && pfl[1] == tagh)) poll_flags(sfl, 8, tag, l); PLD(0); PLD(1); }));
    GBAR(12); GSTEP(1, 0, 0, true, 1, G2D(20, 2, { PLD(2); PLD(3); }));
    GBAR(12); GSTEP(0, 1, 1, true, 1, G2D(21, 0, { PLD(4); PLD(5); }));
    GBAR(12); GSTEP(1, 0, 2, true, 1, G2D(22, 1, { PLD(6); PLD(7); }));
    GBAR(12); GSTEP(0, 1, 3, true, 1, G2D(23, 3, { PLD(8); PLD(9); }));
    GBAR(12); GSTEP(1, 0, 0, true, 1, G2D(24, 2, { PLD(10); PLD(11); }));
    GBAR(12); GSTEP(0, 1, 1, true, 1, G2D(25, 0, { PLD(12); PLD(13); }));
    GBAR(12); GSTEP(1, 0, 2, true, 1, G2D(26, 1, { PLD(14); PLD(15); }));
    GBAR(12); GSTEP(0, 1, 3, true, 1, G2D(27, 3, (void)0));
    GBAR(12); GSTEP(1, 0, 0, true, 1, (void)0);
    u32x4 xv0, xv1; unsigned b2r, b2s;
    asm volatile("global_load_dwordx4 %0, %4, off\n\tglobal_load_dwordx4 %1, %5, off\n\tglobal_load_ushort %2, %6, off\n\tglobal_load_ushort %3, %6, off offset:32"
                 : "=&v"(xv0), "=&v"(xv1), "=&v"(b2r), "=&v"(b2s) : "v"(x + xo), "v"(x + xo + 16 * 512), "v"(A.b2 + srow) : "memory");
    GBAR(16); GSTEP(0, 1, 1, true, 0, (void)0);
    GBAR(10); GSTEP(1, 0, 2, true, 0, (void)0);
    GBAR(4); GSTEP(0, 1, 3, true, 0, (void)0);
    GLAST();
#undef GLA
#undef GLB
    if (!DW) {
      #pragma unroll
      for (int m = 0; m < 8; ++m) *(LDS3 f32x4*)(XO + wd * 8192 + m * 1024 + l * 16) = acc[m >> 1][m & 1];
    }
    LBAR();
    f32x16 yv;
    #pragma unroll
    for (int m = 0; m < 4; ++m) {
      const f32x4 v4 = *(LDS3 f32x4*)(XO + (sb2 * 2 + wi) * 8192 + ((2 * wj + (m & 1)) * 2 + (m >> 1)) * 1024 + l * 16);
      yv[4 * m] = v4[0]; yv[4 * m + 1] = v4[1]; yv[4 * m + 2] = v4[2]; yv[4 * m + 3] = v4[3];
    }
    asm volatile("s_waitcnt vmcnt(0)" : "+v"(xv0), "+v"(xv1), "+v"(b2r), "+v"(b2s) :: "memory");
    if (!DW) asm volatile("" : "+v"(pv[0]), "+v"(pv[1]), "+v"(pv[2]), "+v"(pv[3]), "+v"(pv[4]), "+v"(pv[5]), "+v"(pv[6]), "+v"(pv[7]),
                 "+v"(pv[8]), "+v"(pv[9]), "+v"(pv[10]), "+v"(pv[11]), "+v"(pv[12]), "+v"(pv[13]), "+v"(pv[14]), "+v"(pv[15]) :: "memory");
    #pragma unroll
    for (int e = 0; e < 16; ++e) {
      const float yy = yv[e] + bflo(e < 8 ? b2r : b2s);
      yv[e] = yy * __builtin_amdgcn_rcpf(1.f + __builtin_amdgcn_exp2f(-1.4426950408889634f * yy));
    }
    asm volatile("" : "+v"(yv));
    float t[16];
    #pragma unroll
    for (int e = 0; e < 16; ++e) t[e] = 0.f;
    auto accum = [&](const u32x4& a, const u32x4& c) {
      #pragma unroll
      for (int e = 0; e < 4; ++e) { t[2 * e] += bflo(a[e]); t[2 * e + 1] += bfhi(a[e]); t[8 + 2 * e] += bflo(c[e]); t[8 + 2 * e + 1] += bfhi(c[e]); }
    };
    if (DW) {
      unsigned done = 0;
      const char* pb = ws + WS_PART + (size_t)((2 * pp2 + 1) * 8 + dt2) * 65536 + (wi * 4 + wj) * 1024;
      for (unsigned spins = 0; done != 0xffu && spins < (1u << 20); ++spins) {
        const u64 f = __hip_atomic_load((GAS const u64*)(sfl + (l & 7)), __ATOMIC_RELAXED, __HIP_MEMORY_SCOPE_AGENT);
        unsigned nw = (unsigned)__ballot(f == tag) & 0xffu & ~done;
        done |= nw;
        if (!nw) __builtin_amdgcn_s_sleep(1);
        while (nw) {
          const int gg = __builtin_ctz(nw); nw &= nw - 1;
          dma16o<0>(pb + gg * 8192, l * 16u, wd * 16384 + gg * 2048);
          dma16o<1024>(pb + gg * 8192 + 2048, l * 16u, wd * 16384 + gg * 2048);
        }
      }
      VMWAIT(0);
      #pragma unroll
      for (int c = 0; c < 16; ++c) pv[c] = *(LDS3 u32x4*)(wd * 16384 + c * 1024 + l * 16);
    }
    #pragma unroll
    for (int gg = 0; gg < 8; ++gg) accum(pv[2 * gg], pv[2 * gg + 1]);
    #pragma unroll
    for (int c = 0; c < 2; ++c) {
      #pragma unroll
      for (int e = 0; e < 8; ++e) t[8 * c + e] *= yv[8 * c + e];
      #pragma unroll
      for (int e = 0; e < 4; ++e) {
        auto rr = __builtin_amdgcn_permlane16_swap(__float_as_uint(t[8 * c + e]), __float_as_uint(t[8 * c + 4 + e]), false, false);
        t[8 * c + e] = __uint_as_float(rr[0]); t[8 * c + 4 + e] = __uint_as_float(rr[1]);
      }
      const u32x4 xv = c ? xv1 : xv0;
      u32x4 ov;
      #pragma unroll
      for (int e = 0; e < 4; ++e) ov[e] = pk2(t[8 * c + 2 * e] + bflo(xv[e]), t[8 * c + 2 * e + 1] + bfhi(xv[e]));
      *(u32x4*)(A.out + xo + 16 * 512 * c) = ov;
    }
  }
}

__global__ __launch_bounds__(512, 2) void fused_kernel(Args A) {
  extern __shared__ __attribute__((aligned(16))) char lds_raw[];
  const int l = threadIdx.x & 63;
  const int w = __builtin_amdgcn_readfirstlane(threadIdx.x >> 6);
  if (w < 4) body<true>(A, w, l); else body<false>(A, w, l);
}

extern "C" void kernel_launch(void* const* d_in, const int* in_sizes, int n_in, void* d_out, int out_size, void* d_ws, size_t ws_size, hipStream_t stream) {
  (void)hipFuncSetAttribute((const void*)fused_kernel, hipFuncAttributeMaxDynamicSharedMemorySize, LDS_BYTES);
  Args A;
  A.x = (const __bf16*)d_in[0]; A.kc = (const __bf16*)d_in[1]; A.vc = (const __bf16*)d_in[2]; A.wq = (const __bf16*)d_in[3]; A.wo = (const __bf16*)d_in[4];
  A.w1 = (const __bf16*)d_in[6]; A.b1 = (const __bf16*)d_in[7]; A.w2 = (const __bf16*)d_in[8]; A.b2 = (const __bf16*)d_in[9];
  A.out = (__bf16*)d_out; A.ws = (char*)d_ws;
  fused_kernel<<<256, 512, LDS_BYTES, stream>>>(A);
}
```
